```python
import torch

def _softmax(x, axis=-1):
    return torch.softmax(x, dim=axis)

def _gelu(x):
    return torch.nn.functional.gelu(x, approximate='tanh')

def _silu(x):
    return torch.nn.functional.silu(x)

def _sigmoid(x):
    return torch.sigmoid(x)

def _layernorm(x, axis=-1, eps=1e-05):
    m = x.mean(dim=axis, keepdim=True)
    v = x.var(dim=axis, keepdim=True, unbiased=False)
    return (x - m) / torch.sqrt(v + eps)

def _scatter_add(tgt, idx, src, axis=0):
    return tgt.clone().index_add_(axis, idx, src)

def _segment_sum(src, seg, n):
    out = torch.zeros((n,) + src.shape[1:], dtype=src.dtype)
    return out.index_add_(0, seg, src)

def _pad(x, pw):
    flat = tuple((v for pair in reversed(pw) for v in pair))
    return torch.nn.functional.pad(x, flat)
kernel_name = 'adaLN_cond__gqa_block_cached__moe_arch_mod__prompt'

def setup_inputs():

    _w = lambda a: (lambda t: t.to(torch.bfloat16) if t.is_floating_point() else t)(torch.as_tensor(a))

    def _A_setup():
        B, Sq, D = (1, 2048, 576)
        Hq, Hkv, Sk, Dh = (32, 16, 2048, 128)
        assert Hq % Hkv == 0, (Hq, Hkv)
        return {'x': _w((lambda *s: torch.randn(*s, dtype=torch.bfloat16) * 0.05)(B, Sq, D)), 'k_cache': _w((lambda *s: torch.randn(*s, dtype=torch.bfloat16) * 3.2)(B, Hkv, Sk, Dh)), 'v_cache': _w((lambda *s: torch.randn(*s, dtype=torch.bfloat16) * 0.4)(B, Hkv, Sk, Dh)), 'wq': _w((lambda *s: torch.randn(*s, dtype=torch.bfloat16) * 3.2)(D, Hq * Dh) * D ** (-0.5)), 'wo': _w((lambda *s: torch.randn(*s, dtype=torch.bfloat16) * 0.4)(Hq * Dh, D) * (Hq * Dh) ** (-0.5))}
    _A_in = _A_setup()

    def _B_setup():
        B, S, D, F, C = (1, 2048, 576, 4608, 8)
        return {'x': _w((lambda *s: torch.randn(*s, dtype=torch.bfloat16) * 0.05)(B, S, D)), 'w_r': _w((lambda *s: torch.randn(*s, dtype=torch.bfloat16) * 0.05)(D)), 'w_gate': _w((lambda *s: torch.randn(*s, dtype=torch.bfloat16) * 0.05)(D, F)), 'w_up': _w((lambda *s: torch.randn(*s, dtype=torch.bfloat16) * 0.05)(D, F)), 'w_down': _w((lambda *s: torch.randn(*s, dtype=torch.bfloat16) * 0.05)(F, D))}
    _B_in = _B_setup()
    return {'A_x': _A_in['x'], 'A_k_cache': _A_in['k_cache'], 'A_v_cache': _A_in['v_cache'], 'A_wq': _A_in['wq'], 'A_wo': _A_in['wo'], 'B_x': _B_in['x'], 'B_w_r': _B_in['w_r'], 'B_w_gate': _B_in['w_gate'], 'B_w_up': _B_in['w_up'], 'B_w_down': _B_in['w_down'], 'c': _w((lambda *s: torch.randn(*s, dtype=torch.bfloat16) * 0.1)(1, 576) * 8.0), 'w_mod': _w((lambda *s: torch.randn(*s, dtype=torch.bfloat16) * 0.1)(576, 6 * 576) * 576 ** (-0.5) * 8.0)}

def reference(A_x, A_k_cache, A_v_cache, A_wq, A_wo, B_x, B_w_r, B_w_gate, B_w_up, B_w_down, c, w_mod):

    def _A_ref(x, k_cache, v_cache, wq, wo):
        B, Sq, D = x.shape
        Hq, Dh = (32, 128)
        Hkv = k_cache.shape[1]
        rep = Hq // Hkv
        q = (lambda x, *d: x.permute(*d))((x @ wq).reshape(B, Sq, Hq, Dh), 0, 2, 1, 3)
        k = (lambda x, n, axis: torch.repeat_interleave(x, n, dim=axis))(k_cache, rep, axis=1)
        v = (lambda x, n, axis: torch.repeat_interleave(x, n, dim=axis))(v_cache, rep, axis=1)
        scores = torch.einsum('bhqd,bhkd->bhqk', q, k) * Dh ** (-0.5)
        p = _softmax(scores, axis=-1)
        o = torch.einsum('bhqk,bhkd->bhqd', p, v)
        return (lambda x, *d: x.permute(*d))(o, 0, 2, 1, 3).reshape(B, Sq, Hq * Dh) @ wo

    def _B_ref(x, w_r, w_gate, w_up, w_down):
        B, S, D = x.shape
        C = 8
        r = x @ w_r
        idx = torch.argsort(-r, axis=-1)[:, :C]
        idx_e = (lambda x, n, axis: torch.repeat_interleave(x, n, dim=axis))(idx.reshape(B, C, 1), D, axis=2)
        x_sel = (lambda x, i, axis: torch.take_along_dim(x, i, dim=axis))(x, idx_e, axis=1)
        g = _silu(x_sel @ w_gate)
        u = x_sel @ w_up
        y = g * u @ w_down
        r_sel = _sigmoid((lambda x, i, axis: torch.take_along_dim(x, i, dim=axis))(r, idx, axis=1))
        y = y * r_sel.reshape(B, C, 1)
        b_off = (torch.arange(B) * S).reshape(B, 1)
        flat_idx = (idx + b_off).reshape(B * C)
        zero = (x * 0.0).reshape(B * S, D)
        upd = _scatter_add(zero, flat_idx, y.reshape(B * C, D), axis=0)
        return x + upd.reshape(B, S, D)
    x = A_x
    mod = (_silu(c) @ w_mod).reshape(c.shape[0], 6, 1, -1)
    sa, ca, ga, sb, cb, gb = (mod[:, 0].clone(), mod[:, 1].clone(), mod[:, 2].clone(), mod[:, 3].clone(), mod[:, 4].clone(), mod[:, 5].clone())
    h = _layernorm(x, axis=-1) * (1 + ca) + sa
    x = x + ga * _A_ref(h, A_k_cache, A_v_cache, A_wq, A_wo)
    h = _layernorm(x, axis=-1) * (1 + cb) + sb
    x = x + gb * _B_ref(h, B_w_r, B_w_gate, B_w_up, B_w_down)
    return x
```

```cpp
#include <hip/hip_runtime.h>
#include <hip/hip_bf16.h>
#include <cstdio>
#include <cstdint>

typedef unsigned short u16;
typedef __bf16 bf16x8 __attribute__((ext_vector_type(8)));
typedef __bf16 bf16x2 __attribute__((ext_vector_type(2)));
typedef short s16x8 __attribute__((ext_vector_type(8)));
typedef short s16x4 __attribute__((ext_vector_type(4)));
typedef float f32x16 __attribute__((ext_vector_type(16)));
typedef float f32x4 __attribute__((ext_vector_type(4)));
typedef float f32x2 __attribute__((ext_vector_type(2)));
typedef unsigned u32x4 __attribute__((ext_vector_type(4)));

constexpr int S = 2048, D = 576, HQ = 32, HKV = 16, DH = 128, F = 4608, NSEL = 8;

__device__ __forceinline__ float bf2f(u16 x) { return __uint_as_float(((unsigned)x) << 16); }
__device__ __forceinline__ unsigned cvtpk(float lo, float hi) {
  f32x2 v = {lo, hi}; bf16x2 b = __builtin_convertvector(v, bf16x2); return __builtin_bit_cast(unsigned, b);
}
__device__ __forceinline__ u16 f2bf(float x) { return (u16)(cvtpk(x, 0.f) & 0xffffu); }
__device__ __forceinline__ float rbf(float x) { return bf2f(f2bf(x)); }
__device__ __forceinline__ float wave_sum(float v) { for (int o = 32; o > 0; o >>= 1) v += __shfl_xor(v, o); return v; }

__global__ __launch_bounds__(256) void k_mod(const u16* __restrict__ c, const u16* __restrict__ wmod, float* __restrict__ mod) {
  __shared__ float sc[D]; __shared__ float red[4][64];
  const int tid = threadIdx.x, lane = tid & 63, w = tid >> 6;
  for (int i = tid; i < D; i += 256) { float x = bf2f(c[i]); sc[i] = rbf(x / (1.f + __expf(-x))); }
  __syncthreads();
  const int col = blockIdx.x * 64 + lane; float acc = 0.f;
  for (int k = w * 144; k < (w + 1) * 144; ++k) acc += sc[k] * bf2f(wmod[(size_t)k * (6 * D) + col]);
  red[w][lane] = acc; __syncthreads();
  if (w == 0) mod[col] = rbf(red[0][lane] + red[1][lane] + red[2][lane] + red[3][lane]);
}
__global__ __launch_bounds__(256) void k_ln1(const u16* __restrict__ x, const float* __restrict__ mod, u16* __restrict__ h) {
  const int lane = threadIdx.x & 63, row = blockIdx.x * 4 + (threadIdx.x >> 6);
  float v[9]; float s = 0.f;
  for (int i = 0; i < 9; ++i) { v[i] = bf2f(x[(size_t)row * D + i * 64 + lane]); s += v[i]; }
  const float m = wave_sum(s) * (1.f / D); float q = 0.f;
  for (int i = 0; i < 9; ++i) { v[i] -= m; q += v[i] * v[i]; }
  const float rs = rsqrtf(wave_sum(q) * (1.f / D) + 1e-5f);
  for (int i = 0; i < 9; ++i) { const int k = i * 64 + lane; h[(size_t)row * D + k] = f2bf(v[i] * rs * (1.f + mod[D + k]) + mod[k]); }
}
template <bool OUTF32>
__global__ __launch_bounds__(256) void k_gemm(const u16* __restrict__ A, const u16* __restrict__ B, void* __restrict__ Cv, int M, int N, int K) {
  __shared__ u16 As[64][40]; __shared__ u16 Bs[64][40];
  const int tid = threadIdx.x, lane = tid & 63, w = tid >> 6, wm = w >> 1, wn = w & 1;
  const int m0 = blockIdx.y * 64, n0 = blockIdx.x * 64;
  f32x16 acc = {};
  for (int k0 = 0; k0 < K; k0 += 32) {
    { const int r = tid >> 2, c8 = (tid & 3) * 8; *(u32x4*)&As[r][c8] = *(const u32x4*)&A[(size_t)(m0 + r) * K + k0 + c8]; }
    { const int kk = tid >> 3, n8 = (tid & 7) * 8; u32x4 t = *(const u32x4*)&B[(size_t)(k0 + kk) * N + n0 + n8]; const u16* p = (const u16*)&t;
      for (int j = 0; j < 8; ++j) Bs[n8 + j][kk] = p[j]; }
    __syncthreads();
    for (int ks = 0; ks < 2; ++ks) {
      bf16x8 a = *(const bf16x8*)&As[wm * 32 + (lane & 31)][ks * 16 + (lane >> 5) * 8];
      bf16x8 b = *(const bf16x8*)&Bs[wn * 32 + (lane & 31)][ks * 16 + (lane >> 5) * 8];
      acc = __builtin_amdgcn_mfma_f32_32x32x16_bf16(a, b, acc, 0, 0, 0);
    }
    __syncthreads();
  }
  for (int r = 0; r < 16; ++r) { const int row = m0 + wm * 32 + (r & 3) + 8 * (r >> 2) + 4 * (lane >> 5), col = n0 + wn * 32 + (lane & 31);
    if (OUTF32) ((float*)Cv)[(size_t)row * N + col] = acc[r]; else ((u16*)Cv)[(size_t)row * N + col] = f2bf(acc[r]); }
}
__global__ __launch_bounds__(256) void k_attn(const u16* __restrict__ Q, const u16* __restrict__ Kc, const u16* __restrict__ Vc, u16* __restrict__ O) {
  __shared__ u16 Ks[64][136]; __shared__ u16 Vt[128][72];
  const int tid = threadIdx.x, lane = tid & 63, w = tid >> 6, r32 = lane & 31, hi = lane >> 5;
  const int h = blockIdx.y, kvh = h >> 1, row = blockIdx.x * 128 + w * 32 + r32;
  bf16x8 qr[8];
#pragma unroll
  for (int d0 = 0; d0 < 8; ++d0) qr[d0] = *(const bf16x8*)&Q[(size_t)row * (HQ * DH) + h * DH + d0 * 16 + hi * 8];
  const float C = 0.088388347648318440f * 1.4426950408889634f;
  float m = -1e30f, l = 0.f; f32x16 o[4] = {};
  const u16* Kh = Kc + (size_t)kvh * S * DH; const u16* Vh = Vc + (size_t)kvh * S * DH;
  for (int t = 0; t < S / 64; ++t) {
    __syncthreads();
    for (int i = tid; i < 64 * 16; i += 256) { const int r = i >> 4, c8 = (i & 15) * 8;
      *(u32x4*)&Ks[r][c8] = *(const u32x4*)&Kh[(size_t)(t * 64 + r) * DH + c8];
      u32x4 tv = *(const u32x4*)&Vh[(size_t)(t * 64 + r) * DH + c8]; const u16* p = (const u16*)&tv;
      for (int j = 0; j < 8; ++j) Vt[c8 + j][r] = p[j]; }
    __syncthreads();
    f32x16 s0 = {}, s1 = {};
#pragma unroll
    for (int d0 = 0; d0 < 8; ++d0) {
      bf16x8 a0 = *(const bf16x8*)&Ks[r32][d0 * 16 + hi * 8], a1 = *(const bf16x8*)&Ks[32 + r32][d0 * 16 + hi * 8];
      s0 = __builtin_amdgcn_mfma_f32_32x32x16_bf16(a0, qr[d0], s0, 0, 0, 0);
      s1 = __builtin_amdgcn_mfma_f32_32x32x16_bf16(a1, qr[d0], s1, 0, 0, 0);
    }
    float pm = s0[0];
#pragma unroll
    for (int r = 0; r < 16; ++r) { pm = fmaxf(pm, s0[r]); pm = fmaxf(pm, s1[r]); }
    pm = fmaxf(pm, __shfl_xor(pm, 32));
    const float mn = fmaxf(m, pm), alpha = exp2f((m - mn) * C); m = mn;
    float rs = 0.f;
#pragma unroll
    for (int r = 0; r < 16; ++r) { s0[r] = exp2f((s0[r] - mn) * C); s1[r] = exp2f((s1[r] - mn) * C); rs += s0[r] + s1[r]; }
    rs += __shfl_xor(rs, 32); l = l * alpha + rs;
#pragma unroll
    for (int d = 0; d < 4; ++d)
#pragma unroll
      for (int r = 0; r < 16; ++r) o[d][r] *= alpha;
#pragma unroll
    for (int kb = 0; kb < 2; ++kb)
#pragma unroll
      for (int s = 0; s < 2; ++s) {
        u32x4 pw;
#pragma unroll
        for (int j = 0; j < 4; ++j) pw[j] = kb ? cvtpk(s1[8 * s + 2 * j], s1[8 * s + 2 * j + 1]) : cvtpk(s0[8 * s + 2 * j], s0[8 * s + 2 * j + 1]);
        const bf16x8 pb = __builtin_bit_cast(bf16x8, pw);
#pragma unroll
        for (int d = 0; d < 4; ++d) {
          const u16* vp = &Vt[d * 32 + r32][kb * 32 + 16 * s + 4 * hi];
          s16x4 lo = *(const s16x4*)vp, hi4 = *(const s16x4*)(vp + 8);
          s16x8 av = {lo[0], lo[1], lo[2], lo[3], hi4[0], hi4[1], hi4[2], hi4[3]};
          o[d] = __builtin_amdgcn_mfma_f32_32x32x16_bf16(__builtin_bit_cast(bf16x8, av), pb, o[d], 0, 0, 0);
        }
      }
  }
  const float il = 1.f / l;
#pragma unroll
  for (int d = 0; d < 4; ++d)
#pragma unroll
    for (int g = 0; g < 4; ++g) {
      uint2 pk; pk.x = cvtpk(o[d][4 * g] * il, o[d][4 * g + 1] * il); pk.y = cvtpk(o[d][4 * g + 2] * il, o[d][4 * g + 3] * il);
      *(uint2*)&O[(size_t)row * (HQ * DH) + h * DH + d * 32 + 8 * g + 4 * hi] = pk;
    }
}
__global__ __launch_bounds__(256) void k_ln2(const u16* __restrict__ x, const float* __restrict__ a, const float* __restrict__ mod, const u16* __restrict__ wr,
                                             u16* __restrict__ h2, float* __restrict__ x1o, float* __restrict__ r, u16* __restrict__ out) {
  const int lane = threadIdx.x & 63, row = blockIdx.x * 4 + (threadIdx.x >> 6);
  float v[9], x1[9]; float s = 0.f;
  for (int i = 0; i < 9; ++i) { const int k = i * 64 + lane; x1[i] = bf2f(x[(size_t)row * D + k]) + mod[2 * D + k] * a[(size_t)row * D + k]; v[i] = x1[i]; s += v[i]; }
  const float m = wave_sum(s) * (1.f / D); float q = 0.f;
  for (int i = 0; i < 9; ++i) { v[i] -= m; q += v[i] * v[i]; }
  const float rs = rsqrtf(wave_sum(q) * (1.f / D) + 1e-5f); float rr = 0.f;
  for (int i = 0; i < 9; ++i) { const int k = i * 64 + lane; const float hv = rbf(v[i] * rs * (1.f + mod[4 * D + k]) + mod[3 * D + k]);
    h2[(size_t)row * D + k] = f2bf(hv); x1o[(size_t)row * D + k] = x1[i]; rr += hv * bf2f(wr[k]); out[(size_t)row * D + k] = f2bf(x1[i] + mod[5 * D + k] * hv); }
  rr = wave_sum(rr); if (lane == 0) r[row] = rr;
}
__global__ __launch_bounds__(256) void k_topk(const float* __restrict__ r, int* __restrict__ idx) {
  __shared__ float sv[S]; __shared__ unsigned long long red[4];
  const int tid = threadIdx.x;
  for (int i = tid; i < S; i += 256) sv[i] = r[i];
  __syncthreads();
  for (int it = 0; it < NSEL; ++it) {
    unsigned long long best = 0;
    for (int i = tid; i < S; i += 256) { unsigned u = __float_as_uint(sv[i]); u = (u & 0x80000000u) ? ~u : (u | 0x80000000u);
      unsigned long long key = ((unsigned long long)u << 32) | (unsigned)(S - 1 - i); best = key > best ? key : best; }
    for (int o = 32; o > 0; o >>= 1) { unsigned long long t = __shfl_xor(best, o); best = t > best ? t : best; }
    if ((tid & 63) == 0) red[tid >> 6] = best;
    __syncthreads();
    if (tid == 0) { unsigned long long b = red[0]; for (int k = 1; k < 4; ++k) b = red[k] > b ? red[k] : b; const int id = S - 1 - (int)(b & 0xffffffffu); idx[it] = id; sv[id] = -INFINITY; }
    __syncthreads();
  }
}
__global__ __launch_bounds__(256) void k_gateup(const u16* __restrict__ h2, const int* __restrict__ idx, const u16* __restrict__ wg, const u16* __restrict__ wu, float* __restrict__ act) {
  __shared__ float xs[NSEL][D]; __shared__ float red[4][2][NSEL][64];
  const int tid = threadIdx.x, lane = tid & 63, w = tid >> 6;
  for (int i = tid; i < NSEL * D; i += 256) xs[i / D][i % D] = bf2f(h2[(size_t)idx[i / D] * D + i % D]);
  __syncthreads();
  const int col = blockIdx.x * 64 + lane; float g[NSEL] = {}, u[NSEL] = {};
  for (int k = w * 144; k < (w + 1) * 144; ++k) { const float a = bf2f(wg[(size_t)k * F + col]), b = bf2f(wu[(size_t)k * F + col]);
    for (int j = 0; j < NSEL; ++j) { g[j] += xs[j][k] * a; u[j] += xs[j][k] * b; } }
  for (int j = 0; j < NSEL; ++j) { red[w][0][j][lane] = g[j]; red[w][1][j][lane] = u[j]; }
  __syncthreads();
  for (int j = w; j < NSEL; j += 4) { float gg = 0, uu = 0; for (int k = 0; k < 4; ++k) { gg += red[k][0][j][lane]; uu += red[k][1][j][lane]; }
    gg = rbf(gg); uu = rbf(uu); const float sg = rbf(gg / (1.f + __expf(-gg))); act[(size_t)j * F + col] = rbf(sg * uu); }
}
__global__ __launch_bounds__(1024) void k_down(const float* __restrict__ act, const u16* __restrict__ wd, const int* __restrict__ idx, const float* __restrict__ r,
                                               const float* __restrict__ x1, const u16* __restrict__ h2, const float* __restrict__ mod, u16* __restrict__ out) {
  __shared__ float red[16][NSEL][64];
  const int tid = threadIdx.x, lane = tid & 63, w = tid >> 6, col = blockIdx.x * 64 + lane;
  float y[NSEL] = {};
  for (int k = w * 288; k < (w + 1) * 288; ++k) { const float a = bf2f(wd[(size_t)k * D + col]); for (int j = 0; j < NSEL; ++j) y[j] += act[(size_t)j * F + k] * a; }
  for (int j = 0; j < NSEL; ++j) red[w][j][lane] = y[j];
  __syncthreads();
  if (w < NSEL) { float s = 0; for (int k = 0; k < 16; ++k) s += red[k][w][lane];
    const int row = idx[w]; const float rv = rbf(r[row]); const float sig = 1.f / (1.f + __expf(-rv));
    const float hv = bf2f(h2[(size_t)row * D + col]);
    out[(size_t)row * D + col] = f2bf(x1[(size_t)row * D + col] + mod[5 * D + col] * (hv + s * sig)); }
}

extern "C" void kernel_launch(void* const* d_in, const int* in_sizes, int n_in, void* d_out, int out_size, void* d_ws, size_t ws_size, hipStream_t stream) {
  const u16* x = (const u16*)d_in[0]; const u16* kc = (const u16*)d_in[1]; const u16* vc = (const u16*)d_in[2]; const u16* wq = (const u16*)d_in[3]; const u16* wo = (const u16*)d_in[4];
  const u16* wr = (const u16*)d_in[6]; const u16* wg = (const u16*)d_in[7]; const u16* wu = (const u16*)d_in[8]; const u16* wd = (const u16*)d_in[9];
  const u16* c = (const u16*)d_in[10]; const u16* wmod = (const u16*)d_in[11];
  char* ws = (char*)d_ws; size_t off = 0;
  auto alloc = [&](size_t b) { char* p = ws + off; off += (b + 255) / 256 * 256; return p; };
  float* mod = (float*)alloc(6 * D * 4); u16* h = (u16*)alloc((size_t)S * D * 2); u16* q = (u16*)alloc((size_t)S * 4096 * 2); u16* o = (u16*)alloc((size_t)S * 4096 * 2);
  float* a = (float*)alloc((size_t)S * D * 4); u16* h2 = (u16*)alloc((size_t)S * D * 2); float* x1 = (float*)alloc((size_t)S * D * 4); float* r = (float*)alloc(S * 4);
  int* idx = (int*)alloc(64); float* act = (float*)alloc(NSEL * F * 4);
  u16* out = (u16*)d_out;
  k_mod<<<54, 256, 0, stream>>>(c, wmod, mod);
  k_ln1<<<S / 4, 256, 0, stream>>>(x, mod, h);
  k_gemm<false><<<dim3(4096 / 64, S / 64), 256, 0, stream>>>(h, wq, q, S, 4096, D);
  k_attn<<<dim3(S / 128, HQ), 256, 0, stream>>>(q, kc, vc, o);
  k_gemm<true><<<dim3(D / 64, S / 64), 256, 0, stream>>>(o, wo, a, S, D, 4096);
  k_ln2<<<S / 4, 256, 0, stream>>>(x, a, mod, wr, h2, x1, r, out);
  k_topk<<<1, 256, 0, stream>>>(r, idx);
  k_gateup<<<F / 64, 256, 0, stream>>>(h2, idx, wg, wu, act);
  k_down<<<D / 64, 1024, 0, stream>>>(act, wd, idx, r, x1, h2, mod, out);
}
```

```cpp
#include <hip/hip_runtime.h>
#include <hip/hip_bf16.h>
#include <cstdint>

typedef unsigned short u16;
typedef __bf16 bf16x8 __attribute__((ext_vector_type(8)));
typedef __bf16 bf16x2 __attribute__((ext_vector_type(2)));
typedef float f32x4 __attribute__((ext_vector_type(4)));
typedef float f32x2 __attribute__((ext_vector_type(2)));
typedef unsigned u32x4 __attribute__((ext_vector_type(4)));
typedef unsigned u32x2 __attribute__((ext_vector_type(2)));

constexpr int S = 2048, D = 576, F = 4608, NSEL = 8;

__device__ __forceinline__ float bflo(unsigned w) { return __uint_as_float(w << 16); }
__device__ __forceinline__ float bfhi(unsigned w) { return __uint_as_float(w & 0xffff0000u); }
__device__ __forceinline__ float bf2f(u16 x) { return __uint_as_float(((unsigned)x) << 16); }
__device__ __forceinline__ unsigned cvtpk(float lo, float hi) {
  f32x2 v = {lo, hi}; bf16x2 b = __builtin_convertvector(v, bf16x2); return __builtin_bit_cast(unsigned, b);
}
__device__ __forceinline__ float rbf(float x) { return bflo(cvtpk(x, 0.f)); }


constexpr size_t WS_PAR = 0;
constexpr size_t WS_MOD = 8192;
constexpr size_t WS_R = WS_MOD + 4 * 576 * 4 + 1024;
constexpr size_t WS_RA = (WS_R + 255) / 256 * 256;
constexpr size_t WS_NFR = WS_RA + 8192;
constexpr size_t WS_X1 = WS_NFR + 128 * 18 * 1024;
constexpr size_t WS_H2 = WS_X1 + (size_t)S * D * 2;
constexpr size_t WS_YP = WS_H2 + (size_t)S * D * 2;
constexpr size_t WS_SEL = WS_YP + (size_t)144 * 8 * 576 * 4;
constexpr size_t WS_SYNC = WS_SEL + 256;
constexpr size_t SY_GRAN = 0, SY_CNT1 = 8192, SY_CNT2 = SY_CNT1 + 16 * 256, SY_TMO = SY_CNT2 + 8 * 256, SY_BYTES = SY_TMO + 256;
constexpr size_t WS_PART = (WS_SYNC + SY_BYTES + 4095) / 4096 * 4096;
constexpr size_t WS_END = WS_PART + (size_t)16 * 16 * 4 * 36864;

template <int CTRL> __device__ __forceinline__ float dppf(float v) { return __int_as_float(__builtin_amdgcn_update_dpp(__float_as_int(v), __float_as_int(v), CTRL, 0xf, 0xf, false)); }
__device__ __forceinline__ float xrow_sum(float v) {
  { auto r = __builtin_amdgcn_permlane16_swap(__float_as_uint(v), __float_as_uint(v), false, false); v = __uint_as_float(r[0]) + __uint_as_float(r[1]); }
  { auto r = __builtin_amdgcn_permlane32_swap(__float_as_uint(v), __float_as_uint(v), false, false); v = __uint_as_float(r[0]) + __uint_as_float(r[1]); }
  return v;
}
__global__ __launch_bounds__(256) void k_prep(const u16* __restrict__ c, const u16* __restrict__ wmod, const u16* __restrict__ x, char* __restrict__ ws) {
  __shared__ float red[4][32]; __shared__ float st[2][4][16];
  const int tid = threadIdx.x, lane = tid & 63, w = tid >> 6, b = blockIdx.x;
  unsigned cpf0, cpf1;
  { unsigned long long pc; int off; unsigned len, len0;
    asm volatile("s_getpc_b64 %0\nKP_PC_%=:\n\ts_mov_b32 %1, K_MAIN_CODE_BEGIN-KP_PC_%=\n\ts_mov_b32 %2, K_MAIN_CODE_END-K_MAIN_CODE_BEGIN\n\ts_mov_b32 %3, K_PREP_CODE_END-KP_PC_%="
                 : "=&s"(pc), "=&s"(off), "=&s"(len), "=&s"(len0));
    const __amdgpu_buffer_rsrc_t r0 = __builtin_amdgcn_make_buffer_rsrc((void*)pc, 0, (int)len0, 0x00020000);
    asm volatile("buffer_load_dword %0, %1, %2, 0 offen" : "=&v"(cpf0) : "v"((unsigned)(tid * 128)), "s"(r0) : "memory");
    const __amdgpu_buffer_rsrc_t rc = __builtin_amdgcn_make_buffer_rsrc((void*)(pc + (long long)off), 0, (int)len, 0x00020000);
    asm volatile("buffer_load_dword %0, %1, %2, 0 offen" : "=&v"(cpf1) : "v"((unsigned)((b * 4 + w) * 512 + (lane & 3) * 128)), "s"(rc) : "memory");
  }
  if (b < 108) {
    const int col0 = b * 32 + (lane & 3) * 8;
    u32x4 wv[9]; u16 cv[9];
#pragma unroll
    for (int i = 0; i < 9; ++i) { const int r = w * 144 + i * 16 + (lane >> 2); wv[i] = *(const u32x4*)(wmod + (size_t)r * (6 * D) + col0); cv[i] = c[r]; }
    float acc[8] = {};
#pragma unroll
    for (int i = 0; i < 9; ++i) { const float cf = bf2f(cv[i]), s = rbf(cf * __builtin_amdgcn_rcpf(1.f + __expf(-cf)));
#pragma unroll
      for (int e = 0; e < 4; ++e) { acc[2 * e] = fmaf(s, bflo(wv[i][e]), acc[2 * e]); acc[2 * e + 1] = fmaf(s, bfhi(wv[i][e]), acc[2 * e + 1]); } }
#pragma unroll
    for (int e = 0; e < 8; ++e) { float v = acc[e]; v += dppf<0x124>(v); v += dppf<0x128>(v); acc[e] = xrow_sum(v); }
    if (lane < 4) { *(f32x4*)&red[w][lane * 8] = f32x4{acc[0], acc[1], acc[2], acc[3]}; *(f32x4*)&red[w][lane * 8 + 4] = f32x4{acc[4], acc[5], acc[6], acc[7]}; }
    __syncthreads();
    if (tid < 32) {
      const float m = rbf(red[0][tid] + red[1][tid] + red[2][tid] + red[3][tid]);
      const int col = b * 32 + tid, which = col / D, k = col - which * D;
      float* par = (float*)(ws + WS_PAR); float* mod = (float*)(ws + WS_MOD);
      if (which < 2) {
        const int s = k >> 5, kk = k & 31, G = (kk & 15) >> 2, j = (kk & 3) + ((kk >> 4) << 2);
        const int idx = s * 32 + G * 8 + j;
        if (which == 0) par[576 + idx] = m; else par[idx] = rbf(1.f + m);
      } else if (which == 2) mod[k] = m;
      else if (which == 3) mod[2 * D + k] = m;
      else if (which == 4) mod[D + k] = rbf(1.f + m);
      else mod[3 * D + k] = m;
    }
  } else if (b < 236) {
    const int rbk = b - 108, q = lane & 15, G = lane >> 4;
    const u16* xr = x + (size_t)(rbk * 16 + q) * D + 4 * G;
    const bool five = w < 2;
    u32x2 lo[5], hi[5];
#pragma unroll
    for (int i = 0; i < 5; ++i) { const int s = min(w + 4 * i, 17); lo[i] = *(const u32x2*)(xr + 32 * s); hi[i] = *(const u32x2*)(xr + 32 * s + 16); }
    float v[5][8]; float sum = 0.f;
#pragma unroll
    for (int i = 0; i < 5; ++i) { v[i][0] = bflo(lo[i][0]); v[i][1] = bfhi(lo[i][0]); v[i][2] = bflo(lo[i][1]); v[i][3] = bfhi(lo[i][1]);
      v[i][4] = bflo(hi[i][0]); v[i][5] = bfhi(hi[i][0]); v[i][6] = bflo(hi[i][1]); v[i][7] = bfhi(hi[i][1]);
      float t = 0.f;
#pragma unroll
      for (int e = 0; e < 8; ++e) t += v[i][e];
      sum += (i < 4 || five) ? t : 0.f; }
    sum = xrow_sum(sum);
    const float cnt = five ? 160.f : 128.f, mw = sum / cnt;
    float sq = 0.f;
#pragma unroll
    for (int i = 0; i < 5; ++i) { float t = 0.f;
#pragma unroll
      for (int e = 0; e < 8; ++e) { const float d = v[i][e] - mw; t = fmaf(d, d, t); }
      sq += (i < 4 || five) ? t : 0.f; }
    sq = xrow_sum(sq);
    if (lane < 16) { st[0][w][lane] = sum; st[1][w][lane] = sq; }
    __syncthreads();
    const float s0 = st[0][0][q], s1 = st[0][1][q], s2 = st[0][2][q], s3 = st[0][3][q];
    const float mean = (s0 + s1 + s2 + s3) * (1.f / D);
    const float d0 = s0 * (1.f / 160.f) - mean, d1 = s1 * (1.f / 160.f) - mean, d2 = s2 * (1.f / 128.f) - mean, d3 = s3 * (1.f / 128.f) - mean;
    const float var = (st[1][0][q] + st[1][1][q] + st[1][2][q] + st[1][3][q] + 160.f * (d0 * d0 + d1 * d1) + 128.f * (d2 * d2 + d3 * d3)) * (1.f / D);
    const float sd = rbf(__builtin_sqrtf(rbf(rbf(var) + 1e-5f))), mb = rbf(mean), rsd = 1.f / sd;
    u32x4* nf = (u32x4*)(ws + WS_NFR) + (size_t)rbk * 18 * 64 + lane;
#pragma unroll
    for (int i = 0; i < 5; ++i) { const int s = w + 4 * i; if (s < 18) {
      unsigned o[4];
#pragma unroll
      for (int e = 0; e < 4; ++e) { const unsigned d = cvtpk(v[i][2 * e] - mb, v[i][2 * e + 1] - mb); o[e] = cvtpk(bflo(d) * rsd, bfhi(d) * rsd); }
      nf[s * 64] = u32x4{o[0], o[1], o[2], o[3]}; } }
  } else { const int i = (b - 236) * 256 + tid; if (i < (int)(SY_BYTES / 16)) ((u32x4*)(ws + WS_SYNC))[i] = u32x4{0u, 0u, 0u, 0u}; }
  asm volatile("s_waitcnt vmcnt(0)" :: "v"(cpf0), "v"(cpf1) : "memory");
  asm volatile("K_PREP_CODE_END:");
}

#define RAW_BARRIER() asm volatile("s_waitcnt lgkmcnt(0)\n\ts_barrier" ::: "memory")
__device__ __forceinline__ unsigned wave_max_s(unsigned v) {
  unsigned t = v, r;
  asm volatile("s_nop 1\n\t"
               "v_max_u32_dpp %0, %0, %0 row_shr:1 row_mask:0xf bank_mask:0xf\n\ts_nop 1\n\t"
               "v_max_u32_dpp %0, %0, %0 row_shr:2 row_mask:0xf bank_mask:0xf\n\ts_nop 1\n\t"
               "v_max_u32_dpp %0, %0, %0 row_shr:4 row_mask:0xf bank_mask:0xf\n\ts_nop 1\n\t"
               "v_max_u32_dpp %0, %0, %0 row_shr:8 row_mask:0xf bank_mask:0xf\n\ts_nop 1\n\t"
               "v_max_u32_dpp %0, %0, %0 row_bcast:15 row_mask:0xa bank_mask:0xf\n\ts_nop 1\n\t"
               "v_max_u32_dpp %0, %0, %0 row_bcast:31 row_mask:0xc bank_mask:0xf\n\ts_nop 0\n\t"
               "v_readlane_b32 %1, %0, 63\n\ts_nop 1" : "+v"(t), "=s"(r));
  return r;
}
__device__ __forceinline__ void dma16(__amdgpu_buffer_rsrc_t rs, unsigned voff, unsigned soff, unsigned m0) {
  asm volatile("s_mov_b32 m0, %0\n\ts_nop 0\n\tbuffer_load_dwordx4 %1, %2, %3 offen lds" :: "s"(m0), "v"(voff), "s"(rs), "s"(soff) : "memory");
}
typedef __attribute__((address_space(1))) unsigned gu32;
typedef __attribute__((address_space(1))) unsigned long long gu64;
#define RLX_AGENT __ATOMIC_RELAXED, __HIP_MEMORY_SCOPE_AGENT
constexpr unsigned GTAG = 0x51C0FFEEu;
constexpr unsigned SPIN_CAP = 1u << 15;
template <int CTRL> __device__ __forceinline__ float dpp_f(float v) { return __int_as_float(__builtin_amdgcn_update_dpp(__float_as_int(v), __float_as_int(v), CTRL, 0xf, 0xf, false)); }
__device__ __forceinline__ float sum_hi8(float v) {
  v += dpp_f<0x128>(v);
  { auto r = __builtin_amdgcn_permlane16_swap(__float_as_uint(v), __float_as_uint(v), false, false); v = __uint_as_float(r[0]) + __uint_as_float(r[1]); }
  { auto r = __builtin_amdgcn_permlane32_swap(__float_as_uint(v), __float_as_uint(v), false, false); v = __uint_as_float(r[0]) + __uint_as_float(r[1]); }
  return v;
}
__device__ __forceinline__ u32x4 ld16_sc1(__amdgpu_buffer_rsrc_t rs, unsigned voff, unsigned soff) { return __builtin_amdgcn_raw_buffer_load_b128(rs, voff, soff, 16); }
__device__ __forceinline__ u32x2 ld8_sc1(__amdgpu_buffer_rsrc_t rs, unsigned voff, unsigned soff) { return __builtin_amdgcn_raw_buffer_load_b64(rs, voff, soff, 16); }
__device__ __forceinline__ void st8_sc1(u32x2 v, __amdgpu_buffer_rsrc_t rs, unsigned voff, unsigned soff) { __builtin_amdgcn_raw_buffer_store_b64(v, rs, voff, soff, 16); }
__device__ __forceinline__ void st16_sc1(u32x4 v, __amdgpu_buffer_rsrc_t rs, unsigned voff, unsigned soff) { __builtin_amdgcn_raw_buffer_store_b128(v, rs, voff + soff, 0, 16); }
#define DRAIN() asm volatile("s_waitcnt vmcnt(0)" ::: "memory")

constexpr int TL_GU = 0, TL_DN = 73728, TL_RED = TL_DN + 36864, TL_ST = TL_RED + 16384, TL_CAND = TL_ST + 4096, TAIL_LDS = TL_CAND + 256;

__device__ __forceinline__ void cswap(unsigned& a, unsigned& b) { const unsigned hi = a > b ? a : b, lo = a > b ? b : a; a = hi; b = lo; }
__device__ __forceinline__ bf16x8 frag_of(const u32x2& a, const u32x2& b) { const u32x4 r = {a[0], a[1], b[0], b[1]}; return __builtin_bit_cast(bf16x8, r); }
__device__ __forceinline__ u32x4 widen8(u32x2 first, u32x2 second, bool odd) {
  const u32x2 give = odd ? first : second;
  const unsigned g0 = (unsigned)__builtin_amdgcn_update_dpp(0, (int)give[0], 0x128, 0xf, 0xf, false), g1 = (unsigned)__builtin_amdgcn_update_dpp(0, (int)give[1], 0x128, 0xf, 0xf, false);
  return odd ? u32x4{g0, g1, second[0], second[1]} : u32x4{first[0], first[1], g0, g1};
}
constexpr unsigned YTAG = 0x7A11D00Du, PTAG = 0x600DF1A6u;
#define GAS __attribute__((address_space(1)))
__device__ __forceinline__ void tail_body(const GAS u16* __restrict__ x, const GAS u16* __restrict__ wr, const GAS u16* __restrict__ wg, const GAS u16* __restrict__ wu, const GAS u16* __restrict__ wd,
                                          GAS char* __restrict__ ws, GAS u16* __restrict__ out, char* lds, const int tid, const int blk) {
  const int lane = tid & 63, w = __builtin_amdgcn_readfirstlane(tid >> 6);
  const int g = 2 * (blk & 7) + ((blk >> 3) & 1), rb = blk >> 4;
  const unsigned ldsb = (unsigned)(size_t)lds;
  const __amdgpu_buffer_rsrc_t rws = __builtin_amdgcn_make_buffer_rsrc((void*)ws, 0, (int)WS_PART, 0x00020000);
  const __amdgpu_buffer_rsrc_t rpart = __builtin_amdgcn_make_buffer_rsrc((void*)(ws + WS_PART), 0, 16 * 16 * 4 * 36864, 0x00020000);
  const __amdgpu_buffer_rsrc_t rout = __builtin_amdgcn_make_buffer_rsrc((void*)out, 0, S * D * 2, 0x00020000);
  gu32* sync = (gu32*)(ws + WS_SYNC);
  const bool is_moe = blk < 144, is_fin = blk >= 144 && blk < 216;
  const int slice = (blk & 7) * 18 + (blk >> 3);
  if (is_moe) {
#pragma unroll
    for (int i = 0; i < 5; ++i) { const int o = i * 256 + tid; if (o < NSEL * D / 4) st16_sc1(u32x4{0u, 0u, 0u, 0u}, rws, (unsigned)(slice * NSEL * D * 4 + o * 16), (unsigned)WS_YP); }
  }
  {
    DRAIN(); RAW_BARRIER();
    if (tid == 192) __hip_atomic_store(sync + (SY_CNT1 + rb * 256) / 4 + g, PTAG, RLX_AGENT);
  }
  if (is_moe && w < 3) {
    if (w < 2) {
      const __amdgpu_buffer_rsrc_t rg = __builtin_amdgcn_make_buffer_rsrc((void*)(w == 0 ? wg : wu), 0, D * F * 2, 0x00020000);
      const unsigned vgu = (unsigned)(((lane >> 5) * 8 + ((lane >> 1) & 7)) * (F * 2) + ((lane >> 4) & 1) * 32 + (lane & 1) * 16);
#pragma unroll
      for (int i = 0; i < 36; ++i) dma16(rg, vgu, (unsigned)(16 * i * (F * 2) + slice * 64), ldsb + TL_GU + w * 36864 + i * 1024);
    } else {
      const __amdgpu_buffer_rsrc_t rd = __builtin_amdgcn_make_buffer_rsrc((void*)wd, 0, D * F * 2, 0x00020000);
      const unsigned vdn = (unsigned)(((lane >> 1) & 7) * (D * 2) + (lane >> 4) * 32 + (lane & 1) * 16);
#pragma unroll
      for (int i = 0; i < 36; ++i) dma16(rd, vdn, (unsigned)((32 * slice + 8 * (i / 9)) * (D * 2) + (i % 9) * 128), ldsb + TL_DN + (i / 9) * 9216 + (i % 9) * 1024);
    }
  }
  const int q7 = lane & 7, row = rb * 128 + g * 8 + q7;
  const unsigned pbase = (unsigned)((rb * 16 + g) * 9216) + lane * 16;
  u32x4 p0[16], p1[16], p2[4];
  {
    unsigned done = 0;
    for (unsigned sp = 0; sp < SPIN_CAP; ++sp) {
      asm volatile("" ::: "memory");
      const unsigned f = lane < 16 ? __hip_atomic_load(sync + (SY_CNT1 + rb * 256) / 4 + lane, RLX_AGENT) : 0u;
      const unsigned ready = (unsigned)__ballot(f == PTAG) & 0xffffu, fresh = ready & ~done;
#pragma unroll
      for (int i = 0; i < 16; ++i) if ((fresh >> i) & 1u) {
        p0[i] = ld16_sc1(rpart, pbase + w * 1024, i * (16 * 16 * 9216)); p1[i] = ld16_sc1(rpart, pbase + (w + 4) * 1024, i * (16 * 16 * 9216));
        if ((i >> 2) == w) p2[i & 3] = ld16_sc1(rpart, pbase + 8 * 1024, i * (16 * 16 * 9216)); }
      done = ready;
      if (done == 0xffffu) break;
      __builtin_amdgcn_s_sleep(1);
    }
  }
  const int Gl = lane >> 4, nl = 16 * (Gl & 1) + 8 * (Gl >> 1) + 4 * ((lane >> 3) & 1);
  const GAS float* mod = (const GAS float*)(ws + WS_MOD);
  u32x2 xa[3], xb[3], wa[3], wb[3]; f32x4 m0[3][4], m1[3][4];
#pragma unroll
  for (int k = 0; k < 3; ++k) { const int n0 = (k < 2 ? w + 4 * k : 8) * 64 + nl;
    xa[k] = *(const GAS u32x2*)(x + (size_t)row * D + n0); xb[k] = *(const GAS u32x2*)(x + (size_t)row * D + n0 + 32);
    wa[k] = *(const GAS u32x2*)(wr + n0); wb[k] = *(const GAS u32x2*)(wr + n0 + 32);
#pragma unroll
    for (int v = 0; v < 4; ++v) { m0[k][v] = *(const GAS f32x4*)(mod + v * D + n0); m1[k][v] = *(const GAS f32x4*)(mod + v * D + n0 + 32); } }
  bf16x8 id1, id2;
  { const int i15 = lane & 15; const unsigned one = (i15 & 1) ? 0x3f800000u : 0x00003f80u; u32x4 u1 = {0u, 0u, 0u, 0u}, u2 = {0u, 0u, 0u, 0u};
#pragma unroll
    for (int e = 0; e < 4; ++e) { const bool hit = ((i15 & 7) >> 1) == e; u1[e] = (hit && Gl == (i15 >> 3)) ? one : 0u; u2[e] = (hit && Gl == 2 + (i15 >> 3)) ? one : 0u; }
    id1 = __builtin_bit_cast(bf16x8, u1); id2 = __builtin_bit_cast(bf16x8, u2); }
  f32x4 s0[3] = {}, s1[3] = {};
#pragma unroll
  for (int i = 0; i < 16; ++i) {
    s0[0] = __builtin_amdgcn_mfma_f32_16x16x32_bf16(id1, __builtin_bit_cast(bf16x8, p0[i]), s0[0], 0, 0, 0); s1[0] = __builtin_amdgcn_mfma_f32_16x16x32_bf16(id2, __builtin_bit_cast(bf16x8, p0[i]), s1[0], 0, 0, 0);
    s0[1] = __builtin_amdgcn_mfma_f32_16x16x32_bf16(id1, __builtin_bit_cast(bf16x8, p1[i]), s0[1], 0, 0, 0); s1[1] = __builtin_amdgcn_mfma_f32_16x16x32_bf16(id2, __builtin_bit_cast(bf16x8, p1[i]), s1[1], 0, 0, 0); }
#pragma unroll
  for (int i = 0; i < 4; ++i) {
    s0[2] = __builtin_amdgcn_mfma_f32_16x16x32_bf16(id1, __builtin_bit_cast(bf16x8, p2[i]), s0[2], 0, 0, 0); s1[2] = __builtin_amdgcn_mfma_f32_16x16x32_bf16(id2, __builtin_bit_cast(bf16x8, p2[i]), s1[2], 0, 0, 0); }
  float* stl = (float*)(lds + TL_ST);
  f32x4* c8 = (f32x4*)(lds + TL_RED);
  c8[(w * 64 + lane) * 2] = s0[2]; c8[(w * 64 + lane) * 2 + 1] = s1[2];
  RAW_BARRIER();
  { f32x4 t0 = c8[lane * 2], t1 = c8[lane * 2 + 1];
#pragma unroll
    for (int i = 1; i < 4; ++i) { t0 += c8[(i * 64 + lane) * 2]; t1 += c8[(i * 64 + lane) * 2 + 1]; }
    s0[2] = t0; s1[2] = t1; }
  float x1[3][8], sm = 0.f;
#pragma unroll
  for (int k = 0; k < 3; ++k) {
    const float xv[8] = {bflo(xa[k][0]), bfhi(xa[k][0]), bflo(xa[k][1]), bfhi(xa[k][1]), bflo(xb[k][0]), bfhi(xb[k][0]), bflo(xb[k][1]), bfhi(xb[k][1])};
#pragma unroll
    for (int e = 0; e < 8; e += 2) {
      const float a0 = e < 4 ? s0[k][e] : s1[k][e - 4], a1 = e < 4 ? s0[k][e + 1] : s1[k][e - 3], g0 = e < 4 ? m0[k][0][e] : m1[k][0][e - 4], g1 = e < 4 ? m0[k][0][e + 1] : m1[k][0][e - 3];
      const unsigned pk = cvtpk(fmaf(g0, a0, xv[e]), fmaf(g1, a1, xv[e + 1])); x1[k][e] = bflo(pk); x1[k][e + 1] = bfhi(pk);
      if (k < 2 || w == 0) sm += x1[k][e] + x1[k][e + 1]; } }
  const float cnt = w == 0 ? 192.f : 128.f;
  sm = sum_hi8(sm);
  const float mw = sm * (1.f / cnt);
  float m2 = 0.f;
#pragma unroll
  for (int k = 0; k < 3; ++k) if (k < 2 || w == 0) {
#pragma unroll
    for (int e = 0; e < 8; ++e) { const float d = x1[k][e] - mw; m2 = fmaf(d, d, m2); } }
  m2 = sum_hi8(m2);
  if (lane < 8) { stl[w * 16 + lane] = sm; stl[w * 16 + 8 + lane] = m2; }
  RAW_BARRIER();
  float mean, rs;
  { const float a0 = stl[q7], a1 = stl[16 + q7], a2 = stl[32 + q7], a3 = stl[48 + q7];
    mean = (a0 + a1 + a2 + a3) * (1.f / D);
    const float d0 = a0 * (1.f / 192.f) - mean, d1 = a1 * (1.f / 128.f) - mean, d2 = a2 * (1.f / 128.f) - mean, d3 = a3 * (1.f / 128.f) - mean;
    const float M2 = stl[8 + q7] + stl[24 + q7] + stl[40 + q7] + stl[56 + q7] + 192.f * d0 * d0 + 128.f * (d1 * d1 + d2 * d2 + d3 * d3);
    rs = rsqrtf(M2 * (1.f / D) + 1e-5f); }
  float rr01 = 0.f;
#pragma unroll
  for (int k = 0; k < 3; ++k) {
    const float wrv[8] = {bflo(wa[k][0]), bfhi(wa[k][0]), bflo(wa[k][1]), bfhi(wa[k][1]), bflo(wb[k][0]), bfhi(wb[k][0]), bflo(wb[k][1]), bfhi(wb[k][1])};
    unsigned hp[4], op[4], xp[4];
#pragma unroll
    for (int e = 0; e < 8; e += 2) {
      const float A0 = e < 4 ? m0[k][1][e] : m1[k][1][e - 4], A1 = e < 4 ? m0[k][1][e + 1] : m1[k][1][e - 3], b0 = e < 4 ? m0[k][2][e] : m1[k][2][e - 4], b1 = e < 4 ? m0[k][2][e + 1] : m1[k][2][e - 3];
      const float g0 = e < 4 ? m0[k][3][e] : m1[k][3][e - 4], g1 = e < 4 ? m0[k][3][e + 1] : m1[k][3][e - 3];
      hp[e >> 1] = cvtpk(fmaf((x1[k][e] - mean) * rs, A0, b0), fmaf((x1[k][e + 1] - mean) * rs, A1, b1));
      const float h0 = bflo(hp[e >> 1]), h1 = bfhi(hp[e >> 1]);
      op[e >> 1] = cvtpk(fmaf(g0, h0, x1[k][e]), fmaf(g1, h1, x1[k][e + 1]));
      xp[e >> 1] = (__float_as_uint(x1[k][e]) >> 16) | __float_as_uint(x1[k][e + 1]);
      if (k < 2 || w == 0) rr01 = fmaf(h0, wrv[e], fmaf(h1, wrv[e + 1], rr01)); }
    const bool odd = (lane >> 3) & 1;
    const u32x4 vo = widen8(u32x2{op[0], op[1]}, u32x2{op[2], op[3]}, odd), vh = widen8(u32x2{hp[0], hp[1]}, u32x2{hp[2], hp[3]}, odd), vx = widen8(u32x2{xp[0], xp[1]}, u32x2{xp[2], xp[3]}, odd);
    if (k < 2 || w == 0) {
      const unsigned ob = (unsigned)((row * D + (k < 2 ? w + 4 * k : 8) * 64 + nl + (odd ? 28 : 0)) * 2);
      st16_sc1(vo, rout, ob, 0); st16_sc1(vh, rws, ob, (unsigned)WS_H2); st16_sc1(vx, rws, ob, (unsigned)WS_X1);
    } }
  const float rr2 = 0.f;
  float rr = sum_hi8(rr01 + rr2);
  if (lane < 8) stl[64 + w * 8 + lane] = rr;
  DRAIN();
  RAW_BARRIER();
  if (tid < 4) { const float r0 = stl[64 + 2 * tid] + stl[72 + 2 * tid] + stl[80 + 2 * tid] + stl[88 + 2 * tid];
    const float r1 = stl[65 + 2 * tid] + stl[73 + 2 * tid] + stl[81 + 2 * tid] + stl[89 + 2 * tid];
    __hip_atomic_store((gu64*)(ws + WS_SYNC + SY_GRAN) + (rb * 16 + g) * 4 + tid, ((unsigned long long)GTAG << 32) | cvtpk(r0, r1), RLX_AGENT); }
  if (!is_moe && !is_fin) return;
  u32x2 fa[5][4], fb[5][4], da[9], db[9];
  if (is_moe) {
    const unsigned tgu = ldsb + TL_GU + (lane >> 5) * 512 + ((lane >> 2) & 7) * 32 + (lane & 3) * 8;
#pragma unroll
    for (int i = 0; i < 5; ++i) { const int s = (i < 4) ? w + 4 * i : 16 + (w & 1); const unsigned a0 = tgu + s * 2048, a1 = a0 + 36864;
      asm volatile("ds_read_b64_tr_b16 %0, %8\n\tds_read_b64_tr_b16 %1, %8 offset:1024\n\tds_read_b64_tr_b16 %2, %8 offset:256\n\tds_read_b64_tr_b16 %3, %8 offset:1280\n\t"
                   "ds_read_b64_tr_b16 %4, %9\n\tds_read_b64_tr_b16 %5, %9 offset:1024\n\tds_read_b64_tr_b16 %6, %9 offset:256\n\tds_read_b64_tr_b16 %7, %9 offset:1280\n\ts_waitcnt lgkmcnt(0)"
                   : "=&v"(fa[i][0]), "=&v"(fb[i][0]), "=&v"(fa[i][1]), "=&v"(fb[i][1]), "=&v"(fa[i][2]), "=&v"(fb[i][2]), "=&v"(fa[i][3]), "=&v"(fb[i][3]) : "v"(a0), "v"(a1) : "memory"); }
    const unsigned tdn = ldsb + TL_DN + (lane >> 5) * 9216 + ((lane >> 2) & 7) * 32 + (lane & 3) * 8 + w * 9 * 256, tdn2 = tdn + 2 * 9216;
#pragma unroll
    for (int i = 0; i < 9; i += 3)
      asm volatile("ds_read_b64_tr_b16 %0, %6 offset:%c8\n\tds_read_b64_tr_b16 %1, %7 offset:%c8\n\tds_read_b64_tr_b16 %2, %6 offset:%c9\n\tds_read_b64_tr_b16 %3, %7 offset:%c9\n\t"
                   "ds_read_b64_tr_b16 %4, %6 offset:%c10\n\tds_read_b64_tr_b16 %5, %7 offset:%c10\n\ts_waitcnt lgkmcnt(0)"
                   : "=&v"(da[i]), "=&v"(db[i]), "=&v"(da[i + 1]), "=&v"(db[i + 1]), "=&v"(da[i + 2]), "=&v"(db[i + 2]) : "v"(tdn), "v"(tdn2), "i"(i * 256), "i"(i * 256 + 256), "i"(i * 256 + 512) : "memory");
  }
  unsigned sc[4];
  { for (unsigned sp = 0; sp < SPIN_CAP; ++sp) {
      asm volatile("" ::: "memory");
      const u32x4 ga = ld16_sc1(rws, (unsigned)(WS_SYNC + SY_GRAN) + tid * 32, 0), gb = ld16_sc1(rws, (unsigned)(WS_SYNC + SY_GRAN) + tid * 32 + 16, 0);
      sc[0] = ga[0]; sc[1] = ga[2]; sc[2] = gb[0]; sc[3] = gb[2];
      const bool ok = ga[1] == GTAG && ga[3] == GTAG && gb[1] == GTAG && gb[3] == GTAG;
      if (__all(ok)) break;
      __builtin_amdgcn_s_sleep(1);
    } }
  unsigned k8[8];
#pragma unroll
  for (int e = 0; e < 4; ++e) { const unsigned p = sc[e], x = p ^ ((((p >> 15) & 0x00010001u) * 0x7fffu) | 0x80008000u);
    k8[2 * e] = (x << 16) | (unsigned)(65535 - (tid * 8 + 2 * e)); k8[2 * e + 1] = (x & 0xffff0000u) | (unsigned)(65534 - (tid * 8 + 2 * e)); }
  cswap(k8[0], k8[1]); cswap(k8[2], k8[3]); cswap(k8[4], k8[5]); cswap(k8[6], k8[7]);
  cswap(k8[0], k8[2]); cswap(k8[1], k8[3]); cswap(k8[4], k8[6]); cswap(k8[5], k8[7]);
  cswap(k8[1], k8[2]); cswap(k8[5], k8[6]); cswap(k8[0], k8[4]); cswap(k8[3], k8[7]);
  cswap(k8[1], k8[5]); cswap(k8[2], k8[6]);
  cswap(k8[1], k8[4]); cswap(k8[3], k8[6]);
  cswap(k8[2], k8[4]); cswap(k8[3], k8[5]);
  cswap(k8[3], k8[4]);
  unsigned best = 0;
#pragma unroll
  for (int it = 0; it < 8; ++it) {
    const unsigned wm = wave_max_s(k8[0]);
    const bool win = k8[0] == wm;
#pragma unroll
    for (int e = 0; e < 7 - it; ++e) k8[e] = win ? k8[e + 1] : k8[e];
    best = (lane == it) ? wm : best;
  }
  unsigned* cand = (unsigned*)(lds + TL_CAND);
  if (lane < 8) cand[w * 8 + lane] = best;
  RAW_BARRIER();
  unsigned mykey = 0;
  { unsigned c = lane < 32 ? cand[lane] : 0u;
#pragma unroll
    for (int it = 0; it < 8; ++it) { const unsigned wm = wave_max_s(c); c = (c == wm) ? 0u : c; mykey = ((lane & 7) == it) ? wm : mykey; } }
  const int myrow = 65535 - (int)(mykey & 0xffffu);
  const int G = lane >> 4;
  if (is_moe) {
    u32x2 xl[5], xh[5];
#pragma unroll
    for (int i = 0; i < 5; ++i) { const int s = (i < 4) ? w + 4 * i : 16 + (w & 1); const unsigned o = (unsigned)((myrow * D + 4 * G + 32 * s) * 2);
      xl[i] = ld8_sc1(rws, o, (unsigned)WS_H2); xh[i] = ld8_sc1(rws, o + 32, (unsigned)WS_H2); }
    f32x4 acc[4] = {};
#pragma unroll
    for (int i = 0; i < 5; ++i) { if (i == 4 && w >= 2) break;
      const u32x4 xx = {xl[i][0], xl[i][1], xh[i][0], xh[i][1]}; const bf16x8 xb8 = __builtin_bit_cast(bf16x8, xx);
#pragma unroll
      for (int mc = 0; mc < 4; ++mc) acc[mc] = __builtin_amdgcn_mfma_f32_16x16x32_bf16(frag_of(fa[i][mc], fb[i][mc]), xb8, acc[mc], 0, 0, 0); }
    f32x4* red = (f32x4*)(lds + TL_RED);
#pragma unroll
    for (int mc = 0; mc < 4; ++mc) red[(w * 4 + mc) * 64 + lane] = acc[mc];
    RAW_BARRIER();
    f32x4 t[4];
#pragma unroll
    for (int mc = 0; mc < 4; ++mc) t[mc] = red[mc * 64 + lane] + red[(4 + mc) * 64 + lane] + red[(8 + mc) * 64 + lane] + red[(12 + mc) * 64 + lane];
    float av[8];
#pragma unroll
    for (int e = 0; e < 8; ++e) { const float gg = rbf(t[e >> 2][e & 3]), uu = rbf(t[2 + (e >> 2)][e & 3]); av[e] = rbf(gg * __builtin_amdgcn_rcpf(1.f + __expf(-gg))) * uu; }
    const u32x4 aw = {cvtpk(av[0], av[1]), cvtpk(av[2], av[3]), cvtpk(av[4], av[5]), cvtpk(av[6], av[7])};
    const bf16x8 ab = __builtin_bit_cast(bf16x8, aw);
    const unsigned yo = (unsigned)(((slice * NSEL + (lane & 7)) * D + 4 * G) * 4);
#pragma unroll
    for (int i = 0; i < 9; ++i) { const int nb = w * 9 + i;
      const f32x4 y = __builtin_amdgcn_mfma_f32_16x16x32_bf16(frag_of(da[i], db[i]), ab, f32x4{0.f, 0.f, 0.f, 0.f}, 0, 0, 0);
      if ((lane & 15) < 8) st16_sc1(u32x4{cvtpk(y[0], y[1]), YTAG, cvtpk(y[2], y[3]), YTAG}, rws, yo + nb * 64, (unsigned)WS_YP); }
  } else {
    const int fb = blk - 144, j = fb / 9, o4 = tid & 15, gr = tid >> 4, n0 = (fb - j * 9) * 64 + o4 * 4;
    const unsigned kj = __builtin_amdgcn_readlane(mykey, j);
    const int rowj = 65535 - (int)(kj & 0xffffu);
    unsigned ub = kj >> 16; ub = (ub & 0x8000u) ? (ub & 0x7fffu) : (~ub & 0xffffu);
    const float sig = rbf(1.f / (1.f + __expf(-__uint_as_float(ub << 16))));
    const u32x2 hh = ld8_sc1(rws, (unsigned)((rowj * D + n0) * 2), (unsigned)WS_H2), xx = ld8_sc1(rws, (unsigned)((rowj * D + n0) * 2), (unsigned)WS_X1);
    const f32x4 gb = *(const GAS f32x4*)(mod + 3 * D + n0);
    u32x4 v[9];
    for (unsigned sp = 0; sp < SPIN_CAP; ++sp) {
      asm volatile("" ::: "memory");
      bool ok = true;
#pragma unroll
      for (int i = 0; i < 9; ++i) { v[i] = ld16_sc1(rws, (unsigned)((((gr * 9 + i) * NSEL + j) * D + n0) * 4), (unsigned)WS_YP); }
#pragma unroll
      for (int i = 0; i < 9; ++i) ok = ok && v[i][1] == YTAG && v[i][3] == YTAG;
      if (__all(ok)) break;
      __builtin_amdgcn_s_sleep(1);
    }
    f32x4 s4 = {0.f, 0.f, 0.f, 0.f};
#pragma unroll
    for (int i = 0; i < 9; ++i) s4 += f32x4{bflo(v[i][0]), bfhi(v[i][0]), bflo(v[i][2]), bfhi(v[i][2])};
    f32x4* red = (f32x4*)(lds + TL_RED);
    red[gr * 16 + o4] = s4;
    RAW_BARRIER();
    if (tid < 16) {
      f32x4 y = red[tid];
#pragma unroll
      for (int i = 1; i < 16; ++i) y += red[i * 16 + tid];
      const float hv[4] = {bflo(hh[0]), bfhi(hh[0]), bflo(hh[1]), bfhi(hh[1])}, xv[4] = {bflo(xx[0]), bfhi(xx[0]), bflo(xx[1]), bfhi(xx[1])};
      float o[4];
#pragma unroll
      for (int e = 0; e < 4; ++e) o[e] = xv[e] + rbf(gb[e] * rbf(hv[e] + rbf(rbf(y[e]) * sig)));
      st8_sc1(u32x2{cvtpk(o[0], o[1]), cvtpk(o[2], o[3])}, rout, (unsigned)((rowj * D + n0) * 2), 0);
    }
  }
}

typedef unsigned long long u64_t;
typedef const __attribute__((address_space(4))) u64_t* karg_t;
__global__ __launch_bounds__(256, 1) void k_main(const void* nfr, const void* par, const void* wq, const void* K, const void* V, const void* wo, void* part, void* dbg,
                                                 const void* x, const void* wr, const void* wg, const void* wu, const void* wd, void* ws, void* out) {
  extern __shared__ __attribute__((aligned(16))) char lds[];
  unsigned long long kp = (unsigned long long)__builtin_amdgcn_kernarg_segment_ptr(); unsigned blk = blockIdx.x, tid = threadIdx.x;
  asm volatile(
    "s_load_dwordx16 s[80:95], %0, 0\n"
    "s_mov_b32 s50, %1\n"
    "s_mov_b32 s75, %1\n"
    "v_and_b32 v1, 63, %2\n"
    "v_lshrrev_b32 v2, 6, %2\n"
    "s_nop 0\n"
    "v_readfirstlane_b32 s74, v2\n"
    "s_getpc_b64 s[96:97]\n"
    "KM_PC_%=:\n"
    "K_MAIN_CODE_BEGIN:\n"
    "s_mov_b32 s98, K_MAIN_CODE_END-KM_PC_%=\n"
    "s_and_b32 s97, s97, 0xffff\n"
    "s_mov_b32 s99, 0x20000\n"
    "s_lshr_b32 s52, s75, 3\n"
    "s_lshl_b32 s52, s52, 2\n"
    "s_add_u32 s52, s52, s74\n"
    "s_lshl_b32 s52, s52, 10\n"
    "v_and_b32 v28, 7, v1\n"
    "v_lshlrev_b32 v28, 7, v28\n"
    "buffer_load_dword a250, v28, s[96:99], s52 offen\n"
    "s_and_b32 s27, s50, 7\n"
    "s_lshl_b32 s27, s27, 1\n"
    "s_bfe_u32 s52, s50, 0x10003\n"
    "s_add_u32 s27, s27, s52\n"
    "s_lshr_b32 s28, s50, 4\n"
    "s_lshl_b32 s26, s74, 12\n"
    "s_add_u32 s70, s26, 0x10000\n"
    "s_lshl_b32 s68, s74, 16\n"
    "s_lshl_b32 s52, s27, 9\n"
    "s_add_u32 s68, s68, s52\n"
    "s_lshl_b32 s52, s28, 3\n"
    "s_lshl_b32 s53, s74, 1\n"
    "s_add_u32 s52, s52, s53\n"
    "s_mul_i32 s69, s52, 18432\n"
    "v_lshlrev_b32 v21, 4, v1\n"
    "v_lshrrev_b32 v27, 4, v1\n"
    "v_bfe_u32 v28, v1, 1, 3\n"
    "v_lshlrev_b32 v28, 13, v28\n"
    "v_lshl_add_u32 v28, v27, 5, v28\n"
    "v_and_b32 v29, 1, v1\n"
    "v_lshl_add_u32 v28, v29, 4, v28\n"
    "v_add_u32 v8, 2688, v28\n"
    "v_add_u32 v9, 1792, v28\n"
    "v_add_u32 v10, 896, v28\n"
    "v_add_u32 v11, 0, v28\n"
    "s_waitcnt lgkmcnt(0)\n"
    "s_mov_b64 s[64:65], s[82:83]\n"
    "s_and_b32 s65, s65, 0xffff\n"
    "s_mov_b32 s66, 0x2000\n"
    "s_mov_b32 s67, 0x20000\n"
    "s_mov_b64 s[16:17], s[80:81]\n"
    "s_and_b32 s17, s17, 0xffff\n"
    "s_mov_b32 s18, 0x240000\n"
    "s_mov_b32 s19, 0x20000\n"
    "s_mov_b64 s[60:61], s[84:85]\n"
    "s_sub_u32 s60, s60, 2688\n"
    "s_subb_u32 s61, s61, 0\n"
    "s_and_b32 s61, s61, 0xffff\n"
    "s_mov_b32 s62, 0x480a80\n"
    "s_mov_b32 s63, 0x20000\n"
    "s_lshl_b32 s52, s74, 11\n"
    "s_add_u32 m0, s52, 0xc000\n"
    "s_nop 0\n"
    "buffer_load_dwordx4 v21, s[64:67], s52 offen offset:0 lds\n"
    "buffer_load_dwordx4 v21, s[64:67], s52 offen offset:1024 lds\n"
    "s_add_u32 s55, s69, 0x0\n"
    "buffer_load_dwordx4 v[192:195], v21, s[16:19], s55 offen\n"
    "s_add_u32 s55, s69, 0x4800\n"
    "buffer_load_dwordx4 v[196:199], v21, s[16:19], s55 offen\n"
    "s_add_u32 m0, s70, 0x0\n"
    "s_nop 0\n"
    "buffer_load_dwordx4 v8, s[60:63], s68 offen offset:0 lds\n"
    "buffer_load_dwordx4 v9, s[60:63], s68 offen offset:1024 lds\n"
    "buffer_load_dwordx4 v10, s[60:63], s68 offen offset:2048 lds\n"
    "buffer_load_dwordx4 v11, s[60:63], s68 offen offset:3072 lds\n"
    "s_add_u32 s68, s68, 0x40000\n"
    "s_add_u32 s55, s69, 0x400\n"
    "buffer_load_dwordx4 v[200:203], v21, s[16:19], s55 offen\n"
    "s_add_u32 s55, s69, 0x4c00\n"
    "buffer_load_dwordx4 v[204:207], v21, s[16:19], s55 offen\n"
    "s_add_u32 m0, s70, 0x4000\n"
    "s_nop 0\n"
    "buffer_load_dwordx4 v8, s[60:63], s68 offen offset:0 lds\n"
    "buffer_load_dwordx4 v9, s[60:63], s68 offen offset:1024 lds\n"
    "buffer_load_dwordx4 v10, s[60:63], s68 offen offset:2048 lds\n"
    "buffer_load_dwordx4 v11, s[60:63], s68 offen offset:3072 lds\n"
    "s_add_u32 s68, s68, 0x40000\n"
    "s_add_u32 s55, s69, 0x800\n"
    "buffer_load_dwordx4 v[208:211], v21, s[16:19], s55 offen\n"
    "s_add_u32 s55, s69, 0x5000\n"
    "buffer_load_dwordx4 v[212:215], v21, s[16:19], s55 offen\n"
    "s_add_u32 m0, s70, 0x8000\n"
    "s_nop 0\n"
    "buffer_load_dwordx4 v8, s[60:63], s68 offen offset:0 lds\n"
    "buffer_load_dwordx4 v9, s[60:63], s68 offen offset:1024 lds\n"
    "buffer_load_dwordx4 v10, s[60:63], s68 offen offset:2048 lds\n"
    "buffer_load_dwordx4 v11, s[60:63], s68 offen offset:3072 lds\n"
    "s_add_u32 s68, s68, 0x40000\n"
    "s_add_u32 s55, s69, 0xc00\n"
    "buffer_load_dwordx4 v[216:219], v21, s[16:19], s55 offen\n"
    "s_add_u32 s55, s69, 0x5400\n"
    "buffer_load_dwordx4 v[220:223], v21, s[16:19], s55 offen\n"
    "s_add_u32 m0, s70, 0xc000\n"
    "s_nop 0\n"
    "buffer_load_dwordx4 v8, s[60:63], s68 offen offset:0 lds\n"
    "buffer_load_dwordx4 v9, s[60:63], s68 offen offset:1024 lds\n"
    "buffer_load_dwordx4 v10, s[60:63], s68 offen offset:2048 lds\n"
    "buffer_load_dwordx4 v11, s[60:63], s68 offen offset:3072 lds\n"
    "s_add_u32 s68, s68, 0x40000\n"
    "s_add_u32 s55, s69, 0x1000\n"
    "buffer_load_dwordx4 v[224:227], v21, s[16:19], s55 offen\n"
    "s_add_u32 s55, s69, 0x5800\n"
    "buffer_load_dwordx4 v[228:231], v21, s[16:19], s55 offen\n"
    "s_add_u32 m0, s70, 0x10000\n"
    "s_nop 0\n"
    "buffer_load_dwordx4 v8, s[60:63], s68 offen offset:0 lds\n"
    "buffer_load_dwordx4 v9, s[60:63], s68 offen offset:1024 lds\n"
    "buffer_load_dwordx4 v10, s[60:63], s68 offen offset:2048 lds\n"
    "buffer_load_dwordx4 v11, s[60:63], s68 offen offset:3072 lds\n"
    "s_add_u32 s68, s68, 0x40000\n"
    "s_add_u32 s55, s69, 0x1400\n"
    "buffer_load_dwordx4 v[232:235], v21, s[16:19], s55 offen\n"
    "s_add_u32 s55, s69, 0x5c00\n"
    "buffer_load_dwordx4 v[236:239], v21, s[16:19], s55 offen\n"
    "s_lshl_b32 s73, s74, 13\n"
    "s_lshl_b32 s52, s27, 19\n"
    "s_mov_b64 s[8:9], s[86:87]\n"
    "s_add_u32 s8, s8, s52\n"
    "s_addc_u32 s9, s9, 0\n"
    "s_and_b32 s9, s9, 0xffff\n"
    "s_mov_b32 s10, 0x80000\n"
    "s_mov_b32 s11, 0x20000\n"
    "s_mov_b64 s[12:13], s[88:89]\n"
    "s_add_u32 s12, s12, s52\n"
    "s_addc_u32 s13, s13, 0\n"
    "s_sub_u32 s12, s12, 896\n"
    "s_subb_u32 s13, s13, 0\n"
    "s_and_b32 s13, s13, 0xffff\n"
    "s_mov_b32 s14, 0x80380\n"
    "s_mov_b32 s15, 0x20000\n"
    "s_mov_b32 s25, 0x5f800000\n"
    "s_mov_b32 s41, 0x42cc0000\n"
    "s_add_u32 s22, s26, 0x0\n"
    "s_add_u32 s23, s26, 0x10000\n"
    "s_lshl_b32 s52, s27, 4\n"
    "s_add_u32 s52, s52, s28\n"
    "s_lshl_b32 s52, s52, 2\n"
    "s_add_u32 s52, s52, s74\n"
    "s_mul_i32 s71, s52, 36864\n"
    "s_lshl_b32 s52, s27, 2\n"
    "s_add_u32 s52, s52, s74\n"
    "s_mul_i32 s72, s52, 73728\n"
    "v_and_b32 v26, 15, v1\n"
    "v_lshrrev_b32 v27, 4, v1\n"
    "v_lshlrev_b32 v28, 5, v27\n"
    "v_add_u32 v20, 0xc000, v28\n"
    "v_bfe_u32 v28, v1, 2, 3\n"
    "v_lshlrev_b32 v28, 5, v28\n"
    "v_and_b32 v29, 3, v1\n"
    "v_lshl_add_u32 v28, v29, 3, v28\n"
    "v_lshrrev_b32 v29, 5, v1\n"
    "v_lshl_add_u32 v19, v29, 12, v28\n"
    "v_add_u32 v19, 0x10000, v19\n"
    "v_add_u32 v30, 0xc000, v19\n"
    "v_lshl_add_u32 v12, v29, 11, v28\n"
    "v_add_u32 v12, 0x10000, v12\n"
    "v_add_u32 v28, 0, v27\n"
    "v_xor_b32 v28, v28, v26\n"
    "v_lshlrev_b32 v28, 4, v28\n"
    "v_lshl_add_u32 v4, v26, 8, v28\n"
    "v_lshl_add_u32 v13, v27, 8, v28\n"
    "v_add_u32 v28, 4, v27\n"
    "v_xor_b32 v28, v28, v26\n"
    "v_lshlrev_b32 v28, 4, v28\n"
    "v_lshl_add_u32 v5, v26, 8, v28\n"
    "v_lshl_add_u32 v14, v27, 8, v28\n"
    "v_add_u32 v28, 8, v27\n"
    "v_xor_b32 v28, v28, v26\n"
    "v_lshlrev_b32 v28, 4, v28\n"
    "v_lshl_add_u32 v6, v26, 8, v28\n"
    "v_lshl_add_u32 v15, v27, 8, v28\n"
    "v_add_u32 v28, 12, v27\n"
    "v_xor_b32 v28, v28, v26\n"
    "v_lshlrev_b32 v28, 4, v28\n"
    "v_lshl_add_u32 v7, v26, 8, v28\n"
    "v_lshl_add_u32 v16, v27, 8, v28\n"
    "v_bfe_u32 v28, v1, 1, 3\n"
    "v_lshlrev_b32 v28, 8, v28\n"
    "v_lshl_add_u32 v28, v27, 5, v28\n"
    "v_and_b32 v29, 1, v1\n"
    "v_lshl_add_u32 v18, v29, 4, v28\n"
    "v_add_u32 v17, 896, v18\n"
    "s_load_dwordx8 s[80:87], %0, 0x50\n"
    "s_load_dwordx2 s[88:89], %0, 0x70\n"
    "v_mov_b32 v2, 0\n"
    "v_mov_b32 v3, 0\n"
    "v_mov_b32 v31, 0\n"
    "s_lshl_b32 s52, s75, 2\n"
    "s_add_u32 s52, s52, s74\n"
    "s_waitcnt lgkmcnt(0)\n"
    "s_mul_i32 s53, s52, 5184\n"
    "s_add_u32 s54, s80, s53\n"
    "s_addc_u32 s55, s81, 0\n"
    "s_and_b32 s55, s55, 0xffff\n"
    "v_writelane_b32 v2, s54, 0\n"
    "v_writelane_b32 v3, s55, 0\n"
    "s_mov_b32 s54, 0x1440\n"
    "v_writelane_b32 v31, s54, 0\n"
    "s_add_u32 s54, s82, s53\n"
    "s_addc_u32 s55, s83, 0\n"
    "s_and_b32 s55, s55, 0xffff\n"
    "v_writelane_b32 v2, s54, 1\n"
    "v_writelane_b32 v3, s55, 1\n"
    "s_mov_b32 s54, 0x1440\n"
    "v_writelane_b32 v31, s54, 1\n"
    "s_add_u32 s54, s84, s53\n"
    "s_addc_u32 s55, s85, 0\n"
    "s_and_b32 s55, s55, 0xffff\n"
    "v_writelane_b32 v2, s54, 2\n"
    "v_writelane_b32 v3, s55, 2\n"
    "s_mov_b32 s54, 0x1440\n"
    "v_writelane_b32 v31, s54, 2\n"
    "s_lshl_b32 s53, s28, 4\n"
    "s_add_u32 s53, s53, s27\n"
    "s_lshl_b32 s53, s53, 2\n"
    "s_add_u32 s53, s53, s74\n"
    "s_mul_i32 s53, s53, 2304\n"
    "s_mul_i32 s53, s27, 0x48000\n"
    "s_lshl_b32 s56, s28, 2\n"
    "s_add_u32 s56, s56, s74\n"
    "s_mul_i32 s56, s56, 4608\n"
    "s_add_u32 s53, s53, s56\n"
    "s_add_u32 s54, s90, s53\n"
    "s_addc_u32 s55, s91, 0\n"
    "s_and_b32 s55, s55, 0xffff\n"
    "v_writelane_b32 v2, s54, 6\n"
    "v_writelane_b32 v3, s55, 6\n"
    "s_mov_b32 s54, 0x1200\n"
    "v_writelane_b32 v31, s54, 6\n"
    "v_min_u32 v28, 35, v1\n"
    "v_lshlrev_b32 v28, 13, v28\n"
    "s_mul_i32 s52, s28, 0x48000\n"
    "s_lshl_b32 s53, s27, 9\n"
    "s_add_u32 s52, s52, s53\n"
    "s_lshl_b32 s53, s74, 7\n"
    "s_add_u32 s52, s52, s53\n"
    "s_add_u32 s52, s52, 2688\n"
    "buffer_load_dword a250, v28, s[60:63], s52 offen\n"
    "s_waitcnt vmcnt(25)\n"
    "s_barrier\n"
    "ds_read_b128 v[48:51], v20 offset:0\n"
    "ds_read_b128 v[52:55], v20 offset:16\n"
    "ds_read_b128 v[56:59], v20 offset:2304\n"
    "ds_read_b128 v[60:63], v20 offset:2320\n"
    "ds_read_b128 v[240:243], v20 offset:128\n"
    "ds_read_b128 v[244:247], v20 offset:144\n"
    "ds_read_b128 v[248:251], v20 offset:2432\n"
    "ds_read_b128 v[252:255], v20 offset:2448\n"
    "ds_read_b64_tr_b16 a[128:129], v19 offset:0\n"
    "ds_read_b64_tr_b16 a[130:131], v19 offset:8192\n"
    "ds_read_b64_tr_b16 a[132:133], v19 offset:256\n"
    "ds_read_b64_tr_b16 a[134:135], v19 offset:8448\n"
    "ds_read_b64_tr_b16 a[136:137], v19 offset:512\n"
    "ds_read_b64_tr_b16 a[138:139], v19 offset:8704\n"
    "ds_read_b64_tr_b16 a[140:141], v19 offset:768\n"
    "ds_read_b64_tr_b16 a[142:143], v19 offset:8960\n"
    "ds_read_b64_tr_b16 a[144:145], v19 offset:1024\n"
    "ds_read_b64_tr_b16 a[146:147], v19 offset:9216\n"
    "ds_read_b64_tr_b16 a[148:149], v19 offset:1280\n"
    "ds_read_b64_tr_b16 a[150:151], v19 offset:9472\n"
    "ds_read_b64_tr_b16 a[152:153], v19 offset:1536\n"
    "ds_read_b64_tr_b16 a[154:155], v19 offset:9728\n"
    "ds_read_b64_tr_b16 a[156:157], v19 offset:1792\n"
    "ds_read_b64_tr_b16 a[158:159], v19 offset:9984\n"
    "ds_read_b64_tr_b16 a[160:161], v19 offset:2048\n"
    "ds_read_b64_tr_b16 a[162:163], v19 offset:10240\n"
    "ds_read_b64_tr_b16 a[164:165], v19 offset:2304\n"
    "ds_read_b64_tr_b16 a[166:167], v19 offset:10496\n"
    "ds_read_b64_tr_b16 a[168:169], v19 offset:2560\n"
    "ds_read_b64_tr_b16 a[170:171], v19 offset:10752\n"
    "ds_read_b64_tr_b16 a[172:173], v19 offset:2816\n"
    "ds_read_b64_tr_b16 a[174:175], v19 offset:11008\n"
    "ds_read_b64_tr_b16 a[176:177], v19 offset:3072\n"
    "ds_read_b64_tr_b16 a[178:179], v19 offset:11264\n"
    "ds_read_b64_tr_b16 a[180:181], v19 offset:3328\n"
    "ds_read_b64_tr_b16 a[182:183], v19 offset:11520\n"
    "ds_read_b64_tr_b16 a[184:185], v19 offset:3584\n"
    "ds_read_b64_tr_b16 a[186:187], v19 offset:11776\n"
    "ds_read_b64_tr_b16 a[188:189], v19 offset:3840\n"
    "ds_read_b64_tr_b16 a[190:191], v19 offset:12032\n"
    "v_lshlrev_b32 v22, 16, v192\n"
    "v_lshlrev_b32 v24, 16, v193\n"
    "v_and_b32 v23, 0xffff0000, v192\n"
    "v_and_b32 v25, 0xffff0000, v193\n"
    "s_waitcnt lgkmcnt(14)\n"
    "v_mul_f32 v22, v22, v48\n"
    "v_mul_f32 v24, v24, v50\n"
    "v_mul_f32 v23, v23, v49\n"
    "v_mul_f32 v25, v25, v51\n"
    "v_cvt_pk_bf16_f32 v22, 0, v22\n"
    "v_cvt_pk_bf16_f32 v24, 0, v24\n"
    "v_cvt_pk_bf16_f32 v23, 0, v23\n"
    "v_cvt_pk_bf16_f32 v25, 0, v25\n"
    "v_add_f32 v22, v22, v56\n"
    "v_add_f32 v24, v24, v58\n"
    "v_add_f32 v23, v23, v57\n"
    "v_add_f32 v25, v25, v59\n"
    "v_cvt_pk_bf16_f32 v32, v22, v23\n"
    "v_cvt_pk_bf16_f32 v33, v24, v25\n"
    "v_lshlrev_b32 v22, 16, v194\n"
    "v_lshlrev_b32 v24, 16, v195\n"
    "v_and_b32 v23, 0xffff0000, v194\n"
    "v_and_b32 v25, 0xffff0000, v195\n"
    "v_mul_f32 v22, v22, v52\n"
    "v_mul_f32 v24, v24, v54\n"
    "v_mul_f32 v23, v23, v53\n"
    "v_mul_f32 v25, v25, v55\n"
    "v_cvt_pk_bf16_f32 v22, 0, v22\n"
    "v_cvt_pk_bf16_f32 v24, 0, v24\n"
    "v_cvt_pk_bf16_f32 v23, 0, v23\n"
    "v_cvt_pk_bf16_f32 v25, 0, v25\n"
    "v_add_f32 v22, v22, v60\n"
    "v_add_f32 v24, v24, v62\n"
    "v_add_f32 v23, v23, v61\n"
    "v_add_f32 v25, v25, v63\n"
    "v_cvt_pk_bf16_f32 v34, v22, v23\n"
    "v_cvt_pk_bf16_f32 v35, v24, v25\n"
    "v_lshlrev_b32 v26, 16, v196\n"
    "v_lshlrev_b32 v28, 16, v197\n"
    "v_and_b32 v27, 0xffff0000, v196\n"
    "v_and_b32 v29, 0xffff0000, v197\n"
    "v_mul_f32 v26, v26, v48\n"
    "v_mul_f32 v28, v28, v50\n"
    "v_mul_f32 v27, v27, v49\n"
    "v_mul_f32 v29, v29, v51\n"
    "v_cvt_pk_bf16_f32 v26, 0, v26\n"
    "v_cvt_pk_bf16_f32 v28, 0, v28\n"
    "v_cvt_pk_bf16_f32 v27, 0, v27\n"
    "v_cvt_pk_bf16_f32 v29, 0, v29\n"
    "v_add_f32 v26, v26, v56\n"
    "v_add_f32 v28, v28, v58\n"
    "v_add_f32 v27, v27, v57\n"
    "v_add_f32 v29, v29, v59\n"
    "v_cvt_pk_bf16_f32 v36, v26, v27\n"
    "v_cvt_pk_bf16_f32 v37, v28, v29\n"
    "v_lshlrev_b32 v26, 16, v198\n"
    "v_lshlrev_b32 v28, 16, v199\n"
    "v_and_b32 v27, 0xffff0000, v198\n"
    "v_and_b32 v29, 0xffff0000, v199\n"
    "v_mul_f32 v26, v26, v52\n"
    "v_mul_f32 v28, v28, v54\n"
    "v_mul_f32 v27, v27, v53\n"
    "v_mul_f32 v29, v29, v55\n"
    "v_cvt_pk_bf16_f32 v26, 0, v26\n"
    "v_cvt_pk_bf16_f32 v28, 0, v28\n"
    "v_cvt_pk_bf16_f32 v27, 0, v27\n"
    "v_cvt_pk_bf16_f32 v29, 0, v29\n"
    "v_add_f32 v26, v26, v60\n"
    "v_add_f32 v28, v28, v62\n"
    "v_add_f32 v27, v27, v61\n"
    "v_add_f32 v29, v29, v63\n"
    "v_cvt_pk_bf16_f32 v38, v26, v27\n"
    "v_cvt_pk_bf16_f32 v39, v28, v29\n"
    "v_mfma_f32_16x16x32_bf16 v[64:67], a[128:131], v[32:35], 0\n"
    "s_waitcnt vmcnt(21)\n"
    "s_barrier\n"
    "v_lshlrev_b32 v22, 16, v200\n"
    "v_lshlrev_b32 v24, 16, v201\n"
    "v_mfma_f32_16x16x32_bf16 v[68:71], a[128:131], v[36:39], 0\n"
    "v_lshlrev_b32 v26, 16, v204\n"
    "v_lshlrev_b32 v28, 16, v205\n"
    "v_mfma_f32_16x16x32_bf16 v[72:75], a[132:135], v[32:35], 0\n"
    "v_and_b32 v23, 0xffff0000, v200\n"
    "v_and_b32 v25, 0xffff0000, v201\n"
    "v_mfma_f32_16x16x32_bf16 v[76:79], a[132:135], v[36:39], 0\n"
    "s_add_u32 m0, s70, 0x14000\n"
    "s_nop 0\n"
    "buffer_load_dwordx4 v8, s[60:63], s68 offen offset:0 lds\n"
    "v_and_b32 v27, 0xffff0000, v204\n"
    "v_mfma_f32_16x16x32_bf16 v[80:83], a[136:139], v[32:35], 0\n"
    "ds_read_b64_tr_b16 a[128:129], v19 offset:16384\n"
    "v_and_b32 v29, 0xffff0000, v205\n"
    "v_mul_f32 v22, v22, v240\n"
    "v_mul_f32 v24, v24, v242\n"
    "v_mfma_f32_16x16x32_bf16 v[84:87], a[136:139], v[36:39], 0\n"
    "ds_read_b64_tr_b16 a[130:131], v19 offset:24576\n"
    "v_mul_f32 v26, v26, v240\n"
    "v_mul_f32 v28, v28, v242\n"
    "v_mfma_f32_16x16x32_bf16 v[88:91], a[140:143], v[32:35], 0\n"
    "ds_read_b64_tr_b16 a[132:133], v19 offset:16640\n"
    "ds_read_b128 v[48:51], v20 offset:256\n"
    "buffer_load_dwordx4 v9, s[60:63], s68 offen offset:1024 lds\n"
    "v_mul_f32 v23, v23, v241\n"
    "v_mul_f32 v25, v25, v243\n"
    "v_mfma_f32_16x16x32_bf16 v[92:95], a[140:143], v[36:39], 0\n"
    "ds_read_b64_tr_b16 a[134:135], v19 offset:24832\n"
    "v_mul_f32 v27, v27, v241\n"
    "v_mul_f32 v29, v29, v243\n"
    "v_cvt_pk_bf16_f32 v22, 0, v22\n"
    "v_mfma_f32_16x16x32_bf16 v[96:99], a[144:147], v[32:35], 0\n"
    "ds_read_b64_tr_b16 a[136:137], v19 offset:16896\n"
    "v_cvt_pk_bf16_f32 v24, 0, v24\n"
    "v_cvt_pk_bf16_f32 v26, 0, v26\n"
    "v_mfma_f32_16x16x32_bf16 v[100:103], a[144:147], v[36:39], 0\n"
    "ds_read_b64_tr_b16 a[138:139], v19 offset:25088\n"
    "buffer_load_dwordx4 v10, s[60:63], s68 offen offset:2048 lds\n"
    "v_cvt_pk_bf16_f32 v28, 0, v28\n"
    "v_cvt_pk_bf16_f32 v23, 0, v23\n"
    "v_mfma_f32_16x16x32_bf16 v[104:107], a[148:151], v[32:35], 0\n"
    "ds_read_b64_tr_b16 a[140:141], v19 offset:17152\n"
    "v_cvt_pk_bf16_f32 v25, 0, v25\n"
    "v_cvt_pk_bf16_f32 v27, 0, v27\n"
    "v_cvt_pk_bf16_f32 v29, 0, v29\n"
    "v_mfma_f32_16x16x32_bf16 v[108:111], a[148:151], v[36:39], 0\n"
    "ds_read_b64_tr_b16 a[142:143], v19 offset:25344\n"
    "v_add_f32 v22, v22, v248\n"
    "v_add_f32 v24, v24, v250\n"
    "v_mfma_f32_16x16x32_bf16 v[112:115], a[152:155], v[32:35], 0\n"
    "ds_read_b64_tr_b16 a[144:145], v19 offset:17408\n"
    "ds_read_b128 v[52:55], v20 offset:272\n"
    "buffer_load_dwordx4 v11, s[60:63], s68 offen offset:3072 lds\n"
    "v_add_f32 v26, v26, v248\n"
    "v_add_f32 v28, v28, v250\n"
    "v_mfma_f32_16x16x32_bf16 v[116:119], a[152:155], v[36:39], 0\n"
    "ds_read_b64_tr_b16 a[146:147], v19 offset:25600\n"
    "v_add_f32 v23, v23, v249\n"
    "v_add_f32 v25, v25, v251\n"
    "v_add_f32 v27, v27, v249\n"
    "v_mfma_f32_16x16x32_bf16 v[120:123], a[156:159], v[32:35], 0\n"
    "ds_read_b64_tr_b16 a[148:149], v19 offset:17664\n"
    "v_add_f32 v29, v29, v251\n"
    "v_cvt_pk_bf16_f32 v40, v22, v23\n"
    "v_mfma_f32_16x16x32_bf16 v[124:127], a[156:159], v[36:39], 0\n"
    "ds_read_b64_tr_b16 a[150:151], v19 offset:25856\n"
    "s_add_u32 s68, s68, 0x40000\n"
    "s_add_u32 s55, s69, 0x1800\n"
    "buffer_load_dwordx4 v[192:195], v21, s[16:19], s55 offen\n"
    "v_cvt_pk_bf16_f32 v41, v24, v25\n"
    "v_cvt_pk_bf16_f32 v44, v26, v27\n"
    "v_mfma_f32_16x16x32_bf16 v[128:131], a[160:163], v[32:35], 0\n"
    "ds_read_b64_tr_b16 a[152:153], v19 offset:17920\n"
    "v_cvt_pk_bf16_f32 v45, v28, v29\n"
    "v_lshlrev_b32 v22, 16, v202\n"
    "v_lshlrev_b32 v24, 16, v203\n"
    "v_mfma_f32_16x16x32_bf16 v[132:135], a[160:163], v[36:39], 0\n"
    "ds_read_b64_tr_b16 a[154:155], v19 offset:26112\n"
    "v_lshlrev_b32 v26, 16, v206\n"
    "v_lshlrev_b32 v28, 16, v207\n"
    "s_waitcnt lgkmcnt(8)\n"
    "v_mfma_f32_16x16x32_bf16 v[136:139], a[164:167], v[32:35], 0\n"
    "ds_read_b64_tr_b16 a[156:157], v19 offset:18176\n"
    "ds_read_b128 v[56:59], v20 offset:2560\n"
    "s_add_u32 s55, s69, 0x6000\n"
    "buffer_load_dwordx4 v[196:199], v21, s[16:19], s55 offen\n"
    "v_and_b32 v23, 0xffff0000, v202\n"
    "v_and_b32 v25, 0xffff0000, v203\n"
    "v_mfma_f32_16x16x32_bf16 v[140:143], a[164:167], v[36:39], 0\n"
    "ds_read_b64_tr_b16 a[158:159], v19 offset:26368\n"
    "v_and_b32 v27, 0xffff0000, v206\n"
    "v_and_b32 v29, 0xffff0000, v207\n"
    "v_mul_f32 v22, v22, v244\n"
    "v_mfma_f32_16x16x32_bf16 v[144:147], a[168:171], v[32:35], 0\n"
    "ds_read_b64_tr_b16 a[160:161], v19 offset:18432\n"
    "v_mul_f32 v24, v24, v246\n"
    "v_mul_f32 v26, v26, v244\n"
    "v_mfma_f32_16x16x32_bf16 v[148:151], a[168:171], v[36:39], 0\n"
    "ds_read_b64_tr_b16 a[162:163], v19 offset:26624\n"
    "v_mul_f32 v28, v28, v246\n"
    "v_mul_f32 v23, v23, v245\n"
    "v_mfma_f32_16x16x32_bf16 v[152:155], a[172:175], v[32:35], 0\n"
    "ds_read_b64_tr_b16 a[164:165], v19 offset:18688\n"
    "v_mul_f32 v25, v25, v247\n"
    "v_mul_f32 v27, v27, v245\n"
    "v_mul_f32 v29, v29, v247\n"
    "v_mfma_f32_16x16x32_bf16 v[156:159], a[172:175], v[36:39], 0\n"
    "ds_read_b64_tr_b16 a[166:167], v19 offset:26880\n"
    "v_cvt_pk_bf16_f32 v22, 0, v22\n"
    "v_cvt_pk_bf16_f32 v24, 0, v24\n"
    "v_mfma_f32_16x16x32_bf16 v[160:163], a[176:179], v[32:35], 0\n"
    "ds_read_b64_tr_b16 a[168:169], v19 offset:18944\n"
    "ds_read_b128 v[60:63], v20 offset:2576\n"
    "v_cvt_pk_bf16_f32 v26, 0, v26\n"
    "v_cvt_pk_bf16_f32 v28, 0, v28\n"
    "v_mfma_f32_16x16x32_bf16 v[164:167], a[176:179], v[36:39], 0\n"
    "ds_read_b64_tr_b16 a[170:171], v19 offset:27136\n"
    "v_cvt_pk_bf16_f32 v23, 0, v23\n"
    "v_cvt_pk_bf16_f32 v25, 0, v25\n"
    "v_cvt_pk_bf16_f32 v27, 0, v27\n"
    "v_mfma_f32_16x16x32_bf16 v[168:171], a[180:183], v[32:35], 0\n"
    "ds_read_b64_tr_b16 a[172:173], v19 offset:19200\n"
    "v_cvt_pk_bf16_f32 v29, 0, v29\n"
    "v_add_f32 v22, v22, v252\n"
    "v_mfma_f32_16x16x32_bf16 v[172:175], a[180:183], v[36:39], 0\n"
    "ds_read_b64_tr_b16 a[174:175], v19 offset:27392\n"
    "v_add_f32 v24, v24, v254\n"
    "v_add_f32 v26, v26, v252\n"
    "v_mfma_f32_16x16x32_bf16 v[176:179], a[184:187], v[32:35], 0\n"
    "ds_read_b64_tr_b16 a[176:177], v19 offset:19456\n"
    "v_add_f32 v28, v28, v254\n"
    "v_add_f32 v23, v23, v253\n"
    "v_add_f32 v25, v25, v255\n"
    "v_mfma_f32_16x16x32_bf16 v[180:183], a[184:187], v[36:39], 0\n"
    "ds_read_b64_tr_b16 a[178:179], v19 offset:27648\n"
    "v_add_f32 v27, v27, v253\n"
    "v_add_f32 v29, v29, v255\n"
    "v_mfma_f32_16x16x32_bf16 v[184:187], a[188:191], v[32:35], 0\n"
    "ds_read_b64_tr_b16 a[180:181], v19 offset:19712\n"
    "v_cvt_pk_bf16_f32 v42, v22, v23\n"
    "v_cvt_pk_bf16_f32 v43, v24, v25\n"
    "v_mfma_f32_16x16x32_bf16 v[188:191], a[188:191], v[36:39], 0\n"
    "ds_read_b64_tr_b16 a[182:183], v19 offset:27904\n"
    "ds_read_b64_tr_b16 a[184:185], v19 offset:19968\n"
    "ds_read_b64_tr_b16 a[186:187], v19 offset:28160\n"
    "ds_read_b64_tr_b16 a[188:189], v19 offset:20224\n"
    "ds_read_b64_tr_b16 a[190:191], v19 offset:28416\n"
    "v_cvt_pk_bf16_f32 v46, v26, v27\n"
    "v_cvt_pk_bf16_f32 v47, v28, v29\n"
    "v_mfma_f32_16x16x32_bf16 v[64:67], a[128:131], v[40:43], v[64:67]\n"
    "s_waitcnt vmcnt(21)\n"
    "s_barrier\n"
    "v_lshlrev_b32 v22, 16, v208\n"
    "v_lshlrev_b32 v24, 16, v209\n"
    "v_mfma_f32_16x16x32_bf16 v[68:71], a[128:131], v[44:47], v[68:71]\n"
    "v_lshlrev_b32 v26, 16, v212\n"
    "v_lshlrev_b32 v28, 16, v213\n"
    "v_mfma_f32_16x16x32_bf16 v[72:75], a[132:135], v[40:43], v[72:75]\n"
    "v_and_b32 v23, 0xffff0000, v208\n"
    "v_and_b32 v25, 0xffff0000, v209\n"
    "v_mfma_f32_16x16x32_bf16 v[76:79], a[132:135], v[44:47], v[76:79]\n"
    "s_add_u32 m0, s70, 0x0\n"
    "s_nop 0\n"
    "buffer_load_dwordx4 v8, s[60:63], s68 offen offset:0 lds\n"
    "v_and_b32 v27, 0xffff0000, v212\n"
    "v_mfma_f32_16x16x32_bf16 v[80:83], a[136:139], v[40:43], v[80:83]\n"
    "ds_read_b64_tr_b16 a[128:129], v19 offset:32768\n"
    "v_and_b32 v29, 0xffff0000, v213\n"
    "v_mul_f32 v22, v22, v48\n"
    "v_mul_f32 v24, v24, v50\n"
    "v_mfma_f32_16x16x32_bf16 v[84:87], a[136:139], v[44:47], v[84:87]\n"
    "ds_read_b64_tr_b16 a[130:131], v19 offset:40960\n"
    "v_mul_f32 v26, v26, v48\n"
    "v_mul_f32 v28, v28, v50\n"
    "s_waitcnt lgkmcnt(8)\n"
    "v_mfma_f32_16x16x32_bf16 v[88:91], a[140:143], v[40:43], v[88:91]\n"
    "ds_read_b64_tr_b16 a[132:133], v19 offset:33024\n"
    "ds_read_b128 v[240:243], v20 offset:384\n"
    "buffer_load_dwordx4 v9, s[60:63], s68 offen offset:1024 lds\n"
    "v_mul_f32 v23, v23, v49\n"
    "v_mul_f32 v25, v25, v51\n"
    "v_mfma_f32_16x16x32_bf16 v[92:95], a[140:143], v[44:47], v[92:95]\n"
    "ds_read_b64_tr_b16 a[134:135], v19 offset:41216\n"
    "v_mul_f32 v27, v27, v49\n"
    "v_mul_f32 v29, v29, v51\n"
    "v_cvt_pk_bf16_f32 v22, 0, v22\n"
    "v_mfma_f32_16x16x32_bf16 v[96:99], a[144:147], v[40:43], v[96:99]\n"
    "ds_read_b64_tr_b16 a[136:137], v19 offset:33280\n"
    "v_cvt_pk_bf16_f32 v24, 0, v24\n"
    "v_cvt_pk_bf16_f32 v26, 0, v26\n"
    "v_mfma_f32_16x16x32_bf16 v[100:103], a[144:147], v[44:47], v[100:103]\n"
    "ds_read_b64_tr_b16 a[138:139], v19 offset:41472\n"
    "buffer_load_dwordx4 v10, s[60:63], s68 offen offset:2048 lds\n"
    "v_cvt_pk_bf16_f32 v28, 0, v28\n"
    "v_cvt_pk_bf16_f32 v23, 0, v23\n"
    "v_mfma_f32_16x16x32_bf16 v[104:107], a[148:151], v[40:43], v[104:107]\n"
    "ds_read_b64_tr_b16 a[140:141], v19 offset:33536\n"
    "v_cvt_pk_bf16_f32 v25, 0, v25\n"
    "v_cvt_pk_bf16_f32 v27, 0, v27\n"
    "v_cvt_pk_bf16_f32 v29, 0, v29\n"
    "v_mfma_f32_16x16x32_bf16 v[108:111], a[148:151], v[44:47], v[108:111]\n"
    "ds_read_b64_tr_b16 a[142:143], v19 offset:41728\n"
    "v_add_f32 v22, v22, v56\n"
    "v_add_f32 v24, v24, v58\n"
    "v_mfma_f32_16x16x32_bf16 v[112:115], a[152:155], v[40:43], v[112:115]\n"
    "ds_read_b64_tr_b16 a[144:145], v19 offset:33792\n"
    "ds_read_b128 v[244:247], v20 offset:400\n"
    "buffer_load_dwordx4 v11, s[60:63], s68 offen offset:3072 lds\n"
    "v_add_f32 v26, v26, v56\n"
    "v_add_f32 v28, v28, v58\n"
    "v_mfma_f32_16x16x32_bf16 v[116:119], a[152:155], v[44:47], v[116:119]\n"
    "ds_read_b64_tr_b16 a[146:147], v19 offset:41984\n"
    "v_add_f32 v23, v23, v57\n"
    "v_add_f32 v25, v25, v59\n"
    "v_add_f32 v27, v27, v57\n"
    "v_mfma_f32_16x16x32_bf16 v[120:123], a[156:159], v[40:43], v[120:123]\n"
    "ds_read_b64_tr_b16 a[148:149], v19 offset:34048\n"
    "v_add_f32 v29, v29, v59\n"
    "v_cvt_pk_bf16_f32 v32, v22, v23\n"
    "v_mfma_f32_16x16x32_bf16 v[124:127], a[156:159], v[44:47], v[124:127]\n"
    "ds_read_b64_tr_b16 a[150:151], v19 offset:42240\n"
    "s_add_u32 s68, s68, 0x40000\n"
    "s_add_u32 s55, s69, 0x1c00\n"
    "buffer_load_dwordx4 v[200:203], v21, s[16:19], s55 offen\n"
    "v_cvt_pk_bf16_f32 v33, v24, v25\n"
    "v_cvt_pk_bf16_f32 v36, v26, v27\n"
    "v_mfma_f32_16x16x32_bf16 v[128:131], a[160:163], v[40:43], v[128:131]\n"
    "ds_read_b64_tr_b16 a[152:153], v19 offset:34304\n"
    "v_cvt_pk_bf16_f32 v37, v28, v29\n"
    "v_lshlrev_b32 v22, 16, v210\n"
    "v_lshlrev_b32 v24, 16, v211\n"
    "v_mfma_f32_16x16x32_bf16 v[132:135], a[160:163], v[44:47], v[132:135]\n"
    "ds_read_b64_tr_b16 a[154:155], v19 offset:42496\n"
    "v_lshlrev_b32 v26, 16, v214\n"
    "v_lshlrev_b32 v28, 16, v215\n"
    "v_mfma_f32_16x16x32_bf16 v[136:139], a[164:167], v[40:43], v[136:139]\n"
    "ds_read_b64_tr_b16 a[156:157], v19 offset:34560\n"
    "ds_read_b128 v[248:251], v20 offset:2688\n"
    "s_add_u32 s55, s69, 0x6400\n"
    "buffer_load_dwordx4 v[204:207], v21, s[16:19], s55 offen\n"
    "v_and_b32 v23, 0xffff0000, v210\n"
    "v_and_b32 v25, 0xffff0000, v211\n"
    "v_mfma_f32_16x16x32_bf16 v[140:143], a[164:167], v[44:47], v[140:143]\n"
    "ds_read_b64_tr_b16 a[158:159], v19 offset:42752\n"
    "v_and_b32 v27, 0xffff0000, v214\n"
    "v_and_b32 v29, 0xffff0000, v215\n"
    "v_mul_f32 v22, v22, v52\n"
    "v_mfma_f32_16x16x32_bf16 v[144:147], a[168:171], v[40:43], v[144:147]\n"
    "ds_read_b64_tr_b16 a[160:161], v19 offset:34816\n"
    "v_mul_f32 v24, v24, v54\n"
    "v_mul_f32 v26, v26, v52\n"
    "v_mfma_f32_16x16x32_bf16 v[148:151], a[168:171], v[44:47], v[148:151]\n"
    "ds_read_b64_tr_b16 a[162:163], v19 offset:43008\n"
    "v_mul_f32 v28, v28, v54\n"
    "v_mul_f32 v23, v23, v53\n"
    "v_mfma_f32_16x16x32_bf16 v[152:155], a[172:175], v[40:43], v[152:155]\n"
    "ds_read_b64_tr_b16 a[164:165], v19 offset:35072\n"
    "v_mul_f32 v25, v25, v55\n"
    "v_mul_f32 v27, v27, v53\n"
    "v_mul_f32 v29, v29, v55\n"
    "v_mfma_f32_16x16x32_bf16 v[156:159], a[172:175], v[44:47], v[156:159]\n"
    "ds_read_b64_tr_b16 a[166:167], v19 offset:43264\n"
    "v_cvt_pk_bf16_f32 v22, 0, v22\n"
    "v_cvt_pk_bf16_f32 v24, 0, v24\n"
    "v_mfma_f32_16x16x32_bf16 v[160:163], a[176:179], v[40:43], v[160:163]\n"
    "ds_read_b64_tr_b16 a[168:169], v19 offset:35328\n"
    "ds_read_b128 v[252:255], v20 offset:2704\n"
    "v_cvt_pk_bf16_f32 v26, 0, v26\n"
    "v_cvt_pk_bf16_f32 v28, 0, v28\n"
    "v_mfma_f32_16x16x32_bf16 v[164:167], a[176:179], v[44:47], v[164:167]\n"
    "ds_read_b64_tr_b16 a[170:171], v19 offset:43520\n"
    "v_cvt_pk_bf16_f32 v23, 0, v23\n"
    "v_cvt_pk_bf16_f32 v25, 0, v25\n"
    "v_cvt_pk_bf16_f32 v27, 0, v27\n"
    "s_waitcnt lgkmcnt(8)\n"
    "v_mfma_f32_16x16x32_bf16 v[168:171], a[180:183], v[40:43], v[168:171]\n"
    "ds_read_b64_tr_b16 a[172:173], v19 offset:35584\n"
    "v_cvt_pk_bf16_f32 v29, 0, v29\n"
    "v_add_f32 v22, v22, v60\n"
    "v_mfma_f32_16x16x32_bf16 v[172:175], a[180:183], v[44:47], v[172:175]\n"
    "ds_read_b64_tr_b16 a[174:175], v19 offset:43776\n"
    "v_add_f32 v24, v24, v62\n"
    "v_add_f32 v26, v26, v60\n"
    "v_mfma_f32_16x16x32_bf16 v[176:179], a[184:187], v[40:43], v[176:179]\n"
    "ds_read_b64_tr_b16 a[176:177], v19 offset:35840\n"
    "v_add_f32 v28, v28, v62\n"
    "v_add_f32 v23, v23, v61\n"
    "v_add_f32 v25, v25, v63\n"
    "v_mfma_f32_16x16x32_bf16 v[180:183], a[184:187], v[44:47], v[180:183]\n"
    "ds_read_b64_tr_b16 a[178:179], v19 offset:44032\n"
    "v_add_f32 v27, v27, v61\n"
    "v_add_f32 v29, v29, v63\n"
    "v_mfma_f32_16x16x32_bf16 v[184:187], a[188:191], v[40:43], v[184:187]\n"
    "ds_read_b64_tr_b16 a[180:181], v19 offset:36096\n"
    "v_cvt_pk_bf16_f32 v34, v22, v23\n"
    "v_cvt_pk_bf16_f32 v35, v24, v25\n"
    "v_mfma_f32_16x16x32_bf16 v[188:191], a[188:191], v[44:47], v[188:191]\n"
    "ds_read_b64_tr_b16 a[182:183], v19 offset:44288\n"
    "ds_read_b64_tr_b16 a[184:185], v19 offset:36352\n"
    "ds_read_b64_tr_b16 a[186:187], v19 offset:44544\n"
    "ds_read_b64_tr_b16 a[188:189], v19 offset:36608\n"
    "ds_read_b64_tr_b16 a[190:191], v19 offset:44800\n"
    "v_cvt_pk_bf16_f32 v38, v26, v27\n"
    "v_cvt_pk_bf16_f32 v39, v28, v29\n"
    "v_mfma_f32_16x16x32_bf16 v[64:67], a[128:131], v[32:35], v[64:67]\n"
    "s_waitcnt vmcnt(21)\n"
    "s_barrier\n"
    "v_lshlrev_b32 v22, 16, v216\n"
    "v_lshlrev_b32 v24, 16, v217\n"
    "v_mfma_f32_16x16x32_bf16 v[68:71], a[128:131], v[36:39], v[68:71]\n"
    "v_lshlrev_b32 v26, 16, v220\n"
    "v_lshlrev_b32 v28, 16, v221\n"
    "v_mfma_f32_16x16x32_bf16 v[72:75], a[132:135], v[32:35], v[72:75]\n"
    "v_and_b32 v23, 0xffff0000, v216\n"
    "v_and_b32 v25, 0xffff0000, v217\n"
    "v_mfma_f32_16x16x32_bf16 v[76:79], a[132:135], v[36:39], v[76:79]\n"
    "s_add_u32 m0, s70, 0x4000\n"
    "s_nop 0\n"
    "buffer_load_dwordx4 v8, s[60:63], s68 offen offset:0 lds\n"
    "v_and_b32 v27, 0xffff0000, v220\n"
    "v_mfma_f32_16x16x32_bf16 v[80:83], a[136:139], v[32:35], v[80:83]\n"
    "ds_read_b64_tr_b16 a[128:129], v30 offset:0\n"
    "v_and_b32 v29, 0xffff0000, v221\n"
    "v_mul_f32 v22, v22, v240\n"
    "v_mul_f32 v24, v24, v242\n"
    "v_mfma_f32_16x16x32_bf16 v[84:87], a[136:139], v[36:39], v[84:87]\n"
    "ds_read_b64_tr_b16 a[130:131], v30 offset:8192\n"
    "v_mul_f32 v26, v26, v240\n"
    "v_mul_f32 v28, v28, v242\n"
    "v_mfma_f32_16x16x32_bf16 v[88:91], a[140:143], v[32:35], v[88:91]\n"
    "ds_read_b64_tr_b16 a[132:133], v30 offset:256\n"
    "ds_read_b128 v[48:51], v20 offset:512\n"
    "buffer_load_dwordx4 v9, s[60:63], s68 offen offset:1024 lds\n"
    "v_mul_f32 v23, v23, v241\n"
    "v_mul_f32 v25, v25, v243\n"
    "v_mfma_f32_16x16x32_bf16 v[92:95], a[140:143], v[36:39], v[92:95]\n"
    "ds_read_b64_tr_b16 a[134:135], v30 offset:8448\n"
    "v_mul_f32 v27, v27, v241\n"
    "v_mul_f32 v29, v29, v243\n"
    "v_cvt_pk_bf16_f32 v22, 0, v22\n"
    "v_mfma_f32_16x16x32_bf16 v[96:99], a[144:147], v[32:35], v[96:99]\n"
    "ds_read_b64_tr_b16 a[136:137], v30 offset:512\n"
    "v_cvt_pk_bf16_f32 v24, 0, v24\n"
    "v_cvt_pk_bf16_f32 v26, 0, v26\n"
    "v_mfma_f32_16x16x32_bf16 v[100:103], a[144:147], v[36:39], v[100:103]\n"
    "ds_read_b64_tr_b16 a[138:139], v30 offset:8704\n"
    "buffer_load_dwordx4 v10, s[60:63], s68 offen offset:2048 lds\n"
    "v_cvt_pk_bf16_f32 v28, 0, v28\n"
    "v_cvt_pk_bf16_f32 v23, 0, v23\n"
    "v_mfma_f32_16x16x32_bf16 v[104:107], a[148:151], v[32:35], v[104:107]\n"
    "ds_read_b64_tr_b16 a[140:141], v30 offset:768\n"
    "v_cvt_pk_bf16_f32 v25, 0, v25\n"
    "v_cvt_pk_bf16_f32 v27, 0, v27\n"
    "v_cvt_pk_bf16_f32 v29, 0, v29\n"
    "v_mfma_f32_16x16x32_bf16 v[108:111], a[148:151], v[36:39], v[108:111]\n"
    "ds_read_b64_tr_b16 a[142:143], v30 offset:8960\n"
    "v_add_f32 v22, v22, v248\n"
    "v_add_f32 v24, v24, v250\n"
    "v_mfma_f32_16x16x32_bf16 v[112:115], a[152:155], v[32:35], v[112:115]\n"
    "ds_read_b64_tr_b16 a[144:145], v30 offset:1024\n"
    "ds_read_b128 v[52:55], v20 offset:528\n"
    "buffer_load_dwordx4 v11, s[60:63], s68 offen offset:3072 lds\n"
    "v_add_f32 v26, v26, v248\n"
    "v_add_f32 v28, v28, v250\n"
    "v_mfma_f32_16x16x32_bf16 v[116:119], a[152:155], v[36:39], v[116:119]\n"
    "ds_read_b64_tr_b16 a[146:147], v30 offset:9216\n"
    "v_add_f32 v23, v23, v249\n"
    "v_add_f32 v25, v25, v251\n"
    "v_add_f32 v27, v27, v249\n"
    "s_waitcnt lgkmcnt(8)\n"
    "v_mfma_f32_16x16x32_bf16 v[120:123], a[156:159], v[32:35], v[120:123]\n"
    "ds_read_b64_tr_b16 a[148:149], v30 offset:1280\n"
    "v_add_f32 v29, v29, v251\n"
    "v_cvt_pk_bf16_f32 v40, v22, v23\n"
    "v_mfma_f32_16x16x32_bf16 v[124:127], a[156:159], v[36:39], v[124:127]\n"
    "ds_read_b64_tr_b16 a[150:151], v30 offset:9472\n"
    "s_add_u32 s68, s68, 0x40000\n"
    "s_add_u32 s55, s69, 0x2000\n"
    "buffer_load_dwordx4 v[208:211], v21, s[16:19], s55 offen\n"
    "v_cvt_pk_bf16_f32 v41, v24, v25\n"
    "v_cvt_pk_bf16_f32 v44, v26, v27\n"
    "v_mfma_f32_16x16x32_bf16 v[128:131], a[160:163], v[32:35], v[128:131]\n"
    "ds_read_b64_tr_b16 a[152:153], v30 offset:1536\n"
    "v_cvt_pk_bf16_f32 v45, v28, v29\n"
    "v_lshlrev_b32 v22, 16, v218\n"
    "v_lshlrev_b32 v24, 16, v219\n"
    "v_mfma_f32_16x16x32_bf16 v[132:135], a[160:163], v[36:39], v[132:135]\n"
    "ds_read_b64_tr_b16 a[154:155], v30 offset:9728\n"
    "v_lshlrev_b32 v26, 16, v222\n"
    "v_lshlrev_b32 v28, 16, v223\n"
    "v_mfma_f32_16x16x32_bf16 v[136:139], a[164:167], v[32:35], v[136:139]\n"
    "ds_read_b64_tr_b16 a[156:157], v30 offset:1792\n"
    "ds_read_b128 v[56:59], v20 offset:2816\n"
    "s_add_u32 s55, s69, 0x6800\n"
    "buffer_load_dwordx4 v[212:215], v21, s[16:19], s55 offen\n"
    "v_and_b32 v23, 0xffff0000, v218\n"
    "v_and_b32 v25, 0xffff0000, v219\n"
    "v_mfma_f32_16x16x32_bf16 v[140:143], a[164:167], v[36:39], v[140:143]\n"
    "ds_read_b64_tr_b16 a[158:159], v30 offset:9984\n"
    "v_and_b32 v27, 0xffff0000, v222\n"
    "v_and_b32 v29, 0xffff0000, v223\n"
    "v_mul_f32 v22, v22, v244\n"
    "v_mfma_f32_16x16x32_bf16 v[144:147], a[168:171], v[32:35], v[144:147]\n"
    "ds_read_b64_tr_b16 a[160:161], v30 offset:2048\n"
    "v_mul_f32 v24, v24, v246\n"
    "v_mul_f32 v26, v26, v244\n"
    "v_mfma_f32_16x16x32_bf16 v[148:151], a[168:171], v[36:39], v[148:151]\n"
    "ds_read_b64_tr_b16 a[162:163], v30 offset:10240\n"
    "v_mul_f32 v28, v28, v246\n"
    "v_mul_f32 v23, v23, v245\n"
    "v_mfma_f32_16x16x32_bf16 v[152:155], a[172:175], v[32:35], v[152:155]\n"
    "ds_read_b64_tr_b16 a[164:165], v30 offset:2304\n"
    "v_mul_f32 v25, v25, v247\n"
    "v_mul_f32 v27, v27, v245\n"
    "v_mul_f32 v29, v29, v247\n"
    "v_mfma_f32_16x16x32_bf16 v[156:159], a[172:175], v[36:39], v[156:159]\n"
    "ds_read_b64_tr_b16 a[166:167], v30 offset:10496\n"
    "v_cvt_pk_bf16_f32 v22, 0, v22\n"
    "v_cvt_pk_bf16_f32 v24, 0, v24\n"
    "v_mfma_f32_16x16x32_bf16 v[160:163], a[176:179], v[32:35], v[160:163]\n"
    "ds_read_b64_tr_b16 a[168:169], v30 offset:2560\n"
    "ds_read_b128 v[60:63], v20 offset:2832\n"
    "v_cvt_pk_bf16_f32 v26, 0, v26\n"
    "v_cvt_pk_bf16_f32 v28, 0, v28\n"
    "v_mfma_f32_16x16x32_bf16 v[164:167], a[176:179], v[36:39], v[164:167]\n"
    "ds_read_b64_tr_b16 a[170:171], v30 offset:10752\n"
    "v_cvt_pk_bf16_f32 v23, 0, v23\n"
    "v_cvt_pk_bf16_f32 v25, 0, v25\n"
    "v_cvt_pk_bf16_f32 v27, 0, v27\n"
    "v_mfma_f32_16x16x32_bf16 v[168:171], a[180:183], v[32:35], v[168:171]\n"
    "ds_read_b64_tr_b16 a[172:173], v30 offset:2816\n"
    "v_cvt_pk_bf16_f32 v29, 0, v29\n"
    "v_add_f32 v22, v22, v252\n"
    "v_mfma_f32_16x16x32_bf16 v[172:175], a[180:183], v[36:39], v[172:175]\n"
    "ds_read_b64_tr_b16 a[174:175], v30 offset:11008\n"
    "v_add_f32 v24, v24, v254\n"
    "v_add_f32 v26, v26, v252\n"
    "v_mfma_f32_16x16x32_bf16 v[176:179], a[184:187], v[32:35], v[176:179]\n"
    "ds_read_b64_tr_b16 a[176:177], v30 offset:3072\n"
    "v_add_f32 v28, v28, v254\n"
    "v_add_f32 v23, v23, v253\n"
    "v_add_f32 v25, v25, v255\n"
    "v_mfma_f32_16x16x32_bf16 v[180:183], a[184:187], v[36:39], v[180:183]\n"
    "ds_read_b64_tr_b16 a[178:179], v30 offset:11264\n"
    "v_add_f32 v27, v27, v253\n"
    "v_add_f32 v29, v29, v255\n"
    "v_mfma_f32_16x16x32_bf16 v[184:187], a[188:191], v[32:35], v[184:187]\n"
    "ds_read_b64_tr_b16 a[180:181], v30 offset:3328\n"
    "v_cvt_pk_bf16_f32 v42, v22, v23\n"
    "v_cvt_pk_bf16_f32 v43, v24, v25\n"
    "v_mfma_f32_16x16x32_bf16 v[188:191], a[188:191], v[36:39], v[188:191]\n"
    "ds_read_b64_tr_b16 a[182:183], v30 offset:11520\n"
    "ds_read_b64_tr_b16 a[184:185], v30 offset:3584\n"
    "ds_read_b64_tr_b16 a[186:187], v30 offset:11776\n"
    "ds_read_b64_tr_b16 a[188:189], v30 offset:3840\n"
    "ds_read_b64_tr_b16 a[190:191], v30 offset:12032\n"
    "v_cvt_pk_bf16_f32 v46, v26, v27\n"
    "v_cvt_pk_bf16_f32 v47, v28, v29\n"
    "v_mfma_f32_16x16x32_bf16 v[64:67], a[128:131], v[40:43], v[64:67]\n"
    "s_waitcnt vmcnt(21)\n"
    "s_barrier\n"
    "v_lshlrev_b32 v22, 16, v224\n"
    "v_lshlrev_b32 v24, 16, v225\n"
    "v_mfma_f32_16x16x32_bf16 v[68:71], a[128:131], v[44:47], v[68:71]\n"
    "v_lshlrev_b32 v26, 16, v228\n"
    "v_lshlrev_b32 v28, 16, v229\n"
    "s_waitcnt lgkmcnt(10)\n"
    "v_mfma_f32_16x16x32_bf16 v[72:75], a[132:135], v[40:43], v[72:75]\n"
    "v_and_b32 v23, 0xffff0000, v224\n"
    "v_and_b32 v25, 0xffff0000, v225\n"
    "v_mfma_f32_16x16x32_bf16 v[76:79], a[132:135], v[44:47], v[76:79]\n"
    "s_add_u32 m0, s70, 0x8000\n"
    "s_nop 0\n"
    "buffer_load_dwordx4 v8, s[60:63], s68 offen offset:0 lds\n"
    "v_and_b32 v27, 0xffff0000, v228\n"
    "v_mfma_f32_16x16x32_bf16 v[80:83], a[136:139], v[40:43], v[80:83]\n"
    "ds_read_b64_tr_b16 a[128:129], v30 offset:16384\n"
    "v_and_b32 v29, 0xffff0000, v229\n"
    "v_mul_f32 v22, v22, v48\n"
    "v_mul_f32 v24, v24, v50\n"
    "v_mfma_f32_16x16x32_bf16 v[84:87], a[136:139], v[44:47], v[84:87]\n"
    "ds_read_b64_tr_b16 a[130:131], v30 offset:24576\n"
    "v_mul_f32 v26, v26, v48\n"
    "v_mul_f32 v28, v28, v50\n"
    "v_mfma_f32_16x16x32_bf16 v[88:91], a[140:143], v[40:43], v[88:91]\n"
    "ds_read_b64_tr_b16 a[132:133], v30 offset:16640\n"
    "ds_read_b128 v[240:243], v20 offset:640\n"
    "buffer_load_dwordx4 v9, s[60:63], s68 offen offset:1024 lds\n"
    "v_mul_f32 v23, v23, v49\n"
    "v_mul_f32 v25, v25, v51\n"
    "v_mfma_f32_16x16x32_bf16 v[92:95], a[140:143], v[44:47], v[92:95]\n"
    "ds_read_b64_tr_b16 a[134:135], v30 offset:24832\n"
    "v_mul_f32 v27, v27, v49\n"
    "v_mul_f32 v29, v29, v51\n"
    "v_cvt_pk_bf16_f32 v22, 0, v22\n"
    "v_mfma_f32_16x16x32_bf16 v[96:99], a[144:147], v[40:43], v[96:99]\n"
    "ds_read_b64_tr_b16 a[136:137], v30 offset:16896\n"
    "v_cvt_pk_bf16_f32 v24, 0, v24\n"
    "v_cvt_pk_bf16_f32 v26, 0, v26\n"
    "v_mfma_f32_16x16x32_bf16 v[100:103], a[144:147], v[44:47], v[100:103]\n"
    "ds_read_b64_tr_b16 a[138:139], v30 offset:25088\n"
    "buffer_load_dwordx4 v10, s[60:63], s68 offen offset:2048 lds\n"
    "v_cvt_pk_bf16_f32 v28, 0, v28\n"
    "v_cvt_pk_bf16_f32 v23, 0, v23\n"
    "v_mfma_f32_16x16x32_bf16 v[104:107], a[148:151], v[40:43], v[104:107]\n"
    "ds_read_b64_tr_b16 a[140:141], v30 offset:17152\n"
    "v_cvt_pk_bf16_f32 v25, 0, v25\n"
    "v_cvt_pk_bf16_f32 v27, 0, v27\n"
    "v_cvt_pk_bf16_f32 v29, 0, v29\n"
    "v_mfma_f32_16x16x32_bf16 v[108:111], a[148:151], v[44:47], v[108:111]\n"
    "ds_read_b64_tr_b16 a[142:143], v30 offset:25344\n"
    "v_add_f32 v22, v22, v56\n"
    "v_add_f32 v24, v24, v58\n"
    "v_mfma_f32_16x16x32_bf16 v[112:115], a[152:155], v[40:43], v[112:115]\n"
    "ds_read_b64_tr_b16 a[144:145], v30 offset:17408\n"
    "ds_read_b128 v[244:247], v20 offset:656\n"
    "buffer_load_dwordx4 v11, s[60:63], s68 offen offset:3072 lds\n"
    "v_add_f32 v26, v26, v56\n"
    "v_add_f32 v28, v28, v58\n"
    "v_mfma_f32_16x16x32_bf16 v[116:119], a[152:155], v[44:47], v[116:119]\n"
    "ds_read_b64_tr_b16 a[146:147], v30 offset:25600\n"
    "v_add_f32 v23, v23, v57\n"
    "v_add_f32 v25, v25, v59\n"
    "v_add_f32 v27, v27, v57\n"
    "v_mfma_f32_16x16x32_bf16 v[120:123], a[156:159], v[40:43], v[120:123]\n"
    "ds_read_b64_tr_b16 a[148:149], v30 offset:17664\n"
    "v_add_f32 v29, v29, v59\n"
    "v_cvt_pk_bf16_f32 v32, v22, v23\n"
    "v_mfma_f32_16x16x32_bf16 v[124:127], a[156:159], v[44:47], v[124:127]\n"
    "ds_read_b64_tr_b16 a[150:151], v30 offset:25856\n"
    "s_add_u32 s68, s68, 0x40000\n"
    "s_add_u32 s55, s69, 0x2400\n"
    "buffer_load_dwordx4 v[216:219], v21, s[16:19], s55 offen\n"
    "v_cvt_pk_bf16_f32 v33, v24, v25\n"
    "v_cvt_pk_bf16_f32 v36, v26, v27\n"
    "v_mfma_f32_16x16x32_bf16 v[128:131], a[160:163], v[40:43], v[128:131]\n"
    "ds_read_b64_tr_b16 a[152:153], v30 offset:17920\n"
    "v_cvt_pk_bf16_f32 v37, v28, v29\n"
    "v_lshlrev_b32 v22, 16, v226\n"
    "v_lshlrev_b32 v24, 16, v227\n"
    "v_mfma_f32_16x16x32_bf16 v[132:135], a[160:163], v[44:47], v[132:135]\n"
    "ds_read_b64_tr_b16 a[154:155], v30 offset:26112\n"
    "v_lshlrev_b32 v26, 16, v230\n"
    "v_lshlrev_b32 v28, 16, v231\n"
    "v_mfma_f32_16x16x32_bf16 v[136:139], a[164:167], v[40:43], v[136:139]\n"
    "ds_read_b64_tr_b16 a[156:157], v30 offset:18176\n"
    "ds_read_b128 v[248:251], v20 offset:2944\n"
    "s_add_u32 s55, s69, 0x6c00\n"
    "buffer_load_dwordx4 v[220:223], v21, s[16:19], s55 offen\n"
    "v_and_b32 v23, 0xffff0000, v226\n"
    "v_and_b32 v25, 0xffff0000, v227\n"
    "v_mfma_f32_16x16x32_bf16 v[140:143], a[164:167], v[44:47], v[140:143]\n"
    "ds_read_b64_tr_b16 a[158:159], v30 offset:26368\n"
    "v_and_b32 v27, 0xffff0000, v230\n"
    "v_and_b32 v29, 0xffff0000, v231\n"
    "v_mul_f32 v22, v22, v52\n"
    "v_mfma_f32_16x16x32_bf16 v[144:147], a[168:171], v[40:43], v[144:147]\n"
    "ds_read_b64_tr_b16 a[160:161], v30 offset:18432\n"
    "v_mul_f32 v24, v24, v54\n"
    "v_mul_f32 v26, v26, v52\n"
    "v_mfma_f32_16x16x32_bf16 v[148:151], a[168:171], v[44:47], v[148:151]\n"
    "ds_read_b64_tr_b16 a[162:163], v30 offset:26624\n"
    "v_mul_f32 v28, v28, v54\n"
    "v_mul_f32 v23, v23, v53\n"
    "s_waitcnt lgkmcnt(8)\n"
    "v_mfma_f32_16x16x32_bf16 v[152:155], a[172:175], v[40:43], v[152:155]\n"
    "ds_read_b64_tr_b16 a[164:165], v30 offset:18688\n"
    "v_mul_f32 v25, v25, v55\n"
    "v_mul_f32 v27, v27, v53\n"
    "v_mul_f32 v29, v29, v55\n"
    "v_mfma_f32_16x16x32_bf16 v[156:159], a[172:175], v[44:47], v[156:159]\n"
    "ds_read_b64_tr_b16 a[166:167], v30 offset:26880\n"
    "v_cvt_pk_bf16_f32 v22, 0, v22\n"
    "v_cvt_pk_bf16_f32 v24, 0, v24\n"
    "v_mfma_f32_16x16x32_bf16 v[160:163], a[176:179], v[40:43], v[160:163]\n"
    "ds_read_b64_tr_b16 a[168:169], v30 offset:18944\n"
    "ds_read_b128 v[252:255], v20 offset:2960\n"
    "v_cvt_pk_bf16_f32 v26, 0, v26\n"
    "v_cvt_pk_bf16_f32 v28, 0, v28\n"
    "v_mfma_f32_16x16x32_bf16 v[164:167], a[176:179], v[44:47], v[164:167]\n"
    "ds_read_b64_tr_b16 a[170:171], v30 offset:27136\n"
    "v_cvt_pk_bf16_f32 v23, 0, v23\n"
    "v_cvt_pk_bf16_f32 v25, 0, v25\n"
    "v_cvt_pk_bf16_f32 v27, 0, v27\n"
    "v_mfma_f32_16x16x32_bf16 v[168:171], a[180:183], v[40:43], v[168:171]\n"
    "ds_read_b64_tr_b16 a[172:173], v30 offset:19200\n"
    "v_cvt_pk_bf16_f32 v29, 0, v29\n"
    "v_add_f32 v22, v22, v60\n"
    "v_mfma_f32_16x16x32_bf16 v[172:175], a[180:183], v[44:47], v[172:175]\n"
    "ds_read_b64_tr_b16 a[174:175], v30 offset:27392\n"
    "v_add_f32 v24, v24, v62\n"
    "v_add_f32 v26, v26, v60\n"
    "v_mfma_f32_16x16x32_bf16 v[176:179], a[184:187], v[40:43], v[176:179]\n"
    "ds_read_b64_tr_b16 a[176:177], v30 offset:19456\n"
    "v_add_f32 v28, v28, v62\n"
    "v_add_f32 v23, v23, v61\n"
    "v_add_f32 v25, v25, v63\n"
    "v_mfma_f32_16x16x32_bf16 v[180:183], a[184:187], v[44:47], v[180:183]\n"
    "ds_read_b64_tr_b16 a[178:179], v30 offset:27648\n"
    "v_add_f32 v27, v27, v61\n"
    "v_add_f32 v29, v29, v63\n"
    "v_mfma_f32_16x16x32_bf16 v[184:187], a[188:191], v[40:43], v[184:187]\n"
    "ds_read_b64_tr_b16 a[180:181], v30 offset:19712\n"
    "v_cvt_pk_bf16_f32 v34, v22, v23\n"
    "v_cvt_pk_bf16_f32 v35, v24, v25\n"
    "v_mfma_f32_16x16x32_bf16 v[188:191], a[188:191], v[44:47], v[188:191]\n"
    "ds_read_b64_tr_b16 a[182:183], v30 offset:27904\n"
    "ds_read_b64_tr_b16 a[184:185], v30 offset:19968\n"
    "ds_read_b64_tr_b16 a[186:187], v30 offset:28160\n"
    "ds_read_b64_tr_b16 a[188:189], v30 offset:20224\n"
    "ds_read_b64_tr_b16 a[190:191], v30 offset:28416\n"
    "v_cvt_pk_bf16_f32 v38, v26, v27\n"
    "v_cvt_pk_bf16_f32 v39, v28, v29\n"
    "v_mfma_f32_16x16x32_bf16 v[64:67], a[128:131], v[32:35], v[64:67]\n"
    "s_waitcnt vmcnt(20)\n"
    "s_barrier\n"
    "v_lshlrev_b32 v22, 16, v232\n"
    "v_lshlrev_b32 v24, 16, v233\n"
    "v_mfma_f32_16x16x32_bf16 v[68:71], a[128:131], v[36:39], v[68:71]\n"
    "v_lshlrev_b32 v26, 16, v236\n"
    "v_lshlrev_b32 v28, 16, v237\n"
    "v_mfma_f32_16x16x32_bf16 v[72:75], a[132:135], v[32:35], v[72:75]\n"
    "v_and_b32 v23, 0xffff0000, v232\n"
    "v_and_b32 v25, 0xffff0000, v233\n"
    "v_mfma_f32_16x16x32_bf16 v[76:79], a[132:135], v[36:39], v[76:79]\n"
    "s_add_u32 m0, s70, 0xc000\n"
    "s_nop 0\n"
    "buffer_load_dwordx4 v8, s[60:63], s68 offen offset:0 lds\n"
    "v_and_b32 v27, 0xffff0000, v236\n"
    "v_mfma_f32_16x16x32_bf16 v[80:83], a[136:139], v[32:35], v[80:83]\n"
    "ds_read_b64_tr_b16 a[128:129], v30 offset:32768\n"
    "v_and_b32 v29, 0xffff0000, v237\n"
    "v_mul_f32 v22, v22, v240\n"
    "v_mul_f32 v24, v24, v242\n"
    "v_mfma_f32_16x16x32_bf16 v[84:87], a[136:139], v[36:39], v[84:87]\n"
    "ds_read_b64_tr_b16 a[130:131], v30 offset:40960\n"
    "v_mul_f32 v26, v26, v240\n"
    "v_mul_f32 v28, v28, v242\n"
    "v_mfma_f32_16x16x32_bf16 v[88:91], a[140:143], v[32:35], v[88:91]\n"
    "ds_read_b64_tr_b16 a[132:133], v30 offset:33024\n"
    "ds_read_b128 v[48:51], v20 offset:768\n"
    "buffer_load_dwordx4 v9, s[60:63], s68 offen offset:1024 lds\n"
    "v_mul_f32 v23, v23, v241\n"
    "v_mul_f32 v25, v25, v243\n"
    "v_mfma_f32_16x16x32_bf16 v[92:95], a[140:143], v[36:39], v[92:95]\n"
    "ds_read_b64_tr_b16 a[134:135], v30 offset:41216\n"
    "v_mul_f32 v27, v27, v241\n"
    "v_mul_f32 v29, v29, v243\n"
    "v_cvt_pk_bf16_f32 v22, 0, v22\n"
    "v_mfma_f32_16x16x32_bf16 v[96:99], a[144:147], v[32:35], v[96:99]\n"
    "ds_read_b64_tr_b16 a[136:137], v30 offset:33280\n"
    "v_cvt_pk_bf16_f32 v24, 0, v24\n"
    "v_cvt_pk_bf16_f32 v26, 0, v26\n"
    "v_mfma_f32_16x16x32_bf16 v[100:103], a[144:147], v[36:39], v[100:103]\n"
    "ds_read_b64_tr_b16 a[138:139], v30 offset:41472\n"
    "buffer_load_dwordx4 v10, s[60:63], s68 offen offset:2048 lds\n"
    "v_cvt_pk_bf16_f32 v28, 0, v28\n"
    "v_cvt_pk_bf16_f32 v23, 0, v23\n"
    "s_waitcnt lgkmcnt(7)\n"
    "v_mfma_f32_16x16x32_bf16 v[104:107], a[148:151], v[32:35], v[104:107]\n"
    "ds_read_b64_tr_b16 a[140:141], v30 offset:33536\n"
    "v_cvt_pk_bf16_f32 v25, 0, v25\n"
    "v_cvt_pk_bf16_f32 v27, 0, v27\n"
    "v_cvt_pk_bf16_f32 v29, 0, v29\n"
    "v_mfma_f32_16x16x32_bf16 v[108:111], a[148:151], v[36:39], v[108:111]\n"
    "ds_read_b64_tr_b16 a[142:143], v30 offset:41728\n"
    "v_add_f32 v22, v22, v248\n"
    "v_add_f32 v24, v24, v250\n"
    "v_mfma_f32_16x16x32_bf16 v[112:115], a[152:155], v[32:35], v[112:115]\n"
    "ds_read_b64_tr_b16 a[144:145], v30 offset:33792\n"
    "ds_read_b128 v[52:55], v20 offset:784\n"
    "buffer_load_dwordx4 v11, s[60:63], s68 offen offset:3072 lds\n"
    "v_add_f32 v26, v26, v248\n"
    "v_add_f32 v28, v28, v250\n"
    "v_mfma_f32_16x16x32_bf16 v[116:119], a[152:155], v[36:39], v[116:119]\n"
    "ds_read_b64_tr_b16 a[146:147], v30 offset:41984\n"
    "v_add_f32 v23, v23, v249\n"
    "v_add_f32 v25, v25, v251\n"
    "v_add_f32 v27, v27, v249\n"
    "v_mfma_f32_16x16x32_bf16 v[120:123], a[156:159], v[32:35], v[120:123]\n"
    "ds_read_b64_tr_b16 a[148:149], v30 offset:34048\n"
    "v_add_f32 v29, v29, v251\n"
    "v_cvt_pk_bf16_f32 v40, v22, v23\n"
    "v_mfma_f32_16x16x32_bf16 v[124:127], a[156:159], v[36:39], v[124:127]\n"
    "ds_read_b64_tr_b16 a[150:151], v30 offset:42240\n"
    "s_add_u32 s68, s68, 0x40000\n"
    "s_add_u32 s55, s69, 0x2800\n"
    "buffer_load_dwordx4 v[224:227], v21, s[16:19], s55 offen\n"
    "v_cvt_pk_bf16_f32 v41, v24, v25\n"
    "v_cvt_pk_bf16_f32 v44, v26, v27\n"
    "v_mfma_f32_16x16x32_bf16 v[128:131], a[160:163], v[32:35], v[128:131]\n"
    "ds_read_b64_tr_b16 a[152:153], v30 offset:34304\n"
    "v_cvt_pk_bf16_f32 v45, v28, v29\n"
    "v_lshlrev_b32 v22, 16, v234\n"
    "v_lshlrev_b32 v24, 16, v235\n"
    "v_mfma_f32_16x16x32_bf16 v[132:135], a[160:163], v[36:39], v[132:135]\n"
    "ds_read_b64_tr_b16 a[154:155], v30 offset:42496\n"
    "v_lshlrev_b32 v26, 16, v238\n"
    "v_lshlrev_b32 v28, 16, v239\n"
    "v_mfma_f32_16x16x32_bf16 v[136:139], a[164:167], v[32:35], v[136:139]\n"
    "ds_read_b64_tr_b16 a[156:157], v30 offset:34560\n"
    "ds_read_b128 v[56:59], v20 offset:3072\n"
    "s_add_u32 s55, s69, 0x7000\n"
    "buffer_load_dwordx4 v[228:231], v21, s[16:19], s55 offen\n"
    "v_and_b32 v23, 0xffff0000, v234\n"
    "v_and_b32 v25, 0xffff0000, v235\n"
    "v_mfma_f32_16x16x32_bf16 v[140:143], a[164:167], v[36:39], v[140:143]\n"
    "ds_read_b64_tr_b16 a[158:159], v30 offset:42752\n"
    "v_and_b32 v27, 0xffff0000, v238\n"
    "v_and_b32 v29, 0xffff0000, v239\n"
    "v_mul_f32 v22, v22, v244\n"
    "v_mfma_f32_16x16x32_bf16 v[144:147], a[168:171], v[32:35], v[144:147]\n"
    "ds_read_b64_tr_b16 a[160:161], v30 offset:34816\n"
    "v_mul_f32 v24, v24, v246\n"
    "v_mul_f32 v26, v26, v244\n"
    "v_mfma_f32_16x16x32_bf16 v[148:151], a[168:171], v[36:39], v[148:151]\n"
    "ds_read_b64_tr_b16 a[162:163], v30 offset:43008\n"
    "v_mul_f32 v28, v28, v246\n"
    "v_mul_f32 v23, v23, v245\n"
    "v_mfma_f32_16x16x32_bf16 v[152:155], a[172:175], v[32:35], v[152:155]\n"
    "ds_read_b64_tr_b16 a[164:165], v30 offset:35072\n"
    "v_mul_f32 v25, v25, v247\n"
    "v_mul_f32 v27, v27, v245\n"
    "v_mul_f32 v29, v29, v247\n"
    "v_mfma_f32_16x16x32_bf16 v[156:159], a[172:175], v[36:39], v[156:159]\n"
    "ds_read_b64_tr_b16 a[166:167], v30 offset:43264\n"
    "v_cvt_pk_bf16_f32 v22, 0, v22\n"
    "v_cvt_pk_bf16_f32 v24, 0, v24\n"
    "v_mfma_f32_16x16x32_bf16 v[160:163], a[176:179], v[32:35], v[160:163]\n"
    "ds_read_b64_tr_b16 a[168:169], v30 offset:35328\n"
    "ds_read_b128 v[60:63], v20 offset:3088\n"
    "v_cvt_pk_bf16_f32 v26, 0, v26\n"
    "v_cvt_pk_bf16_f32 v28, 0, v28\n"
    "v_mfma_f32_16x16x32_bf16 v[164:167], a[176:179], v[36:39], v[164:167]\n"
    "ds_read_b64_tr_b16 a[170:171], v30 offset:43520\n"
    "v_cvt_pk_bf16_f32 v23, 0, v23\n"
    "v_cvt_pk_bf16_f32 v25, 0, v25\n"
    "v_cvt_pk_bf16_f32 v27, 0, v27\n"
    "v_mfma_f32_16x16x32_bf16 v[168:171], a[180:183], v[32:35], v[168:171]\n"
    "ds_read_b64_tr_b16 a[172:173], v30 offset:35584\n"
    "v_cvt_pk_bf16_f32 v29, 0, v29\n"
    "v_add_f32 v22, v22, v252\n"
    "v_mfma_f32_16x16x32_bf16 v[172:175], a[180:183], v[36:39], v[172:175]\n"
    "ds_read_b64_tr_b16 a[174:175], v30 offset:43776\n"
    "v_add_f32 v24, v24, v254\n"
    "v_add_f32 v26, v26, v252\n"
    "v_mfma_f32_16x16x32_bf16 v[176:179], a[184:187], v[32:35], v[176:179]\n"
    "ds_read_b64_tr_b16 a[176:177], v30 offset:35840\n"
    "v_add_f32 v28, v28, v254\n"
    "v_add_f32 v23, v23, v253\n"
    "v_add_f32 v25, v25, v255\n"
    "v_mfma_f32_16x16x32_bf16 v[180:183], a[184:187], v[36:39], v[180:183]\n"
    "ds_read_b64_tr_b16 a[178:179], v30 offset:44032\n"
    "v_add_f32 v27, v27, v253\n"
    "v_add_f32 v29, v29, v255\n"
    "v_mfma_f32_16x16x32_bf16 v[184:187], a[188:191], v[32:35], v[184:187]\n"
    "ds_read_b64_tr_b16 a[180:181], v30 offset:36096\n"
    "v_cvt_pk_bf16_f32 v42, v22, v23\n"
    "v_cvt_pk_bf16_f32 v43, v24, v25\n"
    "v_mfma_f32_16x16x32_bf16 v[188:191], a[188:191], v[36:39], v[188:191]\n"
    "ds_read_b64_tr_b16 a[182:183], v30 offset:44288\n"
    "ds_read_b64_tr_b16 a[184:185], v30 offset:36352\n"
    "ds_read_b64_tr_b16 a[186:187], v30 offset:44544\n"
    "ds_read_b64_tr_b16 a[188:189], v30 offset:36608\n"
    "ds_read_b64_tr_b16 a[190:191], v30 offset:44800\n"
    "v_cvt_pk_bf16_f32 v46, v26, v27\n"
    "v_cvt_pk_bf16_f32 v47, v28, v29\n"
    "s_waitcnt lgkmcnt(11)\n"
    "v_mfma_f32_16x16x32_bf16 v[64:67], a[128:131], v[40:43], v[64:67]\n"
    "s_waitcnt vmcnt(20)\n"
    "s_barrier\n"
    "v_lshlrev_b32 v22, 16, v192\n"
    "v_lshlrev_b32 v24, 16, v193\n"
    "v_mfma_f32_16x16x32_bf16 v[68:71], a[128:131], v[44:47], v[68:71]\n"
    "v_lshlrev_b32 v26, 16, v196\n"
    "v_lshlrev_b32 v28, 16, v197\n"
    "v_mfma_f32_16x16x32_bf16 v[72:75], a[132:135], v[40:43], v[72:75]\n"
    "v_and_b32 v23, 0xffff0000, v192\n"
    "v_and_b32 v25, 0xffff0000, v193\n"
    "v_mfma_f32_16x16x32_bf16 v[76:79], a[132:135], v[44:47], v[76:79]\n"
    "s_add_u32 m0, s70, 0x10000\n"
    "s_nop 0\n"
    "buffer_load_dwordx4 v8, s[60:63], s68 offen offset:0 lds\n"
    "v_and_b32 v27, 0xffff0000, v196\n"
    "v_mfma_f32_16x16x32_bf16 v[80:83], a[136:139], v[40:43], v[80:83]\n"
    "ds_read_b64_tr_b16 a[128:129], v19 offset:0\n"
    "v_and_b32 v29, 0xffff0000, v197\n"
    "v_mul_f32 v22, v22, v48\n"
    "v_mul_f32 v24, v24, v50\n"
    "v_mfma_f32_16x16x32_bf16 v[84:87], a[136:139], v[44:47], v[84:87]\n"
    "ds_read_b64_tr_b16 a[130:131], v19 offset:8192\n"
    "v_mul_f32 v26, v26, v48\n"
    "v_mul_f32 v28, v28, v50\n"
    "v_mfma_f32_16x16x32_bf16 v[88:91], a[140:143], v[40:43], v[88:91]\n"
    "ds_read_b64_tr_b16 a[132:133], v19 offset:256\n"
    "ds_read_b128 v[240:243], v20 offset:896\n"
    "buffer_load_dwordx4 v9, s[60:63], s68 offen offset:1024 lds\n"
    "v_mul_f32 v23, v23, v49\n"
    "v_mul_f32 v25, v25, v51\n"
    "v_mfma_f32_16x16x32_bf16 v[92:95], a[140:143], v[44:47], v[92:95]\n"
    "ds_read_b64_tr_b16 a[134:135], v19 offset:8448\n"
    "v_mul_f32 v27, v27, v49\n"
    "v_mul_f32 v29, v29, v51\n"
    "v_cvt_pk_bf16_f32 v22, 0, v22\n"
    "v_mfma_f32_16x16x32_bf16 v[96:99], a[144:147], v[40:43], v[96:99]\n"
    "ds_read_b64_tr_b16 a[136:137], v19 offset:512\n"
    "v_cvt_pk_bf16_f32 v24, 0, v24\n"
    "v_cvt_pk_bf16_f32 v26, 0, v26\n"
    "v_mfma_f32_16x16x32_bf16 v[100:103], a[144:147], v[44:47], v[100:103]\n"
    "ds_read_b64_tr_b16 a[138:139], v19 offset:8704\n"
    "buffer_load_dwordx4 v10, s[60:63], s68 offen offset:2048 lds\n"
    "v_cvt_pk_bf16_f32 v28, 0, v28\n"
    "v_cvt_pk_bf16_f32 v23, 0, v23\n"
    "v_mfma_f32_16x16x32_bf16 v[104:107], a[148:151], v[40:43], v[104:107]\n"
    "ds_read_b64_tr_b16 a[140:141], v19 offset:768\n"
    "v_cvt_pk_bf16_f32 v25, 0, v25\n"
    "v_cvt_pk_bf16_f32 v27, 0, v27\n"
    "v_cvt_pk_bf16_f32 v29, 0, v29\n"
    "v_mfma_f32_16x16x32_bf16 v[108:111], a[148:151], v[44:47], v[108:111]\n"
    "ds_read_b64_tr_b16 a[142:143], v19 offset:8960\n"
    "v_add_f32 v22, v22, v56\n"
    "v_add_f32 v24, v24, v58\n"
    "v_mfma_f32_16x16x32_bf16 v[112:115], a[152:155], v[40:43], v[112:115]\n"
    "ds_read_b64_tr_b16 a[144:145], v19 offset:1024\n"
    "ds_read_b128 v[244:247], v20 offset:912\n"
    "buffer_load_dwordx4 v11, s[60:63], s68 offen offset:3072 lds\n"
    "v_add_f32 v26, v26, v56\n"
    "v_add_f32 v28, v28, v58\n"
    "v_mfma_f32_16x16x32_bf16 v[116:119], a[152:155], v[44:47], v[116:119]\n"
    "ds_read_b64_tr_b16 a[146:147], v19 offset:9216\n"
    "v_add_f32 v23, v23, v57\n"
    "v_add_f32 v25, v25, v59\n"
    "v_add_f32 v27, v27, v57\n"
    "v_mfma_f32_16x16x32_bf16 v[120:123], a[156:159], v[40:43], v[120:123]\n"
    "ds_read_b64_tr_b16 a[148:149], v19 offset:1280\n"
    "v_add_f32 v29, v29, v59\n"
    "v_cvt_pk_bf16_f32 v32, v22, v23\n"
    "v_mfma_f32_16x16x32_bf16 v[124:127], a[156:159], v[44:47], v[124:127]\n"
    "ds_read_b64_tr_b16 a[150:151], v19 offset:9472\n"
    "s_add_u32 s68, s68, 0x40000\n"
    "s_add_u32 s55, s69, 0x2c00\n"
    "buffer_load_dwordx4 v[232:235], v21, s[16:19], s55 offen\n"
    "v_cvt_pk_bf16_f32 v33, v24, v25\n"
    "v_cvt_pk_bf16_f32 v36, v26, v27\n"
    "v_mfma_f32_16x16x32_bf16 v[128:131], a[160:163], v[40:43], v[128:131]\n"
    "ds_read_b64_tr_b16 a[152:153], v19 offset:1536\n"
    "v_cvt_pk_bf16_f32 v37, v28, v29\n"
    "v_lshlrev_b32 v22, 16, v194\n"
    "v_lshlrev_b32 v24, 16, v195\n"
    "v_mfma_f32_16x16x32_bf16 v[132:135], a[160:163], v[44:47], v[132:135]\n"
    "ds_read_b64_tr_b16 a[154:155], v19 offset:9728\n"
    "v_lshlrev_b32 v26, 16, v198\n"
    "v_lshlrev_b32 v28, 16, v199\n"
    "v_mfma_f32_16x16x32_bf16 v[136:139], a[164:167], v[40:43], v[136:139]\n"
    "ds_read_b64_tr_b16 a[156:157], v19 offset:1792\n"
    "ds_read_b128 v[248:251], v20 offset:3200\n"
    "s_add_u32 s55, s69, 0x7400\n"
    "buffer_load_dwordx4 v[236:239], v21, s[16:19], s55 offen\n"
    "v_and_b32 v23, 0xffff0000, v194\n"
    "v_and_b32 v25, 0xffff0000, v195\n"
    "v_mfma_f32_16x16x32_bf16 v[140:143], a[164:167], v[44:47], v[140:143]\n"
    "ds_read_b64_tr_b16 a[158:159], v19 offset:9984\n"
    "v_and_b32 v27, 0xffff0000, v198\n"
    "v_and_b32 v29, 0xffff0000, v199\n"
    "v_mul_f32 v22, v22, v52\n"
    "s_waitcnt lgkmcnt(8)\n"
    "v_mfma_f32_16x16x32_bf16 v[144:147], a[168:171], v[40:43], v[144:147]\n"
    "ds_read_b64_tr_b16 a[160:161], v19 offset:2048\n"
    "v_mul_f32 v24, v24, v54\n"
    "v_mul_f32 v26, v26, v52\n"
    "v_mfma_f32_16x16x32_bf16 v[148:151], a[168:171], v[44:47], v[148:151]\n"
    "ds_read_b64_tr_b16 a[162:163], v19 offset:10240\n"
    "v_mul_f32 v28, v28, v54\n"
    "v_mul_f32 v23, v23, v53\n"
    "v_mfma_f32_16x16x32_bf16 v[152:155], a[172:175], v[40:43], v[152:155]\n"
    "ds_read_b64_tr_b16 a[164:165], v19 offset:2304\n"
    "v_mul_f32 v25, v25, v55\n"
    "v_mul_f32 v27, v27, v53\n"
    "v_mul_f32 v29, v29, v55\n"
    "v_mfma_f32_16x16x32_bf16 v[156:159], a[172:175], v[44:47], v[156:159]\n"
    "ds_read_b64_tr_b16 a[166:167], v19 offset:10496\n"
    "v_cvt_pk_bf16_f32 v22, 0, v22\n"
    "v_cvt_pk_bf16_f32 v24, 0, v24\n"
    "v_mfma_f32_16x16x32_bf16 v[160:163], a[176:179], v[40:43], v[160:163]\n"
    "ds_read_b64_tr_b16 a[168:169], v19 offset:2560\n"
    "ds_read_b128 v[252:255], v20 offset:3216\n"
    "v_cvt_pk_bf16_f32 v26, 0, v26\n"
    "v_cvt_pk_bf16_f32 v28, 0, v28\n"
    "v_mfma_f32_16x16x32_bf16 v[164:167], a[176:179], v[44:47], v[164:167]\n"
    "ds_read_b64_tr_b16 a[170:171], v19 offset:10752\n"
    "v_cvt_pk_bf16_f32 v23, 0, v23\n"
    "v_cvt_pk_bf16_f32 v25, 0, v25\n"
    "v_cvt_pk_bf16_f32 v27, 0, v27\n"
    "v_mfma_f32_16x16x32_bf16 v[168:171], a[180:183], v[40:43], v[168:171]\n"
    "ds_read_b64_tr_b16 a[172:173], v19 offset:2816\n"
    "v_cvt_pk_bf16_f32 v29, 0, v29\n"
    "v_add_f32 v22, v22, v60\n"
    "v_mfma_f32_16x16x32_bf16 v[172:175], a[180:183], v[44:47], v[172:175]\n"
    "ds_read_b64_tr_b16 a[174:175], v19 offset:11008\n"
    "v_add_f32 v24, v24, v62\n"
    "v_add_f32 v26, v26, v60\n"
    "v_mfma_f32_16x16x32_bf16 v[176:179], a[184:187], v[40:43], v[176:179]\n"
    "ds_read_b64_tr_b16 a[176:177], v19 offset:3072\n"
    "v_add_f32 v28, v28, v62\n"
    "v_add_f32 v23, v23, v61\n"
    "v_add_f32 v25, v25, v63\n"
    "v_mfma_f32_16x16x32_bf16 v[180:183], a[184:187], v[44:47], v[180:183]\n"
    "ds_read_b64_tr_b16 a[178:179], v19 offset:11264\n"
    "v_add_f32 v27, v27, v61\n"
    "v_add_f32 v29, v29, v63\n"
    "v_mfma_f32_16x16x32_bf16 v[184:187], a[188:191], v[40:43], v[184:187]\n"
    "ds_read_b64_tr_b16 a[180:181], v19 offset:3328\n"
    "v_cvt_pk_bf16_f32 v34, v22, v23\n"
    "v_cvt_pk_bf16_f32 v35, v24, v25\n"
    "v_mfma_f32_16x16x32_bf16 v[188:191], a[188:191], v[44:47], v[188:191]\n"
    "ds_read_b64_tr_b16 a[182:183], v19 offset:11520\n"
    "ds_read_b64_tr_b16 a[184:185], v19 offset:3584\n"
    "ds_read_b64_tr_b16 a[186:187], v19 offset:11776\n"
    "ds_read_b64_tr_b16 a[188:189], v19 offset:3840\n"
    "ds_read_b64_tr_b16 a[190:191], v19 offset:12032\n"
    "v_cvt_pk_bf16_f32 v38, v26, v27\n"
    "v_cvt_pk_bf16_f32 v39, v28, v29\n"
    "v_mfma_f32_16x16x32_bf16 v[64:67], a[128:131], v[32:35], v[64:67]\n"
    "s_waitcnt vmcnt(20)\n"
    "s_barrier\n"
    "v_lshlrev_b32 v22, 16, v200\n"
    "v_lshlrev_b32 v24, 16, v201\n"
    "v_mfma_f32_16x16x32_bf16 v[68:71], a[128:131], v[36:39], v[68:71]\n"
    "v_lshlrev_b32 v26, 16, v204\n"
    "v_lshlrev_b32 v28, 16, v205\n"
    "v_mfma_f32_16x16x32_bf16 v[72:75], a[132:135], v[32:35], v[72:75]\n"
    "v_and_b32 v23, 0xffff0000, v200\n"
    "v_and_b32 v25, 0xffff0000, v201\n"
    "v_mfma_f32_16x16x32_bf16 v[76:79], a[132:135], v[36:39], v[76:79]\n"
    "s_add_u32 m0, s70, 0x14000\n"
    "s_nop 0\n"
    "buffer_load_dwordx4 v8, s[60:63], s68 offen offset:0 lds\n"
    "v_and_b32 v27, 0xffff0000, v204\n"
    "v_mfma_f32_16x16x32_bf16 v[80:83], a[136:139], v[32:35], v[80:83]\n"
    "ds_read_b64_tr_b16 a[128:129], v19 offset:16384\n"
    "v_and_b32 v29, 0xffff0000, v205\n"
    "v_mul_f32 v22, v22, v240\n"
    "v_mul_f32 v24, v24, v242\n"
    "v_mfma_f32_16x16x32_bf16 v[84:87], a[136:139], v[36:39], v[84:87]\n"
    "ds_read_b64_tr_b16 a[130:131], v19 offset:24576\n"
    "v_mul_f32 v26, v26, v240\n"
    "v_mul_f32 v28, v28, v242\n"
    "v_mfma_f32_16x16x32_bf16 v[88:91], a[140:143], v[32:35], v[88:91]\n"
    "ds_read_b64_tr_b16 a[132:133], v19 offset:16640\n"
    "ds_read_b128 v[48:51], v20 offset:1024\n"
    "buffer_load_dwordx4 v9, s[60:63], s68 offen offset:1024 lds\n"
    "v_mul_f32 v23, v23, v241\n"
    "v_mul_f32 v25, v25, v243\n"
    "v_mfma_f32_16x16x32_bf16 v[92:95], a[140:143], v[36:39], v[92:95]\n"
    "ds_read_b64_tr_b16 a[134:135], v19 offset:24832\n"
    "v_mul_f32 v27, v27, v241\n"
    "v_mul_f32 v29, v29, v243\n"
    "v_cvt_pk_bf16_f32 v22, 0, v22\n"
    "s_waitcnt lgkmcnt(5)\n"
    "v_mfma_f32_16x16x32_bf16 v[96:99], a[144:147], v[32:35], v[96:99]\n"
    "ds_read_b64_tr_b16 a[136:137], v19 offset:16896\n"
    "v_cvt_pk_bf16_f32 v24, 0, v24\n"
    "v_cvt_pk_bf16_f32 v26, 0, v26\n"
    "v_mfma_f32_16x16x32_bf16 v[100:103], a[144:147], v[36:39], v[100:103]\n"
    "ds_read_b64_tr_b16 a[138:139], v19 offset:25088\n"
    "buffer_load_dwordx4 v10, s[60:63], s68 offen offset:2048 lds\n"
    "v_cvt_pk_bf16_f32 v28, 0, v28\n"
    "v_cvt_pk_bf16_f32 v23, 0, v23\n"
    "v_mfma_f32_16x16x32_bf16 v[104:107], a[148:151], v[32:35], v[104:107]\n"
    "ds_read_b64_tr_b16 a[140:141], v19 offset:17152\n"
    "v_cvt_pk_bf16_f32 v25, 0, v25\n"
    "v_cvt_pk_bf16_f32 v27, 0, v27\n"
    "v_cvt_pk_bf16_f32 v29, 0, v29\n"
    "v_mfma_f32_16x16x32_bf16 v[108:111], a[148:151], v[36:39], v[108:111]\n"
    "ds_read_b64_tr_b16 a[142:143], v19 offset:25344\n"
    "v_add_f32 v22, v22, v248\n"
    "v_add_f32 v24, v24, v250\n"
    "v_mfma_f32_16x16x32_bf16 v[112:115], a[152:155], v[32:35], v[112:115]\n"
    "ds_read_b64_tr_b16 a[144:145], v19 offset:17408\n"
    "ds_read_b128 v[52:55], v20 offset:1040\n"
    "buffer_load_dwordx4 v11, s[60:63], s68 offen offset:3072 lds\n"
    "v_add_f32 v26, v26, v248\n"
    "v_add_f32 v28, v28, v250\n"
    "v_mfma_f32_16x16x32_bf16 v[116:119], a[152:155], v[36:39], v[116:119]\n"
    "ds_read_b64_tr_b16 a[146:147], v19 offset:25600\n"
    "v_add_f32 v23, v23, v249\n"
    "v_add_f32 v25, v25, v251\n"
    "v_add_f32 v27, v27, v249\n"
    "v_mfma_f32_16x16x32_bf16 v[120:123], a[156:159], v[32:35], v[120:123]\n"
    "ds_read_b64_tr_b16 a[148:149], v19 offset:17664\n"
    "v_add_f32 v29, v29, v251\n"
    "v_cvt_pk_bf16_f32 v40, v22, v23\n"
    "v_mfma_f32_16x16x32_bf16 v[124:127], a[156:159], v[36:39], v[124:127]\n"
    "ds_read_b64_tr_b16 a[150:151], v19 offset:25856\n"
    "s_add_u32 s68, s68, 0x40000\n"
    "s_add_u32 s55, s69, 0x3000\n"
    "buffer_load_dwordx4 v[192:195], v21, s[16:19], s55 offen\n"
    "v_cvt_pk_bf16_f32 v41, v24, v25\n"
    "v_cvt_pk_bf16_f32 v44, v26, v27\n"
    "v_mfma_f32_16x16x32_bf16 v[128:131], a[160:163], v[32:35], v[128:131]\n"
    "ds_read_b64_tr_b16 a[152:153], v19 offset:17920\n"
    "v_cvt_pk_bf16_f32 v45, v28, v29\n"
    "v_lshlrev_b32 v22, 16, v202\n"
    "v_lshlrev_b32 v24, 16, v203\n"
    "v_mfma_f32_16x16x32_bf16 v[132:135], a[160:163], v[36:39], v[132:135]\n"
    "ds_read_b64_tr_b16 a[154:155], v19 offset:26112\n"
    "v_lshlrev_b32 v26, 16, v206\n"
    "v_lshlrev_b32 v28, 16, v207\n"
    "v_mfma_f32_16x16x32_bf16 v[136:139], a[164:167], v[32:35], v[136:139]\n"
    "ds_read_b64_tr_b16 a[156:157], v19 offset:18176\n"
    "ds_read_b128 v[56:59], v20 offset:3328\n"
    "s_add_u32 s55, s69, 0x7800\n"
    "buffer_load_dwordx4 v[196:199], v21, s[16:19], s55 offen\n"
    "v_and_b32 v23, 0xffff0000, v202\n"
    "v_and_b32 v25, 0xffff0000, v203\n"
    "v_mfma_f32_16x16x32_bf16 v[140:143], a[164:167], v[36:39], v[140:143]\n"
    "ds_read_b64_tr_b16 a[158:159], v19 offset:26368\n"
    "v_and_b32 v27, 0xffff0000, v206\n"
    "v_and_b32 v29, 0xffff0000, v207\n"
    "v_mul_f32 v22, v22, v244\n"
    "v_mfma_f32_16x16x32_bf16 v[144:147], a[168:171], v[32:35], v[144:147]\n"
    "ds_read_b64_tr_b16 a[160:161], v19 offset:18432\n"
    "v_mul_f32 v24, v24, v246\n"
    "v_mul_f32 v26, v26, v244\n"
    "v_mfma_f32_16x16x32_bf16 v[148:151], a[168:171], v[36:39], v[148:151]\n"
    "ds_read_b64_tr_b16 a[162:163], v19 offset:26624\n"
    "v_mul_f32 v28, v28, v246\n"
    "v_mul_f32 v23, v23, v245\n"
    "v_mfma_f32_16x16x32_bf16 v[152:155], a[172:175], v[32:35], v[152:155]\n"
    "ds_read_b64_tr_b16 a[164:165], v19 offset:18688\n"
    "v_mul_f32 v25, v25, v247\n"
    "v_mul_f32 v27, v27, v245\n"
    "v_mul_f32 v29, v29, v247\n"
    "v_mfma_f32_16x16x32_bf16 v[156:159], a[172:175], v[36:39], v[156:159]\n"
    "ds_read_b64_tr_b16 a[166:167], v19 offset:26880\n"
    "v_cvt_pk_bf16_f32 v22, 0, v22\n"
    "v_cvt_pk_bf16_f32 v24, 0, v24\n"
    "v_mfma_f32_16x16x32_bf16 v[160:163], a[176:179], v[32:35], v[160:163]\n"
    "ds_read_b64_tr_b16 a[168:169], v19 offset:18944\n"
    "ds_read_b128 v[60:63], v20 offset:3344\n"
    "v_cvt_pk_bf16_f32 v26, 0, v26\n"
    "v_cvt_pk_bf16_f32 v28, 0, v28\n"
    "v_mfma_f32_16x16x32_bf16 v[164:167], a[176:179], v[36:39], v[164:167]\n"
    "ds_read_b64_tr_b16 a[170:171], v19 offset:27136\n"
    "v_cvt_pk_bf16_f32 v23, 0, v23\n"
    "v_cvt_pk_bf16_f32 v25, 0, v25\n"
    "v_cvt_pk_bf16_f32 v27, 0, v27\n"
    "v_mfma_f32_16x16x32_bf16 v[168:171], a[180:183], v[32:35], v[168:171]\n"
    "ds_read_b64_tr_b16 a[172:173], v19 offset:19200\n"
    "v_cvt_pk_bf16_f32 v29, 0, v29\n"
    "v_add_f32 v22, v22, v252\n"
    "v_mfma_f32_16x16x32_bf16 v[172:175], a[180:183], v[36:39], v[172:175]\n"
    "ds_read_b64_tr_b16 a[174:175], v19 offset:27392\n"
    "v_add_f32 v24, v24, v254\n"
    "v_add_f32 v26, v26, v252\n"
    "v_mfma_f32_16x16x32_bf16 v[176:179], a[184:187], v[32:35], v[176:179]\n"
    "ds_read_b64_tr_b16 a[176:177], v19 offset:19456\n"
    "v_add_f32 v28, v28, v254\n"
    "v_add_f32 v23, v23, v253\n"
    "v_add_f32 v25, v25, v255\n"
    "v_mfma_f32_16x16x32_bf16 v[180:183], a[184:187], v[36:39], v[180:183]\n"
    "ds_read_b64_tr_b16 a[178:179], v19 offset:27648\n"
    "v_add_f32 v27, v27, v253\n"
    "v_add_f32 v29, v29, v255\n"
    "v_mfma_f32_16x16x32_bf16 v[184:187], a[188:191], v[32:35], v[184:187]\n"
    "ds_read_b64_tr_b16 a[180:181], v19 offset:19712\n"
    "v_cvt_pk_bf16_f32 v42, v22, v23\n"
    "v_cvt_pk_bf16_f32 v43, v24, v25\n"
    "v_mfma_f32_16x16x32_bf16 v[188:191], a[188:191], v[36:39], v[188:191]\n"
    "ds_read_b64_tr_b16 a[182:183], v19 offset:27904\n"
    "ds_read_b64_tr_b16 a[184:185], v19 offset:19968\n"
    "ds_read_b64_tr_b16 a[186:187], v19 offset:28160\n"
    "ds_read_b64_tr_b16 a[188:189], v19 offset:20224\n"
    "ds_read_b64_tr_b16 a[190:191], v19 offset:28416\n"
    "v_cvt_pk_bf16_f32 v46, v26, v27\n"
    "v_cvt_pk_bf16_f32 v47, v28, v29\n"
    "s_waitcnt lgkmcnt(11)\n"
    "v_mfma_f32_16x16x32_bf16 v[64:67], a[128:131], v[40:43], v[64:67]\n"
    "s_waitcnt vmcnt(20)\n"
    "s_barrier\n"
    "v_lshlrev_b32 v22, 16, v208\n"
    "v_lshlrev_b32 v24, 16, v209\n"
    "v_mfma_f32_16x16x32_bf16 v[68:71], a[128:131], v[44:47], v[68:71]\n"
    "v_lshlrev_b32 v26, 16, v212\n"
    "v_lshlrev_b32 v28, 16, v213\n"
    "v_mfma_f32_16x16x32_bf16 v[72:75], a[132:135], v[40:43], v[72:75]\n"
    "v_and_b32 v23, 0xffff0000, v208\n"
    "v_and_b32 v25, 0xffff0000, v209\n"
    "v_mfma_f32_16x16x32_bf16 v[76:79], a[132:135], v[44:47], v[76:79]\n"
    "s_add_u32 m0, s70, 0x0\n"
    "s_nop 0\n"
    "buffer_load_dwordx4 v8, s[60:63], s68 offen offset:0 lds\n"
    "v_and_b32 v27, 0xffff0000, v212\n"
    "v_mfma_f32_16x16x32_bf16 v[80:83], a[136:139], v[40:43], v[80:83]\n"
    "ds_read_b64_tr_b16 a[128:129], v19 offset:32768\n"
    "v_and_b32 v29, 0xffff0000, v213\n"
    "v_mul_f32 v22, v22, v48\n"
    "v_mul_f32 v24, v24, v50\n"
    "v_mfma_f32_16x16x32_bf16 v[84:87], a[136:139], v[44:47], v[84:87]\n"
    "ds_read_b64_tr_b16 a[130:131], v19 offset:40960\n"
    "v_mul_f32 v26, v26, v48\n"
    "v_mul_f32 v28, v28, v50\n"
    "v_mfma_f32_16x16x32_bf16 v[88:91], a[140:143], v[40:43], v[88:91]\n"
    "ds_read_b64_tr_b16 a[132:133], v19 offset:33024\n"
    "ds_read_b128 v[240:243], v20 offset:1152\n"
    "buffer_load_dwordx4 v9, s[60:63], s68 offen offset:1024 lds\n"
    "v_mul_f32 v23, v23, v49\n"
    "v_mul_f32 v25, v25, v51\n"
    "v_mfma_f32_16x16x32_bf16 v[92:95], a[140:143], v[44:47], v[92:95]\n"
    "ds_read_b64_tr_b16 a[134:135], v19 offset:41216\n"
    "v_mul_f32 v27, v27, v49\n"
    "v_mul_f32 v29, v29, v51\n"
    "v_cvt_pk_bf16_f32 v22, 0, v22\n"
    "v_mfma_f32_16x16x32_bf16 v[96:99], a[144:147], v[40:43], v[96:99]\n"
    "ds_read_b64_tr_b16 a[136:137], v19 offset:33280\n"
    "v_cvt_pk_bf16_f32 v24, 0, v24\n"
    "v_cvt_pk_bf16_f32 v26, 0, v26\n"
    "v_mfma_f32_16x16x32_bf16 v[100:103], a[144:147], v[44:47], v[100:103]\n"
    "ds_read_b64_tr_b16 a[138:139], v19 offset:41472\n"
    "buffer_load_dwordx4 v10, s[60:63], s68 offen offset:2048 lds\n"
    "v_cvt_pk_bf16_f32 v28, 0, v28\n"
    "v_cvt_pk_bf16_f32 v23, 0, v23\n"
    "v_mfma_f32_16x16x32_bf16 v[104:107], a[148:151], v[40:43], v[104:107]\n"
    "ds_read_b64_tr_b16 a[140:141], v19 offset:33536\n"
    "v_cvt_pk_bf16_f32 v25, 0, v25\n"
    "v_cvt_pk_bf16_f32 v27, 0, v27\n"
    "v_cvt_pk_bf16_f32 v29, 0, v29\n"
    "v_mfma_f32_16x16x32_bf16 v[108:111], a[148:151], v[44:47], v[108:111]\n"
    "ds_read_b64_tr_b16 a[142:143], v19 offset:41728\n"
    "v_add_f32 v22, v22, v56\n"
    "v_add_f32 v24, v24, v58\n"
    "v_mfma_f32_16x16x32_bf16 v[112:115], a[152:155], v[40:43], v[112:115]\n"
    "ds_read_b64_tr_b16 a[144:145], v19 offset:33792\n"
    "ds_read_b128 v[244:247], v20 offset:1168\n"
    "buffer_load_dwordx4 v11, s[60:63], s68 offen offset:3072 lds\n"
    "v_add_f32 v26, v26, v56\n"
    "v_add_f32 v28, v28, v58\n"
    "v_mfma_f32_16x16x32_bf16 v[116:119], a[152:155], v[44:47], v[116:119]\n"
    "ds_read_b64_tr_b16 a[146:147], v19 offset:41984\n"
    "v_add_f32 v23, v23, v57\n"
    "v_add_f32 v25, v25, v59\n"
    "v_add_f32 v27, v27, v57\n"
    "v_mfma_f32_16x16x32_bf16 v[120:123], a[156:159], v[40:43], v[120:123]\n"
    "ds_read_b64_tr_b16 a[148:149], v19 offset:34048\n"
    "v_add_f32 v29, v29, v59\n"
    "v_cvt_pk_bf16_f32 v32, v22, v23\n"
    "v_mfma_f32_16x16x32_bf16 v[124:127], a[156:159], v[44:47], v[124:127]\n"
    "ds_read_b64_tr_b16 a[150:151], v19 offset:42240\n"
    "s_add_u32 s68, s68, 0x40000\n"
    "s_add_u32 s55, s69, 0x3400\n"
    "buffer_load_dwordx4 v[200:203], v21, s[16:19], s55 offen\n"
    "v_cvt_pk_bf16_f32 v33, v24, v25\n"
    "v_cvt_pk_bf16_f32 v36, v26, v27\n"
    "v_mfma_f32_16x16x32_bf16 v[128:131], a[160:163], v[40:43], v[128:131]\n"
    "ds_read_b64_tr_b16 a[152:153], v19 offset:34304\n"
    "v_cvt_pk_bf16_f32 v37, v28, v29\n"
    "v_lshlrev_b32 v22, 16, v210\n"
    "v_lshlrev_b32 v24, 16, v211\n"
    "v_mfma_f32_16x16x32_bf16 v[132:135], a[160:163], v[44:47], v[132:135]\n"
    "ds_read_b64_tr_b16 a[154:155], v19 offset:42496\n"
    "v_lshlrev_b32 v26, 16, v214\n"
    "v_lshlrev_b32 v28, 16, v215\n"
    "v_mfma_f32_16x16x32_bf16 v[136:139], a[164:167], v[40:43], v[136:139]\n"
    "ds_read_b64_tr_b16 a[156:157], v19 offset:34560\n"
    "ds_read_b128 v[248:251], v20 offset:3456\n"
    "s_add_u32 s55, s69, 0x7c00\n"
    "buffer_load_dwordx4 v[204:207], v21, s[16:19], s55 offen\n"
    "v_and_b32 v23, 0xffff0000, v210\n"
    "v_and_b32 v25, 0xffff0000, v211\n"
    "v_mfma_f32_16x16x32_bf16 v[140:143], a[164:167], v[44:47], v[140:143]\n"
    "ds_read_b64_tr_b16 a[158:159], v19 offset:42752\n"
    "v_and_b32 v27, 0xffff0000, v214\n"
    "v_and_b32 v29, 0xffff0000, v215\n"
    "v_mul_f32 v22, v22, v52\n"
    "s_waitcnt lgkmcnt(8)\n"
    "v_mfma_f32_16x16x32_bf16 v[144:147], a[168:171], v[40:43], v[144:147]\n"
    "ds_read_b64_tr_b16 a[160:161], v19 offset:34816\n"
    "v_mul_f32 v24, v24, v54\n"
    "v_mul_f32 v26, v26, v52\n"
    "v_mfma_f32_16x16x32_bf16 v[148:151], a[168:171], v[44:47], v[148:151]\n"
    "ds_read_b64_tr_b16 a[162:163], v19 offset:43008\n"
    "v_mul_f32 v28, v28, v54\n"
    "v_mul_f32 v23, v23, v53\n"
    "v_mfma_f32_16x16x32_bf16 v[152:155], a[172:175], v[40:43], v[152:155]\n"
    "ds_read_b64_tr_b16 a[164:165], v19 offset:35072\n"
    "v_mul_f32 v25, v25, v55\n"
    "v_mul_f32 v27, v27, v53\n"
    "v_mul_f32 v29, v29, v55\n"
    "v_mfma_f32_16x16x32_bf16 v[156:159], a[172:175], v[44:47], v[156:159]\n"
    "ds_read_b64_tr_b16 a[166:167], v19 offset:43264\n"
    "v_cvt_pk_bf16_f32 v22, 0, v22\n"
    "v_cvt_pk_bf16_f32 v24, 0, v24\n"
    "v_mfma_f32_16x16x32_bf16 v[160:163], a[176:179], v[40:43], v[160:163]\n"
    "ds_read_b64_tr_b16 a[168:169], v19 offset:35328\n"
    "ds_read_b128 v[252:255], v20 offset:3472\n"
    "v_cvt_pk_bf16_f32 v26, 0, v26\n"
    "v_cvt_pk_bf16_f32 v28, 0, v28\n"
    "v_mfma_f32_16x16x32_bf16 v[164:167], a[176:179], v[44:47], v[164:167]\n"
    "ds_read_b64_tr_b16 a[170:171], v19 offset:43520\n"
    "v_cvt_pk_bf16_f32 v23, 0, v23\n"
    "v_cvt_pk_bf16_f32 v25, 0, v25\n"
    "v_cvt_pk_bf16_f32 v27, 0, v27\n"
    "v_mfma_f32_16x16x32_bf16 v[168:171], a[180:183], v[40:43], v[168:171]\n"
    "ds_read_b64_tr_b16 a[172:173], v19 offset:35584\n"
    "v_cvt_pk_bf16_f32 v29, 0, v29\n"
    "v_add_f32 v22, v22, v60\n"
    "v_mfma_f32_16x16x32_bf16 v[172:175], a[180:183], v[44:47], v[172:175]\n"
    "ds_read_b64_tr_b16 a[174:175], v19 offset:43776\n"
    "v_add_f32 v24, v24, v62\n"
    "v_add_f32 v26, v26, v60\n"
    "v_mfma_f32_16x16x32_bf16 v[176:179], a[184:187], v[40:43], v[176:179]\n"
    "ds_read_b64_tr_b16 a[176:177], v19 offset:35840\n"
    "v_add_f32 v28, v28, v62\n"
    "v_add_f32 v23, v23, v61\n"
    "v_add_f32 v25, v25, v63\n"
    "v_mfma_f32_16x16x32_bf16 v[180:183], a[184:187], v[44:47], v[180:183]\n"
    "ds_read_b64_tr_b16 a[178:179], v19 offset:44032\n"
    "v_add_f32 v27, v27, v61\n"
    "v_add_f32 v29, v29, v63\n"
    "v_mfma_f32_16x16x32_bf16 v[184:187], a[188:191], v[40:43], v[184:187]\n"
    "ds_read_b64_tr_b16 a[180:181], v19 offset:36096\n"
    "v_cvt_pk_bf16_f32 v34, v22, v23\n"
    "v_cvt_pk_bf16_f32 v35, v24, v25\n"
    "v_mfma_f32_16x16x32_bf16 v[188:191], a[188:191], v[44:47], v[188:191]\n"
    "ds_read_b64_tr_b16 a[182:183], v19 offset:44288\n"
    "ds_read_b64_tr_b16 a[184:185], v19 offset:36352\n"
    "ds_read_b64_tr_b16 a[186:187], v19 offset:44544\n"
    "ds_read_b64_tr_b16 a[188:189], v19 offset:36608\n"
    "ds_read_b64_tr_b16 a[190:191], v19 offset:44800\n"
    "v_cvt_pk_bf16_f32 v38, v26, v27\n"
    "v_cvt_pk_bf16_f32 v39, v28, v29\n"
    "v_mfma_f32_16x16x32_bf16 v[64:67], a[128:131], v[32:35], v[64:67]\n"
    "s_waitcnt vmcnt(20)\n"
    "s_barrier\n"
    "v_lshlrev_b32 v22, 16, v216\n"
    "v_lshlrev_b32 v24, 16, v217\n"
    "v_mfma_f32_16x16x32_bf16 v[68:71], a[128:131], v[36:39], v[68:71]\n"
    "v_lshlrev_b32 v26, 16, v220\n"
    "v_lshlrev_b32 v28, 16, v221\n"
    "v_mfma_f32_16x16x32_bf16 v[72:75], a[132:135], v[32:35], v[72:75]\n"
    "v_and_b32 v23, 0xffff0000, v216\n"
    "v_and_b32 v25, 0xffff0000, v217\n"
    "v_mfma_f32_16x16x32_bf16 v[76:79], a[132:135], v[36:39], v[76:79]\n"
    "s_add_u32 m0, s70, 0x4000\n"
    "s_nop 0\n"
    "buffer_load_dwordx4 v8, s[60:63], s68 offen offset:0 lds\n"
    "v_and_b32 v27, 0xffff0000, v220\n"
    "v_mfma_f32_16x16x32_bf16 v[80:83], a[136:139], v[32:35], v[80:83]\n"
    "ds_read_b64_tr_b16 a[128:129], v30 offset:0\n"
    "v_and_b32 v29, 0xffff0000, v221\n"
    "v_mul_f32 v22, v22, v240\n"
    "v_mul_f32 v24, v24, v242\n"
    "v_mfma_f32_16x16x32_bf16 v[84:87], a[136:139], v[36:39], v[84:87]\n"
    "ds_read_b64_tr_b16 a[130:131], v30 offset:8192\n"
    "v_mul_f32 v26, v26, v240\n"
    "v_mul_f32 v28, v28, v242\n"
    "v_mfma_f32_16x16x32_bf16 v[88:91], a[140:143], v[32:35], v[88:91]\n"
    "ds_read_b64_tr_b16 a[132:133], v30 offset:256\n"
    "ds_read_b128 v[48:51], v20 offset:1280\n"
    "buffer_load_dwordx4 v9, s[60:63], s68 offen offset:1024 lds\n"
    "v_mul_f32 v23, v23, v241\n"
    "v_mul_f32 v25, v25, v243\n"
    "v_mfma_f32_16x16x32_bf16 v[92:95], a[140:143], v[36:39], v[92:95]\n"
    "ds_read_b64_tr_b16 a[134:135], v30 offset:8448\n"
    "v_mul_f32 v27, v27, v241\n"
    "v_mul_f32 v29, v29, v243\n"
    "v_cvt_pk_bf16_f32 v22, 0, v22\n"
    "s_waitcnt lgkmcnt(5)\n"
    "v_mfma_f32_16x16x32_bf16 v[96:99], a[144:147], v[32:35], v[96:99]\n"
    "ds_read_b64_tr_b16 a[136:137], v30 offset:512\n"
    "v_cvt_pk_bf16_f32 v24, 0, v24\n"
    "v_cvt_pk_bf16_f32 v26, 0, v26\n"
    "v_mfma_f32_16x16x32_bf16 v[100:103], a[144:147], v[36:39], v[100:103]\n"
    "ds_read_b64_tr_b16 a[138:139], v30 offset:8704\n"
    "buffer_load_dwordx4 v10, s[60:63], s68 offen offset:2048 lds\n"
    "v_cvt_pk_bf16_f32 v28, 0, v28\n"
    "v_cvt_pk_bf16_f32 v23, 0, v23\n"
    "v_mfma_f32_16x16x32_bf16 v[104:107], a[148:151], v[32:35], v[104:107]\n"
    "ds_read_b64_tr_b16 a[140:141], v30 offset:768\n"
    "v_cvt_pk_bf16_f32 v25, 0, v25\n"
    "v_cvt_pk_bf16_f32 v27, 0, v27\n"
    "v_cvt_pk_bf16_f32 v29, 0, v29\n"
    "v_mfma_f32_16x16x32_bf16 v[108:111], a[148:151], v[36:39], v[108:111]\n"
    "ds_read_b64_tr_b16 a[142:143], v30 offset:8960\n"
    "v_add_f32 v22, v22, v248\n"
    "v_add_f32 v24, v24, v250\n"
    "v_mfma_f32_16x16x32_bf16 v[112:115], a[152:155], v[32:35], v[112:115]\n"
    "ds_read_b64_tr_b16 a[144:145], v30 offset:1024\n"
    "ds_read_b128 v[52:55], v20 offset:1296\n"
    "buffer_load_dwordx4 v11, s[60:63], s68 offen offset:3072 lds\n"
    "v_add_f32 v26, v26, v248\n"
    "v_add_f32 v28, v28, v250\n"
    "v_mfma_f32_16x16x32_bf16 v[116:119], a[152:155], v[36:39], v[116:119]\n"
    "ds_read_b64_tr_b16 a[146:147], v30 offset:9216\n"
    "v_add_f32 v23, v23, v249\n"
    "v_add_f32 v25, v25, v251\n"
    "v_add_f32 v27, v27, v249\n"
    "v_mfma_f32_16x16x32_bf16 v[120:123], a[156:159], v[32:35], v[120:123]\n"
    "ds_read_b64_tr_b16 a[148:149], v30 offset:1280\n"
    "v_add_f32 v29, v29, v251\n"
    "v_cvt_pk_bf16_f32 v40, v22, v23\n"
    "v_mfma_f32_16x16x32_bf16 v[124:127], a[156:159], v[36:39], v[124:127]\n"
    "ds_read_b64_tr_b16 a[150:151], v30 offset:9472\n"
    "s_add_u32 s68, s68, 0x40000\n"
    "s_add_u32 s55, s69, 0x3800\n"
    "buffer_load_dwordx4 v[208:211], v21, s[16:19], s55 offen\n"
    "v_cvt_pk_bf16_f32 v41, v24, v25\n"
    "v_cvt_pk_bf16_f32 v44, v26, v27\n"
    "v_mfma_f32_16x16x32_bf16 v[128:131], a[160:163], v[32:35], v[128:131]\n"
    "ds_read_b64_tr_b16 a[152:153], v30 offset:1536\n"
    "v_cvt_pk_bf16_f32 v45, v28, v29\n"
    "v_lshlrev_b32 v22, 16, v218\n"
    "v_lshlrev_b32 v24, 16, v219\n"
    "v_mfma_f32_16x16x32_bf16 v[132:135], a[160:163], v[36:39], v[132:135]\n"
    "ds_read_b64_tr_b16 a[154:155], v30 offset:9728\n"
    "v_lshlrev_b32 v26, 16, v222\n"
    "v_lshlrev_b32 v28, 16, v223\n"
    "v_mfma_f32_16x16x32_bf16 v[136:139], a[164:167], v[32:35], v[136:139]\n"
    "ds_read_b64_tr_b16 a[156:157], v30 offset:1792\n"
    "ds_read_b128 v[56:59], v20 offset:3584\n"
    "s_add_u32 s55, s69, 0x8000\n"
    "buffer_load_dwordx4 v[212:215], v21, s[16:19], s55 offen\n"
    "v_and_b32 v23, 0xffff0000, v218\n"
    "v_and_b32 v25, 0xffff0000, v219\n"
    "v_mfma_f32_16x16x32_bf16 v[140:143], a[164:167], v[36:39], v[140:143]\n"
    "ds_read_b64_tr_b16 a[158:159], v30 offset:9984\n"
    "v_and_b32 v27, 0xffff0000, v222\n"
    "v_and_b32 v29, 0xffff0000, v223\n"
    "v_mul_f32 v22, v22, v244\n"
    "v_mfma_f32_16x16x32_bf16 v[144:147], a[168:171], v[32:35], v[144:147]\n"
    "ds_read_b64_tr_b16 a[160:161], v30 offset:2048\n"
    "v_mul_f32 v24, v24, v246\n"
    "v_mul_f32 v26, v26, v244\n"
    "v_mfma_f32_16x16x32_bf16 v[148:151], a[168:171], v[36:39], v[148:151]\n"
    "ds_read_b64_tr_b16 a[162:163], v30 offset:10240\n"
    "v_mul_f32 v28, v28, v246\n"
    "v_mul_f32 v23, v23, v245\n"
    "v_mfma_f32_16x16x32_bf16 v[152:155], a[172:175], v[32:35], v[152:155]\n"
    "ds_read_b64_tr_b16 a[164:165], v30 offset:2304\n"
    "v_mul_f32 v25, v25, v247\n"
    "v_mul_f32 v27, v27, v245\n"
    "v_mul_f32 v29, v29, v247\n"
    "v_mfma_f32_16x16x32_bf16 v[156:159], a[172:175], v[36:39], v[156:159]\n"
    "ds_read_b64_tr_b16 a[166:167], v30 offset:10496\n"
    "v_cvt_pk_bf16_f32 v22, 0, v22\n"
    "v_cvt_pk_bf16_f32 v24, 0, v24\n"
    "v_mfma_f32_16x16x32_bf16 v[160:163], a[176:179], v[32:35], v[160:163]\n"
    "ds_read_b64_tr_b16 a[168:169], v30 offset:2560\n"
    "ds_read_b128 v[60:63], v20 offset:3600\n"
    "v_cvt_pk_bf16_f32 v26, 0, v26\n"
    "v_cvt_pk_bf16_f32 v28, 0, v28\n"
    "v_mfma_f32_16x16x32_bf16 v[164:167], a[176:179], v[36:39], v[164:167]\n"
    "ds_read_b64_tr_b16 a[170:171], v30 offset:10752\n"
    "v_cvt_pk_bf16_f32 v23, 0, v23\n"
    "v_cvt_pk_bf16_f32 v25, 0, v25\n"
    "v_cvt_pk_bf16_f32 v27, 0, v27\n"
    "v_mfma_f32_16x16x32_bf16 v[168:171], a[180:183], v[32:35], v[168:171]\n"
    "ds_read_b64_tr_b16 a[172:173], v30 offset:2816\n"
    "v_cvt_pk_bf16_f32 v29, 0, v29\n"
    "v_add_f32 v22, v22, v252\n"
    "v_mfma_f32_16x16x32_bf16 v[172:175], a[180:183], v[36:39], v[172:175]\n"
    "ds_read_b64_tr_b16 a[174:175], v30 offset:11008\n"
    "v_add_f32 v24, v24, v254\n"
    "v_add_f32 v26, v26, v252\n"
    "v_mfma_f32_16x16x32_bf16 v[176:179], a[184:187], v[32:35], v[176:179]\n"
    "ds_read_b64_tr_b16 a[176:177], v30 offset:3072\n"
    "v_add_f32 v28, v28, v254\n"
    "v_add_f32 v23, v23, v253\n"
    "v_add_f32 v25, v25, v255\n"
    "v_mfma_f32_16x16x32_bf16 v[180:183], a[184:187], v[36:39], v[180:183]\n"
    "ds_read_b64_tr_b16 a[178:179], v30 offset:11264\n"
    "v_add_f32 v27, v27, v253\n"
    "v_add_f32 v29, v29, v255\n"
    "v_mfma_f32_16x16x32_bf16 v[184:187], a[188:191], v[32:35], v[184:187]\n"
    "ds_read_b64_tr_b16 a[180:181], v30 offset:3328\n"
    "v_cvt_pk_bf16_f32 v42, v22, v23\n"
    "v_cvt_pk_bf16_f32 v43, v24, v25\n"
    "v_mfma_f32_16x16x32_bf16 v[188:191], a[188:191], v[36:39], v[188:191]\n"
    "ds_read_b64_tr_b16 a[182:183], v30 offset:11520\n"
    "ds_read_b64_tr_b16 a[184:185], v30 offset:3584\n"
    "ds_read_b64_tr_b16 a[186:187], v30 offset:11776\n"
    "ds_read_b64_tr_b16 a[188:189], v30 offset:3840\n"
    "ds_read_b64_tr_b16 a[190:191], v30 offset:12032\n"
    "v_cvt_pk_bf16_f32 v46, v26, v27\n"
    "v_cvt_pk_bf16_f32 v47, v28, v29\n"
    "s_waitcnt lgkmcnt(11)\n"
    "v_mfma_f32_16x16x32_bf16 v[64:67], a[128:131], v[40:43], v[64:67]\n"
    "s_waitcnt vmcnt(20)\n"
    "s_barrier\n"
    "v_lshlrev_b32 v22, 16, v224\n"
    "v_lshlrev_b32 v24, 16, v225\n"
    "v_mfma_f32_16x16x32_bf16 v[68:71], a[128:131], v[44:47], v[68:71]\n"
    "v_lshlrev_b32 v26, 16, v228\n"
    "v_lshlrev_b32 v28, 16, v229\n"
    "v_mfma_f32_16x16x32_bf16 v[72:75], a[132:135], v[40:43], v[72:75]\n"
    "v_and_b32 v23, 0xffff0000, v224\n"
    "v_and_b32 v25, 0xffff0000, v225\n"
    "v_mfma_f32_16x16x32_bf16 v[76:79], a[132:135], v[44:47], v[76:79]\n"
    "s_add_u32 m0, s70, 0x8000\n"
    "s_nop 0\n"
    "buffer_load_dwordx4 v8, s[60:63], s68 offen offset:0 lds\n"
    "v_and_b32 v27, 0xffff0000, v228\n"
    "v_mfma_f32_16x16x32_bf16 v[80:83], a[136:139], v[40:43], v[80:83]\n"
    "ds_read_b64_tr_b16 a[128:129], v30 offset:16384\n"
    "v_and_b32 v29, 0xffff0000, v229\n"
    "v_mul_f32 v22, v22, v48\n"
    "v_mul_f32 v24, v24, v50\n"
    "v_mfma_f32_16x16x32_bf16 v[84:87], a[136:139], v[44:47], v[84:87]\n"
    "ds_read_b64_tr_b16 a[130:131], v30 offset:24576\n"
    "buffer_load_dwordx4 v9, s[60:63], s68 offen offset:1024 lds\n"
    "v_mul_f32 v26, v26, v48\n"
    "v_mul_f32 v28, v28, v50\n"
    "v_mfma_f32_16x16x32_bf16 v[88:91], a[140:143], v[40:43], v[88:91]\n"
    "ds_read_b64_tr_b16 a[132:133], v30 offset:16640\n"
    "ds_read_b128 v[240:243], v20 offset:1408\n"
    "v_mul_f32 v23, v23, v49\n"
    "v_mul_f32 v25, v25, v51\n"
    "v_mfma_f32_16x16x32_bf16 v[92:95], a[140:143], v[44:47], v[92:95]\n"
    "ds_read_b64_tr_b16 a[134:135], v30 offset:24832\n"
    "buffer_load_dwordx4 v10, s[60:63], s68 offen offset:2048 lds\n"
    "v_mul_f32 v27, v27, v49\n"
    "v_mul_f32 v29, v29, v51\n"
    "v_cvt_pk_bf16_f32 v22, 0, v22\n"
    "v_mfma_f32_16x16x32_bf16 v[96:99], a[144:147], v[40:43], v[96:99]\n"
    "ds_read_b64_tr_b16 a[136:137], v30 offset:16896\n"
    "v_cvt_pk_bf16_f32 v24, 0, v24\n"
    "v_cvt_pk_bf16_f32 v26, 0, v26\n"
    "v_mfma_f32_16x16x32_bf16 v[100:103], a[144:147], v[44:47], v[100:103]\n"
    "ds_read_b64_tr_b16 a[138:139], v30 offset:25088\n"
    "buffer_load_dwordx4 v11, s[60:63], s68 offen offset:3072 lds\n"
    "v_cvt_pk_bf16_f32 v28, 0, v28\n"
    "v_cvt_pk_bf16_f32 v23, 0, v23\n"
    "v_mfma_f32_16x16x32_bf16 v[104:107], a[148:151], v[40:43], v[104:107]\n"
    "ds_read_b64_tr_b16 a[140:141], v30 offset:17152\n"
    "v_cvt_pk_bf16_f32 v25, 0, v25\n"
    "v_cvt_pk_bf16_f32 v27, 0, v27\n"
    "v_cvt_pk_bf16_f32 v29, 0, v29\n"
    "v_mfma_f32_16x16x32_bf16 v[108:111], a[148:151], v[44:47], v[108:111]\n"
    "ds_read_b64_tr_b16 a[142:143], v30 offset:25344\n"
    "s_add_u32 s68, s68, 0x40000\n"
    "s_add_u32 s55, s69, 0x3c00\n"
    "buffer_load_dwordx4 v[216:219], v21, s[16:19], s55 offen\n"
    "v_add_f32 v22, v22, v56\n"
    "v_add_f32 v24, v24, v58\n"
    "v_mfma_f32_16x16x32_bf16 v[112:115], a[152:155], v[40:43], v[112:115]\n"
    "ds_read_b64_tr_b16 a[144:145], v30 offset:17408\n"
    "ds_read_b128 v[244:247], v20 offset:1424\n"
    "v_add_f32 v26, v26, v56\n"
    "v_add_f32 v28, v28, v58\n"
    "v_mfma_f32_16x16x32_bf16 v[116:119], a[152:155], v[44:47], v[116:119]\n"
    "ds_read_b64_tr_b16 a[146:147], v30 offset:25600\n"
    "s_add_u32 s55, s69, 0x8400\n"
    "buffer_load_dwordx4 v[220:223], v21, s[16:19], s55 offen\n"
    "v_add_f32 v23, v23, v57\n"
    "v_add_f32 v25, v25, v59\n"
    "v_add_f32 v27, v27, v57\n"
    "v_mfma_f32_16x16x32_bf16 v[120:123], a[156:159], v[40:43], v[120:123]\n"
    "ds_read_b64_tr_b16 a[148:149], v30 offset:17664\n"
    "v_add_f32 v29, v29, v59\n"
    "v_cvt_pk_bf16_f32 v32, v22, v23\n"
    "v_mfma_f32_16x16x32_bf16 v[124:127], a[156:159], v[44:47], v[124:127]\n"
    "ds_read_b64_tr_b16 a[150:151], v30 offset:25856\n"
    "s_add_u32 m0, s22, 0x0\n"
    "s_add_u32 s20, s26, 0x0\n"
    "buffer_load_dwordx4 v13, s[8:11], s20 offen offset:0 lds\n"
    "v_cvt_pk_bf16_f32 v33, v24, v25\n"
    "v_cvt_pk_bf16_f32 v36, v26, v27\n"
    "v_mfma_f32_16x16x32_bf16 v[128:131], a[160:163], v[40:43], v[128:131]\n"
    "ds_read_b64_tr_b16 a[152:153], v30 offset:17920\n"
    "v_cvt_pk_bf16_f32 v37, v28, v29\n"
    "v_lshlrev_b32 v22, 16, v226\n"
    "v_lshlrev_b32 v24, 16, v227\n"
    "v_mfma_f32_16x16x32_bf16 v[132:135], a[160:163], v[44:47], v[132:135]\n"
    "ds_read_b64_tr_b16 a[154:155], v30 offset:26112\n"
    "buffer_load_dwordx4 v14, s[8:11], s20 offen offset:1024 lds\n"
    "v_lshlrev_b32 v26, 16, v230\n"
    "v_lshlrev_b32 v28, 16, v231\n"
    "v_mfma_f32_16x16x32_bf16 v[136:139], a[164:167], v[40:43], v[136:139]\n"
    "ds_read_b64_tr_b16 a[156:157], v30 offset:18176\n"
    "ds_read_b128 v[248:251], v20 offset:3712\n"
    "v_and_b32 v23, 0xffff0000, v226\n"
    "v_and_b32 v25, 0xffff0000, v227\n"
    "v_mfma_f32_16x16x32_bf16 v[140:143], a[164:167], v[44:47], v[140:143]\n"
    "ds_read_b64_tr_b16 a[158:159], v30 offset:26368\n"
    "buffer_load_dwordx4 v15, s[8:11], s20 offen offset:2048 lds\n"
    "v_and_b32 v27, 0xffff0000, v230\n"
    "v_and_b32 v29, 0xffff0000, v231\n"
    "v_mul_f32 v22, v22, v52\n"
    "s_waitcnt lgkmcnt(7)\n"
    "v_mfma_f32_16x16x32_bf16 v[144:147], a[168:171], v[40:43], v[144:147]\n"
    "ds_read_b64_tr_b16 a[160:161], v30 offset:18432\n"
    "v_mul_f32 v24, v24, v54\n"
    "v_mul_f32 v26, v26, v52\n"
    "v_mfma_f32_16x16x32_bf16 v[148:151], a[168:171], v[44:47], v[148:151]\n"
    "ds_read_b64_tr_b16 a[162:163], v30 offset:26624\n"
    "buffer_load_dwordx4 v16, s[8:11], s20 offen offset:3072 lds\n"
    "v_mul_f32 v28, v28, v54\n"
    "v_mul_f32 v23, v23, v53\n"
    "v_mfma_f32_16x16x32_bf16 v[152:155], a[172:175], v[40:43], v[152:155]\n"
    "ds_read_b64_tr_b16 a[164:165], v30 offset:18688\n"
    "v_mul_f32 v25, v25, v55\n"
    "v_mul_f32 v27, v27, v53\n"
    "v_mul_f32 v29, v29, v55\n"
    "v_mfma_f32_16x16x32_bf16 v[156:159], a[172:175], v[44:47], v[156:159]\n"
    "ds_read_b64_tr_b16 a[166:167], v30 offset:26880\n"
    "v_cvt_pk_bf16_f32 v22, 0, v22\n"
    "v_cvt_pk_bf16_f32 v24, 0, v24\n"
    "v_mfma_f32_16x16x32_bf16 v[160:163], a[176:179], v[40:43], v[160:163]\n"
    "ds_read_b64_tr_b16 a[168:169], v30 offset:18944\n"
    "ds_read_b128 v[252:255], v20 offset:3728\n"
    "v_cvt_pk_bf16_f32 v26, 0, v26\n"
    "v_cvt_pk_bf16_f32 v28, 0, v28\n"
    "v_mfma_f32_16x16x32_bf16 v[164:167], a[176:179], v[44:47], v[164:167]\n"
    "ds_read_b64_tr_b16 a[170:171], v30 offset:27136\n"
    "v_cvt_pk_bf16_f32 v23, 0, v23\n"
    "v_cvt_pk_bf16_f32 v25, 0, v25\n"
    "v_cvt_pk_bf16_f32 v27, 0, v27\n"
    "v_mfma_f32_16x16x32_bf16 v[168:171], a[180:183], v[40:43], v[168:171]\n"
    "ds_read_b64_tr_b16 a[172:173], v30 offset:19200\n"
    "v_cvt_pk_bf16_f32 v29, 0, v29\n"
    "v_add_f32 v22, v22, v60\n"
    "v_mfma_f32_16x16x32_bf16 v[172:175], a[180:183], v[44:47], v[172:175]\n"
    "ds_read_b64_tr_b16 a[174:175], v30 offset:27392\n"
    "v_add_f32 v24, v24, v62\n"
    "v_add_f32 v26, v26, v60\n"
    "v_mfma_f32_16x16x32_bf16 v[176:179], a[184:187], v[40:43], v[176:179]\n"
    "ds_read_b64_tr_b16 a[176:177], v30 offset:19456\n"
    "v_add_f32 v28, v28, v62\n"
    "v_add_f32 v23, v23, v61\n"
    "v_add_f32 v25, v25, v63\n"
    "v_mfma_f32_16x16x32_bf16 v[180:183], a[184:187], v[44:47], v[180:183]\n"
    "ds_read_b64_tr_b16 a[178:179], v30 offset:27648\n"
    "v_add_f32 v27, v27, v61\n"
    "v_add_f32 v29, v29, v63\n"
    "v_mfma_f32_16x16x32_bf16 v[184:187], a[188:191], v[40:43], v[184:187]\n"
    "ds_read_b64_tr_b16 a[180:181], v30 offset:19712\n"
    "v_cvt_pk_bf16_f32 v34, v22, v23\n"
    "v_cvt_pk_bf16_f32 v35, v24, v25\n"
    "v_mfma_f32_16x16x32_bf16 v[188:191], a[188:191], v[44:47], v[188:191]\n"
    "ds_read_b64_tr_b16 a[182:183], v30 offset:27904\n"
    "ds_read_b64_tr_b16 a[184:185], v30 offset:19968\n"
    "ds_read_b64_tr_b16 a[186:187], v30 offset:28160\n"
    "ds_read_b64_tr_b16 a[188:189], v30 offset:20224\n"
    "ds_read_b64_tr_b16 a[190:191], v30 offset:28416\n"
    "v_cvt_pk_bf16_f32 v38, v26, v27\n"
    "v_cvt_pk_bf16_f32 v39, v28, v29\n"
    "v_mfma_f32_16x16x32_bf16 v[64:67], a[128:131], v[32:35], v[64:67]\n"
    "s_waitcnt vmcnt(24)\n"
    "s_barrier\n"
    "v_lshlrev_b32 v22, 16, v232\n"
    "v_lshlrev_b32 v24, 16, v233\n"
    "v_mfma_f32_16x16x32_bf16 v[68:71], a[128:131], v[36:39], v[68:71]\n"
    "v_lshlrev_b32 v26, 16, v236\n"
    "v_lshlrev_b32 v28, 16, v237\n"
    "v_mfma_f32_16x16x32_bf16 v[72:75], a[132:135], v[32:35], v[72:75]\n"
    "v_and_b32 v23, 0xffff0000, v232\n"
    "v_and_b32 v25, 0xffff0000, v233\n"
    "v_mfma_f32_16x16x32_bf16 v[76:79], a[132:135], v[36:39], v[76:79]\n"
    "s_add_u32 m0, s70, 0xc000\n"
    "s_nop 0\n"
    "buffer_load_dwordx4 v8, s[60:63], s68 offen offset:0 lds\n"
    "v_and_b32 v27, 0xffff0000, v236\n"
    "v_mfma_f32_16x16x32_bf16 v[80:83], a[136:139], v[32:35], v[80:83]\n"
    "ds_read_b64_tr_b16 a[128:129], v30 offset:32768\n"
    "v_and_b32 v29, 0xffff0000, v237\n"
    "v_mul_f32 v22, v22, v240\n"
    "v_mul_f32 v24, v24, v242\n"
    "v_mfma_f32_16x16x32_bf16 v[84:87], a[136:139], v[36:39], v[84:87]\n"
    "ds_read_b64_tr_b16 a[130:131], v30 offset:40960\n"
    "v_mul_f32 v26, v26, v240\n"
    "v_mul_f32 v28, v28, v242\n"
    "v_mfma_f32_16x16x32_bf16 v[88:91], a[140:143], v[32:35], v[88:91]\n"
    "ds_read_b64_tr_b16 a[132:133], v30 offset:33024\n"
    "ds_read_b128 v[48:51], v20 offset:1536\n"
    "buffer_load_dwordx4 v9, s[60:63], s68 offen offset:1024 lds\n"
    "v_mul_f32 v23, v23, v241\n"
    "v_mul_f32 v25, v25, v243\n"
    "v_mfma_f32_16x16x32_bf16 v[92:95], a[140:143], v[36:39], v[92:95]\n"
    "ds_read_b64_tr_b16 a[134:135], v30 offset:41216\n"
    "v_mul_f32 v27, v27, v241\n"
    "v_mul_f32 v29, v29, v243\n"
    "v_cvt_pk_bf16_f32 v22, 0, v22\n"
    "v_mfma_f32_16x16x32_bf16 v[96:99], a[144:147], v[32:35], v[96:99]\n"
    "ds_read_b64_tr_b16 a[136:137], v30 offset:33280\n"
    "v_cvt_pk_bf16_f32 v24, 0, v24\n"
    "v_cvt_pk_bf16_f32 v26, 0, v26\n"
    "v_mfma_f32_16x16x32_bf16 v[100:103], a[144:147], v[36:39], v[100:103]\n"
    "ds_read_b64_tr_b16 a[138:139], v30 offset:41472\n"
    "buffer_load_dwordx4 v10, s[60:63], s68 offen offset:2048 lds\n"
    "v_cvt_pk_bf16_f32 v28, 0, v28\n"
    "v_cvt_pk_bf16_f32 v23, 0, v23\n"
    "s_waitcnt lgkmcnt(7)\n"
    "v_mfma_f32_16x16x32_bf16 v[104:107], a[148:151], v[32:35], v[104:107]\n"
    "ds_read_b64_tr_b16 a[140:141], v30 offset:33536\n"
    "v_cvt_pk_bf16_f32 v25, 0, v25\n"
    "v_cvt_pk_bf16_f32 v27, 0, v27\n"
    "v_cvt_pk_bf16_f32 v29, 0, v29\n"
    "v_mfma_f32_16x16x32_bf16 v[108:111], a[148:151], v[36:39], v[108:111]\n"
    "ds_read_b64_tr_b16 a[142:143], v30 offset:41728\n"
    "v_add_f32 v22, v22, v248\n"
    "v_add_f32 v24, v24, v250\n"
    "v_mfma_f32_16x16x32_bf16 v[112:115], a[152:155], v[32:35], v[112:115]\n"
    "ds_read_b64_tr_b16 a[144:145], v30 offset:33792\n"
    "ds_read_b128 v[52:55], v20 offset:1552\n"
    "buffer_load_dwordx4 v11, s[60:63], s68 offen offset:3072 lds\n"
    "v_add_f32 v26, v26, v248\n"
    "v_add_f32 v28, v28, v250\n"
    "v_mfma_f32_16x16x32_bf16 v[116:119], a[152:155], v[36:39], v[116:119]\n"
    "ds_read_b64_tr_b16 a[146:147], v30 offset:41984\n"
    "v_add_f32 v23, v23, v249\n"
    "v_add_f32 v25, v25, v251\n"
    "v_add_f32 v27, v27, v249\n"
    "v_mfma_f32_16x16x32_bf16 v[120:123], a[156:159], v[32:35], v[120:123]\n"
    "ds_read_b64_tr_b16 a[148:149], v30 offset:34048\n"
    "v_add_f32 v29, v29, v251\n"
    "v_cvt_pk_bf16_f32 v40, v22, v23\n"
    "v_mfma_f32_16x16x32_bf16 v[124:127], a[156:159], v[36:39], v[124:127]\n"
    "ds_read_b64_tr_b16 a[150:151], v30 offset:42240\n"
    "s_add_u32 s68, s68, 0x40000\n"
    "s_add_u32 s55, s69, 0x4000\n"
    "buffer_load_dwordx4 v[224:227], v21, s[16:19], s55 offen\n"
    "v_cvt_pk_bf16_f32 v41, v24, v25\n"
    "v_cvt_pk_bf16_f32 v44, v26, v27\n"
    "v_mfma_f32_16x16x32_bf16 v[128:131], a[160:163], v[32:35], v[128:131]\n"
    "ds_read_b64_tr_b16 a[152:153], v30 offset:34304\n"
    "v_cvt_pk_bf16_f32 v45, v28, v29\n"
    "v_lshlrev_b32 v22, 16, v234\n"
    "v_lshlrev_b32 v24, 16, v235\n"
    "v_mfma_f32_16x16x32_bf16 v[132:135], a[160:163], v[36:39], v[132:135]\n"
    "ds_read_b64_tr_b16 a[154:155], v30 offset:42496\n"
    "v_lshlrev_b32 v26, 16, v238\n"
    "v_lshlrev_b32 v28, 16, v239\n"
    "v_mfma_f32_16x16x32_bf16 v[136:139], a[164:167], v[32:35], v[136:139]\n"
    "ds_read_b64_tr_b16 a[156:157], v30 offset:34560\n"
    "ds_read_b128 v[56:59], v20 offset:3840\n"
    "s_add_u32 s55, s69, 0x8800\n"
    "buffer_load_dwordx4 v[228:231], v21, s[16:19], s55 offen\n"
    "v_and_b32 v23, 0xffff0000, v234\n"
    "v_and_b32 v25, 0xffff0000, v235\n"
    "v_mfma_f32_16x16x32_bf16 v[140:143], a[164:167], v[36:39], v[140:143]\n"
    "ds_read_b64_tr_b16 a[158:159], v30 offset:42752\n"
    "v_and_b32 v27, 0xffff0000, v238\n"
    "v_and_b32 v29, 0xffff0000, v239\n"
    "v_mul_f32 v22, v22, v244\n"
    "v_mfma_f32_16x16x32_bf16 v[144:147], a[168:171], v[32:35], v[144:147]\n"
    "ds_read_b64_tr_b16 a[160:161], v30 offset:34816\n"
    "v_mul_f32 v24, v24, v246\n"
    "v_mul_f32 v26, v26, v244\n"
    "v_mfma_f32_16x16x32_bf16 v[148:151], a[168:171], v[36:39], v[148:151]\n"
    "ds_read_b64_tr_b16 a[162:163], v30 offset:43008\n"
    "v_mul_f32 v28, v28, v246\n"
    "v_mul_f32 v23, v23, v245\n"
    "v_mfma_f32_16x16x32_bf16 v[152:155], a[172:175], v[32:35], v[152:155]\n"
    "ds_read_b64_tr_b16 a[164:165], v30 offset:35072\n"
    "v_mul_f32 v25, v25, v247\n"
    "v_mul_f32 v27, v27, v245\n"
    "v_mul_f32 v29, v29, v247\n"
    "v_mfma_f32_16x16x32_bf16 v[156:159], a[172:175], v[36:39], v[156:159]\n"
    "ds_read_b64_tr_b16 a[166:167], v30 offset:43264\n"
    "v_cvt_pk_bf16_f32 v22, 0, v22\n"
    "v_cvt_pk_bf16_f32 v24, 0, v24\n"
    "v_mfma_f32_16x16x32_bf16 v[160:163], a[176:179], v[32:35], v[160:163]\n"
    "ds_read_b64_tr_b16 a[168:169], v30 offset:35328\n"
    "ds_read_b128 v[60:63], v20 offset:3856\n"
    "v_cvt_pk_bf16_f32 v26, 0, v26\n"
    "v_cvt_pk_bf16_f32 v28, 0, v28\n"
    "v_mfma_f32_16x16x32_bf16 v[164:167], a[176:179], v[36:39], v[164:167]\n"
    "ds_read_b64_tr_b16 a[170:171], v30 offset:43520\n"
    "v_cvt_pk_bf16_f32 v23, 0, v23\n"
    "v_cvt_pk_bf16_f32 v25, 0, v25\n"
    "v_cvt_pk_bf16_f32 v27, 0, v27\n"
    "v_mfma_f32_16x16x32_bf16 v[168:171], a[180:183], v[32:35], v[168:171]\n"
    "ds_read_b64_tr_b16 a[172:173], v30 offset:35584\n"
    "v_cvt_pk_bf16_f32 v29, 0, v29\n"
    "v_add_f32 v22, v22, v252\n"
    "v_mfma_f32_16x16x32_bf16 v[172:175], a[180:183], v[36:39], v[172:175]\n"
    "ds_read_b64_tr_b16 a[174:175], v30 offset:43776\n"
    "v_add_f32 v24, v24, v254\n"
    "v_add_f32 v26, v26, v252\n"
    "v_mfma_f32_16x16x32_bf16 v[176:179], a[184:187], v[32:35], v[176:179]\n"
    "ds_read_b64_tr_b16 a[176:177], v30 offset:35840\n"
    "v_add_f32 v28, v28, v254\n"
    "v_add_f32 v23, v23, v253\n"
    "v_add_f32 v25, v25, v255\n"
    "v_mfma_f32_16x16x32_bf16 v[180:183], a[184:187], v[36:39], v[180:183]\n"
    "ds_read_b64_tr_b16 a[178:179], v30 offset:44032\n"
    "v_add_f32 v27, v27, v253\n"
    "v_add_f32 v29, v29, v255\n"
    "v_mfma_f32_16x16x32_bf16 v[184:187], a[188:191], v[32:35], v[184:187]\n"
    "ds_read_b64_tr_b16 a[180:181], v30 offset:36096\n"
    "v_cvt_pk_bf16_f32 v42, v22, v23\n"
    "v_cvt_pk_bf16_f32 v43, v24, v25\n"
    "v_mfma_f32_16x16x32_bf16 v[188:191], a[188:191], v[36:39], v[188:191]\n"
    "ds_read_b64_tr_b16 a[182:183], v30 offset:44288\n"
    "ds_read_b64_tr_b16 a[184:185], v30 offset:36352\n"
    "ds_read_b64_tr_b16 a[186:187], v30 offset:44544\n"
    "ds_read_b64_tr_b16 a[188:189], v30 offset:36608\n"
    "ds_read_b64_tr_b16 a[190:191], v30 offset:44800\n"
    "v_cvt_pk_bf16_f32 v46, v26, v27\n"
    "v_cvt_pk_bf16_f32 v47, v28, v29\n"
    "s_waitcnt lgkmcnt(11)\n"
    "v_mfma_f32_16x16x32_bf16 v[64:67], a[128:131], v[40:43], v[64:67]\n"
    "s_waitcnt vmcnt(24)\n"
    "s_barrier\n"
    "v_lshlrev_b32 v22, 16, v192\n"
    "v_lshlrev_b32 v24, 16, v193\n"
    "v_mfma_f32_16x16x32_bf16 v[68:71], a[128:131], v[44:47], v[68:71]\n"
    "v_lshlrev_b32 v26, 16, v196\n"
    "v_lshlrev_b32 v28, 16, v197\n"
    "v_mfma_f32_16x16x32_bf16 v[72:75], a[132:135], v[40:43], v[72:75]\n"
    "v_and_b32 v23, 0xffff0000, v192\n"
    "v_and_b32 v25, 0xffff0000, v193\n"
    "v_mfma_f32_16x16x32_bf16 v[76:79], a[132:135], v[44:47], v[76:79]\n"
    "s_add_u32 m0, s70, 0x10000\n"
    "s_nop 0\n"
    "buffer_load_dwordx4 v8, s[60:63], s68 offen offset:0 lds\n"
    "v_and_b32 v27, 0xffff0000, v196\n"
    "v_mfma_f32_16x16x32_bf16 v[80:83], a[136:139], v[40:43], v[80:83]\n"
    "ds_read_b64_tr_b16 a[128:129], v19 offset:0\n"
    "v_and_b32 v29, 0xffff0000, v197\n"
    "v_mul_f32 v22, v22, v48\n"
    "v_mul_f32 v24, v24, v50\n"
    "v_mfma_f32_16x16x32_bf16 v[84:87], a[136:139], v[44:47], v[84:87]\n"
    "ds_read_b64_tr_b16 a[130:131], v19 offset:8192\n"
    "buffer_load_dwordx4 v9, s[60:63], s68 offen offset:1024 lds\n"
    "v_mul_f32 v26, v26, v48\n"
    "v_mul_f32 v28, v28, v50\n"
    "v_mfma_f32_16x16x32_bf16 v[88:91], a[140:143], v[40:43], v[88:91]\n"
    "ds_read_b64_tr_b16 a[132:133], v19 offset:256\n"
    "ds_read_b128 v[240:243], v20 offset:1664\n"
    "v_mul_f32 v23, v23, v49\n"
    "v_mul_f32 v25, v25, v51\n"
    "v_mfma_f32_16x16x32_bf16 v[92:95], a[140:143], v[44:47], v[92:95]\n"
    "ds_read_b64_tr_b16 a[134:135], v19 offset:8448\n"
    "buffer_load_dwordx4 v10, s[60:63], s68 offen offset:2048 lds\n"
    "v_mul_f32 v27, v27, v49\n"
    "v_mul_f32 v29, v29, v51\n"
    "v_cvt_pk_bf16_f32 v22, 0, v22\n"
    "v_mfma_f32_16x16x32_bf16 v[96:99], a[144:147], v[40:43], v[96:99]\n"
    "ds_read_b64_tr_b16 a[136:137], v19 offset:512\n"
    "v_cvt_pk_bf16_f32 v24, 0, v24\n"
    "v_cvt_pk_bf16_f32 v26, 0, v26\n"
    "v_mfma_f32_16x16x32_bf16 v[100:103], a[144:147], v[44:47], v[100:103]\n"
    "ds_read_b64_tr_b16 a[138:139], v19 offset:8704\n"
    "buffer_load_dwordx4 v11, s[60:63], s68 offen offset:3072 lds\n"
    "v_cvt_pk_bf16_f32 v28, 0, v28\n"
    "v_cvt_pk_bf16_f32 v23, 0, v23\n"
    "v_mfma_f32_16x16x32_bf16 v[104:107], a[148:151], v[40:43], v[104:107]\n"
    "ds_read_b64_tr_b16 a[140:141], v19 offset:768\n"
    "v_cvt_pk_bf16_f32 v25, 0, v25\n"
    "v_cvt_pk_bf16_f32 v27, 0, v27\n"
    "v_cvt_pk_bf16_f32 v29, 0, v29\n"
    "v_mfma_f32_16x16x32_bf16 v[108:111], a[148:151], v[44:47], v[108:111]\n"
    "ds_read_b64_tr_b16 a[142:143], v19 offset:8960\n"
    "s_add_u32 s68, s68, 0x40000\n"
    "s_add_u32 s55, s69, 0x4400\n"
    "buffer_load_dwordx4 v[232:235], v21, s[16:19], s55 offen\n"
    "v_add_f32 v22, v22, v56\n"
    "v_add_f32 v24, v24, v58\n"
    "v_mfma_f32_16x16x32_bf16 v[112:115], a[152:155], v[40:43], v[112:115]\n"
    "ds_read_b64_tr_b16 a[144:145], v19 offset:1024\n"
    "ds_read_b128 v[244:247], v20 offset:1680\n"
    "v_add_f32 v26, v26, v56\n"
    "v_add_f32 v28, v28, v58\n"
    "v_mfma_f32_16x16x32_bf16 v[116:119], a[152:155], v[44:47], v[116:119]\n"
    "ds_read_b64_tr_b16 a[146:147], v19 offset:9216\n"
    "s_add_u32 s55, s69, 0x8c00\n"
    "buffer_load_dwordx4 v[236:239], v21, s[16:19], s55 offen\n"
    "v_add_f32 v23, v23, v57\n"
    "v_add_f32 v25, v25, v59\n"
    "v_add_f32 v27, v27, v57\n"
    "v_mfma_f32_16x16x32_bf16 v[120:123], a[156:159], v[40:43], v[120:123]\n"
    "ds_read_b64_tr_b16 a[148:149], v19 offset:1280\n"
    "v_add_f32 v29, v29, v59\n"
    "v_cvt_pk_bf16_f32 v32, v22, v23\n"
    "v_mfma_f32_16x16x32_bf16 v[124:127], a[156:159], v[44:47], v[124:127]\n"
    "ds_read_b64_tr_b16 a[150:151], v19 offset:9472\n"
    "s_add_u32 m0, s22, 0x4000\n"
    "s_add_u32 s20, s26, 0x4000\n"
    "buffer_load_dwordx4 v13, s[8:11], s20 offen offset:0 lds\n"
    "v_cvt_pk_bf16_f32 v33, v24, v25\n"
    "v_cvt_pk_bf16_f32 v36, v26, v27\n"
    "v_mfma_f32_16x16x32_bf16 v[128:131], a[160:163], v[40:43], v[128:131]\n"
    "ds_read_b64_tr_b16 a[152:153], v19 offset:1536\n"
    "v_cvt_pk_bf16_f32 v37, v28, v29\n"
    "v_lshlrev_b32 v22, 16, v194\n"
    "v_lshlrev_b32 v24, 16, v195\n"
    "v_mfma_f32_16x16x32_bf16 v[132:135], a[160:163], v[44:47], v[132:135]\n"
    "ds_read_b64_tr_b16 a[154:155], v19 offset:9728\n"
    "buffer_load_dwordx4 v14, s[8:11], s20 offen offset:1024 lds\n"
    "v_lshlrev_b32 v26, 16, v198\n"
    "v_lshlrev_b32 v28, 16, v199\n"
    "v_mfma_f32_16x16x32_bf16 v[136:139], a[164:167], v[40:43], v[136:139]\n"
    "ds_read_b64_tr_b16 a[156:157], v19 offset:1792\n"
    "ds_read_b128 v[248:251], v20 offset:3968\n"
    "v_and_b32 v23, 0xffff0000, v194\n"
    "v_and_b32 v25, 0xffff0000, v195\n"
    "v_mfma_f32_16x16x32_bf16 v[140:143], a[164:167], v[44:47], v[140:143]\n"
    "ds_read_b64_tr_b16 a[158:159], v19 offset:9984\n"
    "buffer_load_dwordx4 v15, s[8:11], s20 offen offset:2048 lds\n"
    "v_and_b32 v27, 0xffff0000, v198\n"
    "v_and_b32 v29, 0xffff0000, v199\n"
    "v_mul_f32 v22, v22, v52\n"
    "s_waitcnt lgkmcnt(7)\n"
    "v_mfma_f32_16x16x32_bf16 v[144:147], a[168:171], v[40:43], v[144:147]\n"
    "ds_read_b64_tr_b16 a[160:161], v19 offset:2048\n"
    "v_mul_f32 v24, v24, v54\n"
    "v_mul_f32 v26, v26, v52\n"
    "v_mfma_f32_16x16x32_bf16 v[148:151], a[168:171], v[44:47], v[148:151]\n"
    "ds_read_b64_tr_b16 a[162:163], v19 offset:10240\n"
    "buffer_load_dwordx4 v16, s[8:11], s20 offen offset:3072 lds\n"
    "v_mul_f32 v28, v28, v54\n"
    "v_mul_f32 v23, v23, v53\n"
    "v_mfma_f32_16x16x32_bf16 v[152:155], a[172:175], v[40:43], v[152:155]\n"
    "ds_read_b64_tr_b16 a[164:165], v19 offset:2304\n"
    "v_mul_f32 v25, v25, v55\n"
    "v_mul_f32 v27, v27, v53\n"
    "v_mul_f32 v29, v29, v55\n"
    "v_mfma_f32_16x16x32_bf16 v[156:159], a[172:175], v[44:47], v[156:159]\n"
    "ds_read_b64_tr_b16 a[166:167], v19 offset:10496\n"
    "v_cvt_pk_bf16_f32 v22, 0, v22\n"
    "v_cvt_pk_bf16_f32 v24, 0, v24\n"
    "v_mfma_f32_16x16x32_bf16 v[160:163], a[176:179], v[40:43], v[160:163]\n"
    "ds_read_b64_tr_b16 a[168:169], v19 offset:2560\n"
    "ds_read_b128 v[252:255], v20 offset:3984\n"
    "v_cvt_pk_bf16_f32 v26, 0, v26\n"
    "v_cvt_pk_bf16_f32 v28, 0, v28\n"
    "v_mfma_f32_16x16x32_bf16 v[164:167], a[176:179], v[44:47], v[164:167]\n"
    "ds_read_b64_tr_b16 a[170:171], v19 offset:10752\n"
    "v_cvt_pk_bf16_f32 v23, 0, v23\n"
    "v_cvt_pk_bf16_f32 v25, 0, v25\n"
    "v_cvt_pk_bf16_f32 v27, 0, v27\n"
    "v_mfma_f32_16x16x32_bf16 v[168:171], a[180:183], v[40:43], v[168:171]\n"
    "ds_read_b64_tr_b16 a[172:173], v19 offset:2816\n"
    "v_cvt_pk_bf16_f32 v29, 0, v29\n"
    "v_add_f32 v22, v22, v60\n"
    "v_mfma_f32_16x16x32_bf16 v[172:175], a[180:183], v[44:47], v[172:175]\n"
    "ds_read_b64_tr_b16 a[174:175], v19 offset:11008\n"
    "v_add_f32 v24, v24, v62\n"
    "v_add_f32 v26, v26, v60\n"
    "v_mfma_f32_16x16x32_bf16 v[176:179], a[184:187], v[40:43], v[176:179]\n"
    "ds_read_b64_tr_b16 a[176:177], v19 offset:3072\n"
    "v_add_f32 v28, v28, v62\n"
    "v_add_f32 v23, v23, v61\n"
    "v_add_f32 v25, v25, v63\n"
    "v_mfma_f32_16x16x32_bf16 v[180:183], a[184:187], v[44:47], v[180:183]\n"
    "ds_read_b64_tr_b16 a[178:179], v19 offset:11264\n"
    "v_add_f32 v27, v27, v61\n"
    "v_add_f32 v29, v29, v63\n"
    "v_mfma_f32_16x16x32_bf16 v[184:187], a[188:191], v[40:43], v[184:187]\n"
    "ds_read_b64_tr_b16 a[180:181], v19 offset:3328\n"
    "v_cvt_pk_bf16_f32 v34, v22, v23\n"
    "v_cvt_pk_bf16_f32 v35, v24, v25\n"
    "v_mfma_f32_16x16x32_bf16 v[188:191], a[188:191], v[44:47], v[188:191]\n"
    "ds_read_b64_tr_b16 a[182:183], v19 offset:11520\n"
    "ds_read_b64_tr_b16 a[184:185], v19 offset:3584\n"
    "ds_read_b64_tr_b16 a[186:187], v19 offset:11776\n"
    "ds_read_b64_tr_b16 a[188:189], v19 offset:3840\n"
    "ds_read_b64_tr_b16 a[190:191], v19 offset:12032\n"
    "v_cvt_pk_bf16_f32 v38, v26, v27\n"
    "v_cvt_pk_bf16_f32 v39, v28, v29\n"
    "v_mfma_f32_16x16x32_bf16 v[64:67], a[128:131], v[32:35], v[64:67]\n"
    "s_waitcnt vmcnt(28)\n"
    "s_barrier\n"
    "v_lshlrev_b32 v22, 16, v200\n"
    "v_lshlrev_b32 v24, 16, v201\n"
    "v_mfma_f32_16x16x32_bf16 v[68:71], a[128:131], v[36:39], v[68:71]\n"
    "v_lshlrev_b32 v26, 16, v204\n"
    "v_lshlrev_b32 v28, 16, v205\n"
    "v_mfma_f32_16x16x32_bf16 v[72:75], a[132:135], v[32:35], v[72:75]\n"
    "v_and_b32 v23, 0xffff0000, v200\n"
    "v_and_b32 v25, 0xffff0000, v201\n"
    "v_mfma_f32_16x16x32_bf16 v[76:79], a[132:135], v[36:39], v[76:79]\n"
    "s_add_u32 m0, s70, 0x14000\n"
    "s_nop 0\n"
    "buffer_load_dwordx4 v8, s[60:63], s68 offen offset:0 lds\n"
    "v_and_b32 v27, 0xffff0000, v204\n"
    "v_mfma_f32_16x16x32_bf16 v[80:83], a[136:139], v[32:35], v[80:83]\n"
    "ds_read_b64_tr_b16 a[128:129], v19 offset:16384\n"
    "v_and_b32 v29, 0xffff0000, v205\n"
    "v_mul_f32 v22, v22, v240\n"
    "v_mul_f32 v24, v24, v242\n"
    "v_mfma_f32_16x16x32_bf16 v[84:87], a[136:139], v[36:39], v[84:87]\n"
    "ds_read_b64_tr_b16 a[130:131], v19 offset:24576\n"
    "v_mul_f32 v26, v26, v240\n"
    "v_mul_f32 v28, v28, v242\n"
    "v_mfma_f32_16x16x32_bf16 v[88:91], a[140:143], v[32:35], v[88:91]\n"
    "ds_read_b64_tr_b16 a[132:133], v19 offset:16640\n"
    "ds_read_b128 v[48:51], v20 offset:1792\n"
    "buffer_load_dwordx4 v9, s[60:63], s68 offen offset:1024 lds\n"
    "v_mul_f32 v23, v23, v241\n"
    "v_mul_f32 v25, v25, v243\n"
    "v_mfma_f32_16x16x32_bf16 v[92:95], a[140:143], v[36:39], v[92:95]\n"
    "ds_read_b64_tr_b16 a[134:135], v19 offset:24832\n"
    "v_mul_f32 v27, v27, v241\n"
    "v_mul_f32 v29, v29, v243\n"
    "v_cvt_pk_bf16_f32 v22, 0, v22\n"
    "v_mfma_f32_16x16x32_bf16 v[96:99], a[144:147], v[32:35], v[96:99]\n"
    "ds_read_b64_tr_b16 a[136:137], v19 offset:16896\n"
    "v_cvt_pk_bf16_f32 v24, 0, v24\n"
    "v_cvt_pk_bf16_f32 v26, 0, v26\n"
    "v_mfma_f32_16x16x32_bf16 v[100:103], a[144:147], v[36:39], v[100:103]\n"
    "ds_read_b64_tr_b16 a[138:139], v19 offset:25088\n"
    "buffer_load_dwordx4 v10, s[60:63], s68 offen offset:2048 lds\n"
    "v_cvt_pk_bf16_f32 v28, 0, v28\n"
    "v_cvt_pk_bf16_f32 v23, 0, v23\n"
    "s_waitcnt lgkmcnt(7)\n"
    "v_mfma_f32_16x16x32_bf16 v[104:107], a[148:151], v[32:35], v[104:107]\n"
    "ds_read_b64_tr_b16 a[140:141], v19 offset:17152\n"
    "v_cvt_pk_bf16_f32 v25, 0, v25\n"
    "v_cvt_pk_bf16_f32 v27, 0, v27\n"
    "v_cvt_pk_bf16_f32 v29, 0, v29\n"
    "v_mfma_f32_16x16x32_bf16 v[108:111], a[148:151], v[36:39], v[108:111]\n"
    "ds_read_b64_tr_b16 a[142:143], v19 offset:25344\n"
    "v_add_f32 v22, v22, v248\n"
    "v_add_f32 v24, v24, v250\n"
    "v_mfma_f32_16x16x32_bf16 v[112:115], a[152:155], v[32:35], v[112:115]\n"
    "ds_read_b64_tr_b16 a[144:145], v19 offset:17408\n"
    "ds_read_b128 v[52:55], v20 offset:1808\n"
    "buffer_load_dwordx4 v11, s[60:63], s68 offen offset:3072 lds\n"
    "v_add_f32 v26, v26, v248\n"
    "v_add_f32 v28, v28, v250\n"
    "v_mfma_f32_16x16x32_bf16 v[116:119], a[152:155], v[36:39], v[116:119]\n"
    "ds_read_b64_tr_b16 a[146:147], v19 offset:25600\n"
    "v_add_f32 v23, v23, v249\n"
    "v_add_f32 v25, v25, v251\n"
    "v_add_f32 v27, v27, v249\n"
    "v_mfma_f32_16x16x32_bf16 v[120:123], a[156:159], v[32:35], v[120:123]\n"
    "ds_read_b64_tr_b16 a[148:149], v19 offset:17664\n"
    "v_add_f32 v29, v29, v251\n"
    "v_cvt_pk_bf16_f32 v40, v22, v23\n"
    "v_mfma_f32_16x16x32_bf16 v[124:127], a[156:159], v[36:39], v[124:127]\n"
    "ds_read_b64_tr_b16 a[150:151], v19 offset:25856\n"
    "v_cvt_pk_bf16_f32 v41, v24, v25\n"
    "v_cvt_pk_bf16_f32 v44, v26, v27\n"
    "v_mfma_f32_16x16x32_bf16 v[128:131], a[160:163], v[32:35], v[128:131]\n"
    "ds_read_b64_tr_b16 a[152:153], v19 offset:17920\n"
    "v_cvt_pk_bf16_f32 v45, v28, v29\n"
    "v_lshlrev_b32 v22, 16, v202\n"
    "v_lshlrev_b32 v24, 16, v203\n"
    "v_mfma_f32_16x16x32_bf16 v[132:135], a[160:163], v[36:39], v[132:135]\n"
    "ds_read_b64_tr_b16 a[154:155], v19 offset:26112\n"
    "v_lshlrev_b32 v26, 16, v206\n"
    "v_lshlrev_b32 v28, 16, v207\n"
    "v_mfma_f32_16x16x32_bf16 v[136:139], a[164:167], v[32:35], v[136:139]\n"
    "ds_read_b64_tr_b16 a[156:157], v19 offset:18176\n"
    "ds_read_b128 v[56:59], v20 offset:4096\n"
    "v_and_b32 v23, 0xffff0000, v202\n"
    "v_and_b32 v25, 0xffff0000, v203\n"
    "v_mfma_f32_16x16x32_bf16 v[140:143], a[164:167], v[36:39], v[140:143]\n"
    "ds_read_b64_tr_b16 a[158:159], v19 offset:26368\n"
    "v_and_b32 v27, 0xffff0000, v206\n"
    "v_and_b32 v29, 0xffff0000, v207\n"
    "v_mul_f32 v22, v22, v244\n"
    "v_mfma_f32_16x16x32_bf16 v[144:147], a[168:171], v[32:35], v[144:147]\n"
    "ds_read_b64_tr_b16 a[160:161], v19 offset:18432\n"
    "v_mul_f32 v24, v24, v246\n"
    "v_mul_f32 v26, v26, v244\n"
    "v_mfma_f32_16x16x32_bf16 v[148:151], a[168:171], v[36:39], v[148:151]\n"
    "ds_read_b64_tr_b16 a[162:163], v19 offset:26624\n"
    "v_mul_f32 v28, v28, v246\n"
    "v_mul_f32 v23, v23, v245\n"
    "v_mfma_f32_16x16x32_bf16 v[152:155], a[172:175], v[32:35], v[152:155]\n"
    "ds_read_b64_tr_b16 a[164:165], v19 offset:18688\n"
    "v_mul_f32 v25, v25, v247\n"
    "v_mul_f32 v27, v27, v245\n"
    "v_mul_f32 v29, v29, v247\n"
    "v_mfma_f32_16x16x32_bf16 v[156:159], a[172:175], v[36:39], v[156:159]\n"
    "ds_read_b64_tr_b16 a[166:167], v19 offset:26880\n"
    "v_cvt_pk_bf16_f32 v22, 0, v22\n"
    "v_cvt_pk_bf16_f32 v24, 0, v24\n"
    "v_mfma_f32_16x16x32_bf16 v[160:163], a[176:179], v[32:35], v[160:163]\n"
    "ds_read_b64_tr_b16 a[168:169], v19 offset:18944\n"
    "ds_read_b128 v[60:63], v20 offset:4112\n"
    "v_cvt_pk_bf16_f32 v26, 0, v26\n"
    "v_cvt_pk_bf16_f32 v28, 0, v28\n"
    "v_mfma_f32_16x16x32_bf16 v[164:167], a[176:179], v[36:39], v[164:167]\n"
    "ds_read_b64_tr_b16 a[170:171], v19 offset:27136\n"
    "v_cvt_pk_bf16_f32 v23, 0, v23\n"
    "v_cvt_pk_bf16_f32 v25, 0, v25\n"
    "v_cvt_pk_bf16_f32 v27, 0, v27\n"
    "v_mfma_f32_16x16x32_bf16 v[168:171], a[180:183], v[32:35], v[168:171]\n"
    "ds_read_b64_tr_b16 a[172:173], v19 offset:19200\n"
    "v_cvt_pk_bf16_f32 v29, 0, v29\n"
    "v_add_f32 v22, v22, v252\n"
    "v_mfma_f32_16x16x32_bf16 v[172:175], a[180:183], v[36:39], v[172:175]\n"
    "ds_read_b64_tr_b16 a[174:175], v19 offset:27392\n"
    "v_add_f32 v24, v24, v254\n"
    "v_add_f32 v26, v26, v252\n"
    "v_mfma_f32_16x16x32_bf16 v[176:179], a[184:187], v[32:35], v[176:179]\n"
    "ds_read_b64_tr_b16 a[176:177], v19 offset:19456\n"
    "v_add_f32 v28, v28, v254\n"
    "v_add_f32 v23, v23, v253\n"
    "v_add_f32 v25, v25, v255\n"
    "v_mfma_f32_16x16x32_bf16 v[180:183], a[184:187], v[36:39], v[180:183]\n"
    "ds_read_b64_tr_b16 a[178:179], v19 offset:27648\n"
    "v_add_f32 v27, v27, v253\n"
    "v_add_f32 v29, v29, v255\n"
    "v_mfma_f32_16x16x32_bf16 v[184:187], a[188:191], v[32:35], v[184:187]\n"
    "ds_read_b64_tr_b16 a[180:181], v19 offset:19712\n"
    "v_cvt_pk_bf16_f32 v42, v22, v23\n"
    "v_cvt_pk_bf16_f32 v43, v24, v25\n"
    "v_mfma_f32_16x16x32_bf16 v[188:191], a[188:191], v[36:39], v[188:191]\n"
    "s_add_u32 s68, s68, 0x40000\n"
    "ds_read_b64_tr_b16 a[182:183], v19 offset:27904\n"
    "ds_read_b64_tr_b16 a[184:185], v19 offset:19968\n"
    "ds_read_b64_tr_b16 a[186:187], v19 offset:28160\n"
    "ds_read_b64_tr_b16 a[188:189], v19 offset:20224\n"
    "ds_read_b64_tr_b16 a[190:191], v19 offset:28416\n"
    "v_cvt_pk_bf16_f32 v46, v26, v27\n"
    "v_cvt_pk_bf16_f32 v47, v28, v29\n"
    "s_waitcnt lgkmcnt(11)\n"
    "v_mfma_f32_16x16x32_bf16 v[64:67], a[128:131], v[40:43], v[64:67]\n"
    "s_waitcnt vmcnt(26)\n"
    "s_barrier\n"
    "v_lshlrev_b32 v22, 16, v208\n"
    "v_lshlrev_b32 v24, 16, v209\n"
    "v_mfma_f32_16x16x32_bf16 v[68:71], a[128:131], v[44:47], v[68:71]\n"
    "v_lshlrev_b32 v26, 16, v212\n"
    "v_lshlrev_b32 v28, 16, v213\n"
    "v_mfma_f32_16x16x32_bf16 v[72:75], a[132:135], v[40:43], v[72:75]\n"
    "v_and_b32 v23, 0xffff0000, v208\n"
    "v_and_b32 v25, 0xffff0000, v209\n"
    "v_mfma_f32_16x16x32_bf16 v[76:79], a[132:135], v[44:47], v[76:79]\n"
    "ds_read_b64_tr_b16 a[128:129], v19 offset:32768\n"
    "v_and_b32 v27, 0xffff0000, v212\n"
    "v_mfma_f32_16x16x32_bf16 v[80:83], a[136:139], v[40:43], v[80:83]\n"
    "v_and_b32 v29, 0xffff0000, v213\n"
    "v_mul_f32 v22, v22, v48\n"
    "v_mul_f32 v24, v24, v50\n"
    "v_mfma_f32_16x16x32_bf16 v[84:87], a[136:139], v[44:47], v[84:87]\n"
    "ds_read_b64_tr_b16 a[130:131], v19 offset:40960\n"
    "v_mul_f32 v26, v26, v48\n"
    "v_mul_f32 v28, v28, v50\n"
    "v_mfma_f32_16x16x32_bf16 v[88:91], a[140:143], v[40:43], v[88:91]\n"
    "ds_read_b64_tr_b16 a[132:133], v19 offset:33024\n"
    "ds_read_b128 v[240:243], v20 offset:1920\n"
    "v_mul_f32 v23, v23, v49\n"
    "v_mul_f32 v25, v25, v51\n"
    "v_mfma_f32_16x16x32_bf16 v[92:95], a[140:143], v[44:47], v[92:95]\n"
    "ds_read_b64_tr_b16 a[134:135], v19 offset:41216\n"
    "v_mul_f32 v27, v27, v49\n"
    "v_mul_f32 v29, v29, v51\n"
    "v_cvt_pk_bf16_f32 v22, 0, v22\n"
    "v_mfma_f32_16x16x32_bf16 v[96:99], a[144:147], v[40:43], v[96:99]\n"
    "ds_read_b64_tr_b16 a[136:137], v19 offset:33280\n"
    "v_cvt_pk_bf16_f32 v24, 0, v24\n"
    "v_cvt_pk_bf16_f32 v26, 0, v26\n"
    "v_mfma_f32_16x16x32_bf16 v[100:103], a[144:147], v[44:47], v[100:103]\n"
    "ds_read_b64_tr_b16 a[138:139], v19 offset:41472\n"
    "v_cvt_pk_bf16_f32 v28, 0, v28\n"
    "v_cvt_pk_bf16_f32 v23, 0, v23\n"
    "v_mfma_f32_16x16x32_bf16 v[104:107], a[148:151], v[40:43], v[104:107]\n"
    "ds_read_b64_tr_b16 a[140:141], v19 offset:33536\n"
    "v_cvt_pk_bf16_f32 v25, 0, v25\n"
    "v_cvt_pk_bf16_f32 v27, 0, v27\n"
    "v_cvt_pk_bf16_f32 v29, 0, v29\n"
    "v_mfma_f32_16x16x32_bf16 v[108:111], a[148:151], v[44:47], v[108:111]\n"
    "ds_read_b64_tr_b16 a[142:143], v19 offset:41728\n"
    "v_add_f32 v22, v22, v56\n"
    "v_add_f32 v24, v24, v58\n"
    "v_mfma_f32_16x16x32_bf16 v[112:115], a[152:155], v[40:43], v[112:115]\n"
    "ds_read_b64_tr_b16 a[144:145], v19 offset:33792\n"
    "ds_read_b128 v[244:247], v20 offset:1936\n"
    "v_add_f32 v26, v26, v56\n"
    "v_add_f32 v28, v28, v58\n"
    "v_mfma_f32_16x16x32_bf16 v[116:119], a[152:155], v[44:47], v[116:119]\n"
    "ds_read_b64_tr_b16 a[146:147], v19 offset:41984\n"
    "v_add_f32 v23, v23, v57\n"
    "v_add_f32 v25, v25, v59\n"
    "v_add_f32 v27, v27, v57\n"
    "v_mfma_f32_16x16x32_bf16 v[120:123], a[156:159], v[40:43], v[120:123]\n"
    "ds_read_b64_tr_b16 a[148:149], v19 offset:34048\n"
    "v_add_f32 v29, v29, v59\n"
    "v_cvt_pk_bf16_f32 v32, v22, v23\n"
    "v_mfma_f32_16x16x32_bf16 v[124:127], a[156:159], v[44:47], v[124:127]\n"
    "ds_read_b64_tr_b16 a[150:151], v19 offset:42240\n"
    "v_cvt_pk_bf16_f32 v33, v24, v25\n"
    "v_cvt_pk_bf16_f32 v36, v26, v27\n"
    "v_mfma_f32_16x16x32_bf16 v[128:131], a[160:163], v[40:43], v[128:131]\n"
    "ds_read_b64_tr_b16 a[152:153], v19 offset:34304\n"
    "v_cvt_pk_bf16_f32 v37, v28, v29\n"
    "v_lshlrev_b32 v22, 16, v210\n"
    "v_lshlrev_b32 v24, 16, v211\n"
    "v_mfma_f32_16x16x32_bf16 v[132:135], a[160:163], v[44:47], v[132:135]\n"
    "ds_read_b64_tr_b16 a[154:155], v19 offset:42496\n"
    "v_lshlrev_b32 v26, 16, v214\n"
    "v_lshlrev_b32 v28, 16, v215\n"
    "v_mfma_f32_16x16x32_bf16 v[136:139], a[164:167], v[40:43], v[136:139]\n"
    "ds_read_b64_tr_b16 a[156:157], v19 offset:34560\n"
    "ds_read_b128 v[248:251], v20 offset:4224\n"
    "v_and_b32 v23, 0xffff0000, v210\n"
    "v_and_b32 v25, 0xffff0000, v211\n"
    "v_mfma_f32_16x16x32_bf16 v[140:143], a[164:167], v[44:47], v[140:143]\n"
    "ds_read_b64_tr_b16 a[158:159], v19 offset:42752\n"
    "v_and_b32 v27, 0xffff0000, v214\n"
    "v_and_b32 v29, 0xffff0000, v215\n"
    "v_mul_f32 v22, v22, v52\n"
    "s_waitcnt lgkmcnt(10)\n"
    "v_mfma_f32_16x16x32_bf16 v[144:147], a[168:171], v[40:43], v[144:147]\n"
    "ds_read_b64_tr_b16 a[160:161], v19 offset:34816\n"
    "v_mul_f32 v24, v24, v54\n"
    "v_mul_f32 v26, v26, v52\n"
    "v_mfma_f32_16x16x32_bf16 v[148:151], a[168:171], v[44:47], v[148:151]\n"
    "ds_read_b64_tr_b16 a[162:163], v19 offset:43008\n"
    "v_mul_f32 v28, v28, v54\n"
    "v_mul_f32 v23, v23, v53\n"
    "v_mfma_f32_16x16x32_bf16 v[152:155], a[172:175], v[40:43], v[152:155]\n"
    "ds_read_b64_tr_b16 a[164:165], v19 offset:35072\n"
    "v_mul_f32 v25, v25, v55\n"
    "v_mul_f32 v27, v27, v53\n"
    "v_mul_f32 v29, v29, v55\n"
    "v_mfma_f32_16x16x32_bf16 v[156:159], a[172:175], v[44:47], v[156:159]\n"
    "ds_read_b64_tr_b16 a[166:167], v19 offset:43264\n"
    "v_cvt_pk_bf16_f32 v22, 0, v22\n"
    "v_cvt_pk_bf16_f32 v24, 0, v24\n"
    "v_mfma_f32_16x16x32_bf16 v[160:163], a[176:179], v[40:43], v[160:163]\n"
    "ds_read_b64_tr_b16 a[168:169], v19 offset:35328\n"
    "ds_read_b128 v[252:255], v20 offset:4240\n"
    "v_cvt_pk_bf16_f32 v26, 0, v26\n"
    "v_cvt_pk_bf16_f32 v28, 0, v28\n"
    "v_mfma_f32_16x16x32_bf16 v[164:167], a[176:179], v[44:47], v[164:167]\n"
    "ds_read_b64_tr_b16 a[170:171], v19 offset:43520\n"
    "v_cvt_pk_bf16_f32 v23, 0, v23\n"
    "v_cvt_pk_bf16_f32 v25, 0, v25\n"
    "v_cvt_pk_bf16_f32 v27, 0, v27\n"
    "v_mfma_f32_16x16x32_bf16 v[168:171], a[180:183], v[40:43], v[168:171]\n"
    "ds_read_b64_tr_b16 a[172:173], v19 offset:35584\n"
    "v_cvt_pk_bf16_f32 v29, 0, v29\n"
    "v_add_f32 v22, v22, v60\n"
    "v_mfma_f32_16x16x32_bf16 v[172:175], a[180:183], v[44:47], v[172:175]\n"
    "ds_read_b64_tr_b16 a[174:175], v19 offset:43776\n"
    "v_add_f32 v24, v24, v62\n"
    "v_add_f32 v26, v26, v60\n"
    "v_mfma_f32_16x16x32_bf16 v[176:179], a[184:187], v[40:43], v[176:179]\n"
    "ds_read_b64_tr_b16 a[176:177], v19 offset:35840\n"
    "v_add_f32 v28, v28, v62\n"
    "v_add_f32 v23, v23, v61\n"
    "v_add_f32 v25, v25, v63\n"
    "v_mfma_f32_16x16x32_bf16 v[180:183], a[184:187], v[44:47], v[180:183]\n"
    "ds_read_b64_tr_b16 a[178:179], v19 offset:44032\n"
    "v_add_f32 v27, v27, v61\n"
    "v_add_f32 v29, v29, v63\n"
    "v_mfma_f32_16x16x32_bf16 v[184:187], a[188:191], v[40:43], v[184:187]\n"
    "ds_read_b64_tr_b16 a[180:181], v19 offset:36096\n"
    "v_cvt_pk_bf16_f32 v34, v22, v23\n"
    "v_cvt_pk_bf16_f32 v35, v24, v25\n"
    "v_mfma_f32_16x16x32_bf16 v[188:191], a[188:191], v[44:47], v[188:191]\n"
    "ds_read_b64_tr_b16 a[182:183], v19 offset:44288\n"
    "ds_read_b64_tr_b16 a[184:185], v19 offset:36352\n"
    "ds_read_b64_tr_b16 a[186:187], v19 offset:44544\n"
    "ds_read_b64_tr_b16 a[188:189], v19 offset:36608\n"
    "ds_read_b64_tr_b16 a[190:191], v19 offset:44800\n"
    "v_cvt_pk_bf16_f32 v38, v26, v27\n"
    "v_cvt_pk_bf16_f32 v39, v28, v29\n"
    "v_mfma_f32_16x16x32_bf16 v[64:67], a[128:131], v[32:35], v[64:67]\n"
    "s_waitcnt vmcnt(16)\n"
    "s_barrier\n"
    "v_lshlrev_b32 v22, 16, v216\n"
    "v_lshlrev_b32 v24, 16, v217\n"
    "v_mfma_f32_16x16x32_bf16 v[68:71], a[128:131], v[36:39], v[68:71]\n"
    "v_lshlrev_b32 v26, 16, v220\n"
    "v_lshlrev_b32 v28, 16, v221\n"
    "v_mfma_f32_16x16x32_bf16 v[72:75], a[132:135], v[32:35], v[72:75]\n"
    "v_and_b32 v23, 0xffff0000, v216\n"
    "v_and_b32 v25, 0xffff0000, v217\n"
    "v_mfma_f32_16x16x32_bf16 v[76:79], a[132:135], v[36:39], v[76:79]\n"
    "s_add_u32 m0, s23, 0x0\n"
    "s_add_u32 s21, s26, 0x0\n"
    "buffer_load_dwordx4 v17, s[12:15], s21 offen offset:0 lds\n"
    "v_and_b32 v27, 0xffff0000, v220\n"
    "v_mfma_f32_16x16x32_bf16 v[80:83], a[136:139], v[32:35], v[80:83]\n"
    "ds_read_b64_tr_b16 a[128:129], v30 offset:0\n"
    "v_and_b32 v29, 0xffff0000, v221\n"
    "v_mul_f32 v22, v22, v240\n"
    "v_mul_f32 v24, v24, v242\n"
    "v_mfma_f32_16x16x32_bf16 v[84:87], a[136:139], v[36:39], v[84:87]\n"
    "ds_read_b64_tr_b16 a[130:131], v30 offset:8192\n"
    "v_mul_f32 v26, v26, v240\n"
    "v_mul_f32 v28, v28, v242\n"
    "v_mfma_f32_16x16x32_bf16 v[88:91], a[140:143], v[32:35], v[88:91]\n"
    "ds_read_b64_tr_b16 a[132:133], v30 offset:256\n"
    "ds_read_b128 v[48:51], v20 offset:2048\n"
    "buffer_load_dwordx4 v18, s[12:15], s21 offen offset:1024 lds\n"
    "v_mul_f32 v23, v23, v241\n"
    "v_mul_f32 v25, v25, v243\n"
    "v_mfma_f32_16x16x32_bf16 v[92:95], a[140:143], v[36:39], v[92:95]\n"
    "ds_read_b64_tr_b16 a[134:135], v30 offset:8448\n"
    "v_mul_f32 v27, v27, v241\n"
    "v_mul_f32 v29, v29, v243\n"
    "v_cvt_pk_bf16_f32 v22, 0, v22\n"
    "s_waitcnt lgkmcnt(5)\n"
    "v_mfma_f32_16x16x32_bf16 v[96:99], a[144:147], v[32:35], v[96:99]\n"
    "ds_read_b64_tr_b16 a[136:137], v30 offset:512\n"
    "v_cvt_pk_bf16_f32 v24, 0, v24\n"
    "v_cvt_pk_bf16_f32 v26, 0, v26\n"
    "v_mfma_f32_16x16x32_bf16 v[100:103], a[144:147], v[36:39], v[100:103]\n"
    "ds_read_b64_tr_b16 a[138:139], v30 offset:8704\n"
    "buffer_load_dwordx4 v17, s[12:15], s21 offen offset:2048 lds\n"
    "v_cvt_pk_bf16_f32 v28, 0, v28\n"
    "v_cvt_pk_bf16_f32 v23, 0, v23\n"
    "v_mfma_f32_16x16x32_bf16 v[104:107], a[148:151], v[32:35], v[104:107]\n"
    "ds_read_b64_tr_b16 a[140:141], v30 offset:768\n"
    "v_cvt_pk_bf16_f32 v25, 0, v25\n"
    "v_cvt_pk_bf16_f32 v27, 0, v27\n"
    "v_cvt_pk_bf16_f32 v29, 0, v29\n"
    "v_mfma_f32_16x16x32_bf16 v[108:111], a[148:151], v[36:39], v[108:111]\n"
    "ds_read_b64_tr_b16 a[142:143], v30 offset:8960\n"
    "v_add_f32 v22, v22, v248\n"
    "v_add_f32 v24, v24, v250\n"
    "v_mfma_f32_16x16x32_bf16 v[112:115], a[152:155], v[32:35], v[112:115]\n"
    "ds_read_b64_tr_b16 a[144:145], v30 offset:1024\n"
    "ds_read_b128 v[52:55], v20 offset:2064\n"
    "buffer_load_dwordx4 v18, s[12:15], s21 offen offset:3072 lds\n"
    "v_add_f32 v26, v26, v248\n"
    "v_add_f32 v28, v28, v250\n"
    "v_mfma_f32_16x16x32_bf16 v[116:119], a[152:155], v[36:39], v[116:119]\n"
    "ds_read_b64_tr_b16 a[146:147], v30 offset:9216\n"
    "v_add_f32 v23, v23, v249\n"
    "v_add_f32 v25, v25, v251\n"
    "v_add_f32 v27, v27, v249\n"
    "v_mfma_f32_16x16x32_bf16 v[120:123], a[156:159], v[32:35], v[120:123]\n"
    "ds_read_b64_tr_b16 a[148:149], v30 offset:1280\n"
    "v_add_f32 v29, v29, v251\n"
    "v_cvt_pk_bf16_f32 v40, v22, v23\n"
    "v_mfma_f32_16x16x32_bf16 v[124:127], a[156:159], v[36:39], v[124:127]\n"
    "ds_read_b64_tr_b16 a[150:151], v30 offset:9472\n"
    "v_cvt_pk_bf16_f32 v41, v24, v25\n"
    "v_cvt_pk_bf16_f32 v44, v26, v27\n"
    "v_mfma_f32_16x16x32_bf16 v[128:131], a[160:163], v[32:35], v[128:131]\n"
    "ds_read_b64_tr_b16 a[152:153], v30 offset:1536\n"
    "v_cvt_pk_bf16_f32 v45, v28, v29\n"
    "v_lshlrev_b32 v22, 16, v218\n"
    "v_lshlrev_b32 v24, 16, v219\n"
    "v_mfma_f32_16x16x32_bf16 v[132:135], a[160:163], v[36:39], v[132:135]\n"
    "ds_read_b64_tr_b16 a[154:155], v30 offset:9728\n"
    "v_lshlrev_b32 v26, 16, v222\n"
    "v_lshlrev_b32 v28, 16, v223\n"
    "v_mfma_f32_16x16x32_bf16 v[136:139], a[164:167], v[32:35], v[136:139]\n"
    "ds_read_b64_tr_b16 a[156:157], v30 offset:1792\n"
    "ds_read_b128 v[56:59], v20 offset:4352\n"
    "v_and_b32 v23, 0xffff0000, v218\n"
    "v_and_b32 v25, 0xffff0000, v219\n"
    "v_mfma_f32_16x16x32_bf16 v[140:143], a[164:167], v[36:39], v[140:143]\n"
    "ds_read_b64_tr_b16 a[158:159], v30 offset:9984\n"
    "v_and_b32 v27, 0xffff0000, v222\n"
    "v_and_b32 v29, 0xffff0000, v223\n"
    "v_mul_f32 v22, v22, v244\n"
    "v_mfma_f32_16x16x32_bf16 v[144:147], a[168:171], v[32:35], v[144:147]\n"
    "ds_read_b64_tr_b16 a[160:161], v30 offset:2048\n"
    "v_mul_f32 v24, v24, v246\n"
    "v_mul_f32 v26, v26, v244\n"
    "v_mfma_f32_16x16x32_bf16 v[148:151], a[168:171], v[36:39], v[148:151]\n"
    "ds_read_b64_tr_b16 a[162:163], v30 offset:10240\n"
    "v_mul_f32 v28, v28, v246\n"
    "v_mul_f32 v23, v23, v245\n"
    "v_mfma_f32_16x16x32_bf16 v[152:155], a[172:175], v[32:35], v[152:155]\n"
    "ds_read_b64_tr_b16 a[164:165], v30 offset:2304\n"
    "v_mul_f32 v25, v25, v247\n"
    "v_mul_f32 v27, v27, v245\n"
    "v_mul_f32 v29, v29, v247\n"
    "v_mfma_f32_16x16x32_bf16 v[156:159], a[172:175], v[36:39], v[156:159]\n"
    "ds_read_b64_tr_b16 a[166:167], v30 offset:10496\n"
    "v_cvt_pk_bf16_f32 v22, 0, v22\n"
    "v_cvt_pk_bf16_f32 v24, 0, v24\n"
    "v_mfma_f32_16x16x32_bf16 v[160:163], a[176:179], v[32:35], v[160:163]\n"
    "ds_read_b64_tr_b16 a[168:169], v30 offset:2560\n"
    "ds_read_b128 v[60:63], v20 offset:4368\n"
    "v_cvt_pk_bf16_f32 v26, 0, v26\n"
    "v_cvt_pk_bf16_f32 v28, 0, v28\n"
    "v_mfma_f32_16x16x32_bf16 v[164:167], a[176:179], v[36:39], v[164:167]\n"
    "ds_read_b64_tr_b16 a[170:171], v30 offset:10752\n"
    "v_cvt_pk_bf16_f32 v23, 0, v23\n"
    "v_cvt_pk_bf16_f32 v25, 0, v25\n"
    "v_cvt_pk_bf16_f32 v27, 0, v27\n"
    "v_mfma_f32_16x16x32_bf16 v[168:171], a[180:183], v[32:35], v[168:171]\n"
    "ds_read_b64_tr_b16 a[172:173], v30 offset:2816\n"
    "v_cvt_pk_bf16_f32 v29, 0, v29\n"
    "v_add_f32 v22, v22, v252\n"
    "v_mfma_f32_16x16x32_bf16 v[172:175], a[180:183], v[36:39], v[172:175]\n"
    "ds_read_b64_tr_b16 a[174:175], v30 offset:11008\n"
    "v_add_f32 v24, v24, v254\n"
    "v_add_f32 v26, v26, v252\n"
    "v_mfma_f32_16x16x32_bf16 v[176:179], a[184:187], v[32:35], v[176:179]\n"
    "ds_read_b64_tr_b16 a[176:177], v30 offset:3072\n"
    "v_add_f32 v28, v28, v254\n"
    "v_add_f32 v23, v23, v253\n"
    "v_add_f32 v25, v25, v255\n"
    "v_mfma_f32_16x16x32_bf16 v[180:183], a[184:187], v[36:39], v[180:183]\n"
    "ds_read_b64_tr_b16 a[178:179], v30 offset:11264\n"
    "v_add_f32 v27, v27, v253\n"
    "v_add_f32 v29, v29, v255\n"
    "v_mfma_f32_16x16x32_bf16 v[184:187], a[188:191], v[32:35], v[184:187]\n"
    "ds_read_b64_tr_b16 a[180:181], v30 offset:3328\n"
    "v_cvt_pk_bf16_f32 v42, v22, v23\n"
    "v_cvt_pk_bf16_f32 v43, v24, v25\n"
    "v_mfma_f32_16x16x32_bf16 v[188:191], a[188:191], v[36:39], v[188:191]\n"
    "ds_read_b64_tr_b16 a[182:183], v30 offset:11520\n"
    "ds_read_b64_tr_b16 a[184:185], v30 offset:3584\n"
    "ds_read_b64_tr_b16 a[186:187], v30 offset:11776\n"
    "ds_read_b64_tr_b16 a[188:189], v30 offset:3840\n"
    "ds_read_b64_tr_b16 a[190:191], v30 offset:12032\n"
    "v_cvt_pk_bf16_f32 v46, v26, v27\n"
    "v_cvt_pk_bf16_f32 v47, v28, v29\n"
    "s_waitcnt lgkmcnt(11)\n"
    "v_mfma_f32_16x16x32_bf16 v[64:67], a[128:131], v[40:43], v[64:67]\n"
    "s_waitcnt vmcnt(14)\n"
    "s_barrier\n"
    "v_lshlrev_b32 v22, 16, v224\n"
    "v_lshlrev_b32 v24, 16, v225\n"
    "v_mfma_f32_16x16x32_bf16 v[68:71], a[128:131], v[44:47], v[68:71]\n"
    "v_lshlrev_b32 v26, 16, v228\n"
    "v_lshlrev_b32 v28, 16, v229\n"
    "v_mfma_f32_16x16x32_bf16 v[72:75], a[132:135], v[40:43], v[72:75]\n"
    "v_and_b32 v23, 0xffff0000, v224\n"
    "v_and_b32 v25, 0xffff0000, v225\n"
    "v_mfma_f32_16x16x32_bf16 v[76:79], a[132:135], v[44:47], v[76:79]\n"
    "s_add_u32 m0, s22, 0x8000\n"
    "s_add_u32 s20, s26, 0x8000\n"
    "buffer_load_dwordx4 v13, s[8:11], s20 offen offset:0 lds\n"
    "v_and_b32 v27, 0xffff0000, v228\n"
    "v_mfma_f32_16x16x32_bf16 v[80:83], a[136:139], v[40:43], v[80:83]\n"
    "ds_read_b64_tr_b16 a[128:129], v30 offset:16384\n"
    "v_and_b32 v29, 0xffff0000, v229\n"
    "v_mul_f32 v22, v22, v48\n"
    "v_mul_f32 v24, v24, v50\n"
    "v_mfma_f32_16x16x32_bf16 v[84:87], a[136:139], v[44:47], v[84:87]\n"
    "ds_read_b64_tr_b16 a[130:131], v30 offset:24576\n"
    "v_mul_f32 v26, v26, v48\n"
    "v_mul_f32 v28, v28, v50\n"
    "v_mfma_f32_16x16x32_bf16 v[88:91], a[140:143], v[40:43], v[88:91]\n"
    "ds_read_b64_tr_b16 a[132:133], v30 offset:16640\n"
    "ds_read_b128 v[240:243], v20 offset:2176\n"
    "buffer_load_dwordx4 v14, s[8:11], s20 offen offset:1024 lds\n"
    "v_mul_f32 v23, v23, v49\n"
    "v_mul_f32 v25, v25, v51\n"
    "v_mfma_f32_16x16x32_bf16 v[92:95], a[140:143], v[44:47], v[92:95]\n"
    "ds_read_b64_tr_b16 a[134:135], v30 offset:24832\n"
    "v_mul_f32 v27, v27, v49\n"
    "v_mul_f32 v29, v29, v51\n"
    "v_cvt_pk_bf16_f32 v22, 0, v22\n"
    "v_mfma_f32_16x16x32_bf16 v[96:99], a[144:147], v[40:43], v[96:99]\n"
    "ds_read_b64_tr_b16 a[136:137], v30 offset:16896\n"
    "v_cvt_pk_bf16_f32 v24, 0, v24\n"
    "v_cvt_pk_bf16_f32 v26, 0, v26\n"
    "v_mfma_f32_16x16x32_bf16 v[100:103], a[144:147], v[44:47], v[100:103]\n"
    "ds_read_b64_tr_b16 a[138:139], v30 offset:25088\n"
    "buffer_load_dwordx4 v15, s[8:11], s20 offen offset:2048 lds\n"
    "v_cvt_pk_bf16_f32 v28, 0, v28\n"
    "v_cvt_pk_bf16_f32 v23, 0, v23\n"
    "v_mfma_f32_16x16x32_bf16 v[104:107], a[148:151], v[40:43], v[104:107]\n"
    "ds_read_b64_tr_b16 a[140:141], v30 offset:17152\n"
    "v_cvt_pk_bf16_f32 v25, 0, v25\n"
    "v_cvt_pk_bf16_f32 v27, 0, v27\n"
    "v_cvt_pk_bf16_f32 v29, 0, v29\n"
    "v_mfma_f32_16x16x32_bf16 v[108:111], a[148:151], v[44:47], v[108:111]\n"
    "ds_read_b64_tr_b16 a[142:143], v30 offset:25344\n"
    "v_add_f32 v22, v22, v56\n"
    "v_add_f32 v24, v24, v58\n"
    "v_mfma_f32_16x16x32_bf16 v[112:115], a[152:155], v[40:43], v[112:115]\n"
    "ds_read_b64_tr_b16 a[144:145], v30 offset:17408\n"
    "ds_read_b128 v[244:247], v20 offset:2192\n"
    "buffer_load_dwordx4 v16, s[8:11], s20 offen offset:3072 lds\n"
    "v_add_f32 v26, v26, v56\n"
    "v_add_f32 v28, v28, v58\n"
    "v_mfma_f32_16x16x32_bf16 v[116:119], a[152:155], v[44:47], v[116:119]\n"
    "ds_read_b64_tr_b16 a[146:147], v30 offset:25600\n"
    "v_add_f32 v23, v23, v57\n"
    "v_add_f32 v25, v25, v59\n"
    "v_add_f32 v27, v27, v57\n"
    "v_mfma_f32_16x16x32_bf16 v[120:123], a[156:159], v[40:43], v[120:123]\n"
    "ds_read_b64_tr_b16 a[148:149], v30 offset:17664\n"
    "v_add_f32 v29, v29, v59\n"
    "v_cvt_pk_bf16_f32 v32, v22, v23\n"
    "v_mfma_f32_16x16x32_bf16 v[124:127], a[156:159], v[44:47], v[124:127]\n"
    "ds_read_b64_tr_b16 a[150:151], v30 offset:25856\n"
    "v_cvt_pk_bf16_f32 v33, v24, v25\n"
    "v_cvt_pk_bf16_f32 v36, v26, v27\n"
    "v_mfma_f32_16x16x32_bf16 v[128:131], a[160:163], v[40:43], v[128:131]\n"
    "ds_read_b64_tr_b16 a[152:153], v30 offset:17920\n"
    "v_cvt_pk_bf16_f32 v37, v28, v29\n"
    "v_lshlrev_b32 v22, 16, v226\n"
    "v_lshlrev_b32 v24, 16, v227\n"
    "v_mfma_f32_16x16x32_bf16 v[132:135], a[160:163], v[44:47], v[132:135]\n"
    "ds_read_b64_tr_b16 a[154:155], v30 offset:26112\n"
    "v_lshlrev_b32 v26, 16, v230\n"
    "v_lshlrev_b32 v28, 16, v231\n"
    "v_mfma_f32_16x16x32_bf16 v[136:139], a[164:167], v[40:43], v[136:139]\n"
    "ds_read_b64_tr_b16 a[156:157], v30 offset:18176\n"
    "ds_read_b128 v[248:251], v20 offset:4480\n"
    "v_and_b32 v23, 0xffff0000, v226\n"
    "v_and_b32 v25, 0xffff0000, v227\n"
    "v_mfma_f32_16x16x32_bf16 v[140:143], a[164:167], v[44:47], v[140:143]\n"
    "ds_read_b64_tr_b16 a[158:159], v30 offset:26368\n"
    "v_and_b32 v27, 0xffff0000, v230\n"
    "v_and_b32 v29, 0xffff0000, v231\n"
    "v_mul_f32 v22, v22, v52\n"
    "s_waitcnt lgkmcnt(9)\n"
    "v_mfma_f32_16x16x32_bf16 v[144:147], a[168:171], v[40:43], v[144:147]\n"
    "ds_read_b64_tr_b16 a[160:161], v30 offset:18432\n"
    "v_mul_f32 v24, v24, v54\n"
    "v_mul_f32 v26, v26, v52\n"
    "v_mfma_f32_16x16x32_bf16 v[148:151], a[168:171], v[44:47], v[148:151]\n"
    "ds_read_b64_tr_b16 a[162:163], v30 offset:26624\n"
    "v_mul_f32 v28, v28, v54\n"
    "v_mul_f32 v23, v23, v53\n"
    "v_mfma_f32_16x16x32_bf16 v[152:155], a[172:175], v[40:43], v[152:155]\n"
    "ds_read_b64_tr_b16 a[164:165], v30 offset:18688\n"
    "v_mul_f32 v25, v25, v55\n"
    "v_mul_f32 v27, v27, v53\n"
    "v_mul_f32 v29, v29, v55\n"
    "v_mfma_f32_16x16x32_bf16 v[156:159], a[172:175], v[44:47], v[156:159]\n"
    "ds_read_b64_tr_b16 a[166:167], v30 offset:26880\n"
    "v_cvt_pk_bf16_f32 v22, 0, v22\n"
    "v_cvt_pk_bf16_f32 v24, 0, v24\n"
    "v_mfma_f32_16x16x32_bf16 v[160:163], a[176:179], v[40:43], v[160:163]\n"
    "ds_read_b64_tr_b16 a[168:169], v30 offset:18944\n"
    "ds_read_b128 v[252:255], v20 offset:4496\n"
    "v_cvt_pk_bf16_f32 v26, 0, v26\n"
    "v_cvt_pk_bf16_f32 v28, 0, v28\n"
    "v_mfma_f32_16x16x32_bf16 v[164:167], a[176:179], v[44:47], v[164:167]\n"
    "ds_read_b64_tr_b16 a[170:171], v30 offset:27136\n"
    "v_cvt_pk_bf16_f32 v23, 0, v23\n"
    "v_cvt_pk_bf16_f32 v25, 0, v25\n"
    "v_cvt_pk_bf16_f32 v27, 0, v27\n"
    "v_mfma_f32_16x16x32_bf16 v[168:171], a[180:183], v[40:43], v[168:171]\n"
    "ds_read_b64_tr_b16 a[172:173], v30 offset:19200\n"
    "v_cvt_pk_bf16_f32 v29, 0, v29\n"
    "v_add_f32 v22, v22, v60\n"
    "v_mfma_f32_16x16x32_bf16 v[172:175], a[180:183], v[44:47], v[172:175]\n"
    "ds_read_b64_tr_b16 a[174:175], v30 offset:27392\n"
    "v_add_f32 v24, v24, v62\n"
    "v_add_f32 v26, v26, v60\n"
    "v_mfma_f32_16x16x32_bf16 v[176:179], a[184:187], v[40:43], v[176:179]\n"
    "ds_read_b64_tr_b16 a[176:177], v30 offset:19456\n"
    "v_add_f32 v28, v28, v62\n"
    "v_add_f32 v23, v23, v61\n"
    "v_add_f32 v25, v25, v63\n"
    "v_mfma_f32_16x16x32_bf16 v[180:183], a[184:187], v[44:47], v[180:183]\n"
    "ds_read_b64_tr_b16 a[178:179], v30 offset:27648\n"
    "v_add_f32 v27, v27, v61\n"
    "v_add_f32 v29, v29, v63\n"
    "v_mfma_f32_16x16x32_bf16 v[184:187], a[188:191], v[40:43], v[184:187]\n"
    "ds_read_b64_tr_b16 a[180:181], v30 offset:19712\n"
    "v_cvt_pk_bf16_f32 v34, v22, v23\n"
    "v_cvt_pk_bf16_f32 v35, v24, v25\n"
    "v_mfma_f32_16x16x32_bf16 v[188:191], a[188:191], v[44:47], v[188:191]\n"
    "ds_read_b64_tr_b16 a[182:183], v30 offset:27904\n"
    "ds_read_b64_tr_b16 a[184:185], v30 offset:19968\n"
    "ds_read_b64_tr_b16 a[186:187], v30 offset:28160\n"
    "ds_read_b64_tr_b16 a[188:189], v30 offset:20224\n"
    "ds_read_b64_tr_b16 a[190:191], v30 offset:28416\n"
    "v_cvt_pk_bf16_f32 v38, v26, v27\n"
    "v_cvt_pk_bf16_f32 v39, v28, v29\n"
    "v_mfma_f32_16x16x32_bf16 v[64:67], a[128:131], v[32:35], v[64:67]\n"
    "s_waitcnt vmcnt(8)\n"
    "s_barrier\n"
    "v_lshlrev_b32 v22, 16, v232\n"
    "v_lshlrev_b32 v24, 16, v233\n"
    "v_mfma_f32_16x16x32_bf16 v[68:71], a[128:131], v[36:39], v[68:71]\n"
    "v_lshlrev_b32 v26, 16, v236\n"
    "v_lshlrev_b32 v28, 16, v237\n"
    "v_mfma_f32_16x16x32_bf16 v[72:75], a[132:135], v[32:35], v[72:75]\n"
    "v_and_b32 v23, 0xffff0000, v232\n"
    "v_and_b32 v25, 0xffff0000, v233\n"
    "v_mfma_f32_16x16x32_bf16 v[76:79], a[132:135], v[36:39], v[76:79]\n"
    "s_add_u32 m0, s23, 0x4000\n"
    "s_add_u32 s21, s26, 0x4000\n"
    "buffer_load_dwordx4 v17, s[12:15], s21 offen offset:0 lds\n"
    "v_and_b32 v27, 0xffff0000, v236\n"
    "v_mfma_f32_16x16x32_bf16 v[80:83], a[136:139], v[32:35], v[80:83]\n"
    "ds_read_b64_tr_b16 a[128:129], v30 offset:32768\n"
    "v_and_b32 v29, 0xffff0000, v237\n"
    "v_mul_f32 v22, v22, v240\n"
    "v_mul_f32 v24, v24, v242\n"
    "v_mfma_f32_16x16x32_bf16 v[84:87], a[136:139], v[36:39], v[84:87]\n"
    "ds_read_b64_tr_b16 a[130:131], v30 offset:40960\n"
    "v_mul_f32 v26, v26, v240\n"
    "v_mul_f32 v28, v28, v242\n"
    "v_mfma_f32_16x16x32_bf16 v[88:91], a[140:143], v[32:35], v[88:91]\n"
    "ds_read_b64_tr_b16 a[132:133], v30 offset:33024\n"
    "buffer_load_dwordx4 v18, s[12:15], s21 offen offset:1024 lds\n"
    "v_mul_f32 v23, v23, v241\n"
    "v_mul_f32 v25, v25, v243\n"
    "v_mfma_f32_16x16x32_bf16 v[92:95], a[140:143], v[36:39], v[92:95]\n"
    "ds_read_b64_tr_b16 a[134:135], v30 offset:41216\n"
    "v_mul_f32 v27, v27, v241\n"
    "v_mul_f32 v29, v29, v243\n"
    "v_cvt_pk_bf16_f32 v22, 0, v22\n"
    "s_waitcnt lgkmcnt(4)\n"
    "v_mfma_f32_16x16x32_bf16 v[96:99], a[144:147], v[32:35], v[96:99]\n"
    "ds_read_b64_tr_b16 a[136:137], v30 offset:33280\n"
    "v_cvt_pk_bf16_f32 v24, 0, v24\n"
    "v_cvt_pk_bf16_f32 v26, 0, v26\n"
    "v_mfma_f32_16x16x32_bf16 v[100:103], a[144:147], v[36:39], v[100:103]\n"
    "ds_read_b64_tr_b16 a[138:139], v30 offset:41472\n"
    "buffer_load_dwordx4 v17, s[12:15], s21 offen offset:2048 lds\n"
    "v_cvt_pk_bf16_f32 v28, 0, v28\n"
    "v_cvt_pk_bf16_f32 v23, 0, v23\n"
    "v_mfma_f32_16x16x32_bf16 v[104:107], a[148:151], v[32:35], v[104:107]\n"
    "ds_read_b64_tr_b16 a[140:141], v30 offset:33536\n"
    "v_cvt_pk_bf16_f32 v25, 0, v25\n"
    "v_cvt_pk_bf16_f32 v27, 0, v27\n"
    "v_cvt_pk_bf16_f32 v29, 0, v29\n"
    "v_mfma_f32_16x16x32_bf16 v[108:111], a[148:151], v[36:39], v[108:111]\n"
    "ds_read_b64_tr_b16 a[142:143], v30 offset:41728\n"
    "v_add_f32 v22, v22, v248\n"
    "v_add_f32 v24, v24, v250\n"
    "v_mfma_f32_16x16x32_bf16 v[112:115], a[152:155], v[32:35], v[112:115]\n"
    "ds_read_b64_tr_b16 a[144:145], v30 offset:33792\n"
    "buffer_load_dwordx4 v18, s[12:15], s21 offen offset:3072 lds\n"
    "v_add_f32 v26, v26, v248\n"
    "v_add_f32 v28, v28, v250\n"
    "v_mfma_f32_16x16x32_bf16 v[116:119], a[152:155], v[36:39], v[116:119]\n"
    "ds_read_b64_tr_b16 a[146:147], v30 offset:41984\n"
    "v_add_f32 v23, v23, v249\n"
    "v_add_f32 v25, v25, v251\n"
    "v_add_f32 v27, v27, v249\n"
    "v_mfma_f32_16x16x32_bf16 v[120:123], a[156:159], v[32:35], v[120:123]\n"
    "ds_read_b64_tr_b16 a[148:149], v30 offset:34048\n"
    "v_add_f32 v29, v29, v251\n"
    "v_cvt_pk_bf16_f32 v40, v22, v23\n"
    "v_mfma_f32_16x16x32_bf16 v[124:127], a[156:159], v[36:39], v[124:127]\n"
    "ds_read_b64_tr_b16 a[150:151], v30 offset:42240\n"
    "v_cvt_pk_bf16_f32 v41, v24, v25\n"
    "v_cvt_pk_bf16_f32 v44, v26, v27\n"
    "v_mfma_f32_16x16x32_bf16 v[128:131], a[160:163], v[32:35], v[128:131]\n"
    "ds_read_b64_tr_b16 a[152:153], v30 offset:34304\n"
    "v_cvt_pk_bf16_f32 v45, v28, v29\n"
    "v_lshlrev_b32 v22, 16, v234\n"
    "v_lshlrev_b32 v24, 16, v235\n"
    "v_mfma_f32_16x16x32_bf16 v[132:135], a[160:163], v[36:39], v[132:135]\n"
    "ds_read_b64_tr_b16 a[154:155], v30 offset:42496\n"
    "v_lshlrev_b32 v26, 16, v238\n"
    "v_lshlrev_b32 v28, 16, v239\n"
    "v_mfma_f32_16x16x32_bf16 v[136:139], a[164:167], v[32:35], v[136:139]\n"
    "ds_read_b64_tr_b16 a[156:157], v30 offset:34560\n"
    "v_and_b32 v23, 0xffff0000, v234\n"
    "v_and_b32 v25, 0xffff0000, v235\n"
    "v_mfma_f32_16x16x32_bf16 v[140:143], a[164:167], v[36:39], v[140:143]\n"
    "ds_read_b64_tr_b16 a[158:159], v30 offset:42752\n"
    "v_and_b32 v27, 0xffff0000, v238\n"
    "v_and_b32 v29, 0xffff0000, v239\n"
    "v_mul_f32 v22, v22, v244\n"
    "v_mfma_f32_16x16x32_bf16 v[144:147], a[168:171], v[32:35], v[144:147]\n"
    "ds_read_b64_tr_b16 a[160:161], v30 offset:34816\n"
    "v_mul_f32 v24, v24, v246\n"
    "v_mul_f32 v26, v26, v244\n"
    "v_mfma_f32_16x16x32_bf16 v[148:151], a[168:171], v[36:39], v[148:151]\n"
    "ds_read_b64_tr_b16 a[162:163], v30 offset:43008\n"
    "v_mul_f32 v28, v28, v246\n"
    "v_mul_f32 v23, v23, v245\n"
    "v_mfma_f32_16x16x32_bf16 v[152:155], a[172:175], v[32:35], v[152:155]\n"
    "ds_read_b64_tr_b16 a[164:165], v30 offset:35072\n"
    "v_mul_f32 v25, v25, v247\n"
    "v_mul_f32 v27, v27, v245\n"
    "v_mul_f32 v29, v29, v247\n"
    "v_mfma_f32_16x16x32_bf16 v[156:159], a[172:175], v[36:39], v[156:159]\n"
    "ds_read_b64_tr_b16 a[166:167], v30 offset:43264\n"
    "v_cvt_pk_bf16_f32 v22, 0, v22\n"
    "v_cvt_pk_bf16_f32 v24, 0, v24\n"
    "v_mfma_f32_16x16x32_bf16 v[160:163], a[176:179], v[32:35], v[160:163]\n"
    "ds_read_b64_tr_b16 a[168:169], v30 offset:35328\n"
    "v_cvt_pk_bf16_f32 v26, 0, v26\n"
    "v_cvt_pk_bf16_f32 v28, 0, v28\n"
    "v_mfma_f32_16x16x32_bf16 v[164:167], a[176:179], v[36:39], v[164:167]\n"
    "ds_read_b64_tr_b16 a[170:171], v30 offset:43520\n"
    "v_cvt_pk_bf16_f32 v23, 0, v23\n"
    "v_cvt_pk_bf16_f32 v25, 0, v25\n"
    "v_cvt_pk_bf16_f32 v27, 0, v27\n"
    "v_mfma_f32_16x16x32_bf16 v[168:171], a[180:183], v[32:35], v[168:171]\n"
    "ds_read_b64_tr_b16 a[172:173], v30 offset:35584\n"
    "v_cvt_pk_bf16_f32 v29, 0, v29\n"
    "v_add_f32 v22, v22, v252\n"
    "v_mfma_f32_16x16x32_bf16 v[172:175], a[180:183], v[36:39], v[172:175]\n"
    "ds_read_b64_tr_b16 a[174:175], v30 offset:43776\n"
    "v_add_f32 v24, v24, v254\n"
    "v_add_f32 v26, v26, v252\n"
    "v_mfma_f32_16x16x32_bf16 v[176:179], a[184:187], v[32:35], v[176:179]\n"
    "ds_read_b64_tr_b16 a[176:177], v30 offset:35840\n"
    "v_add_f32 v28, v28, v254\n"
    "v_add_f32 v23, v23, v253\n"
    "v_add_f32 v25, v25, v255\n"
    "v_mfma_f32_16x16x32_bf16 v[180:183], a[184:187], v[36:39], v[180:183]\n"
    "ds_read_b64_tr_b16 a[178:179], v30 offset:44032\n"
    "v_add_f32 v27, v27, v253\n"
    "v_add_f32 v29, v29, v255\n"
    "v_mfma_f32_16x16x32_bf16 v[184:187], a[188:191], v[32:35], v[184:187]\n"
    "ds_read_b64_tr_b16 a[180:181], v30 offset:36096\n"
    "v_cvt_pk_bf16_f32 v42, v22, v23\n"
    "v_cvt_pk_bf16_f32 v43, v24, v25\n"
    "v_mfma_f32_16x16x32_bf16 v[188:191], a[188:191], v[36:39], v[188:191]\n"
    "ds_read_b64_tr_b16 a[182:183], v30 offset:44288\n"
    "ds_read_b64_tr_b16 a[184:185], v30 offset:36352\n"
    "ds_read_b64_tr_b16 a[186:187], v30 offset:44544\n"
    "ds_read_b64_tr_b16 a[188:189], v30 offset:36608\n"
    "ds_read_b64_tr_b16 a[190:191], v30 offset:44800\n"
    "v_cvt_pk_bf16_f32 v46, v26, v27\n"
    "v_cvt_pk_bf16_f32 v47, v28, v29\n"
    "s_waitcnt lgkmcnt(11)\n"
    "v_mfma_f32_16x16x32_bf16 v[64:67], a[128:131], v[40:43], v[64:67]\n"
    "s_waitcnt vmcnt(32)\n"
    "s_barrier\n"
    "v_mfma_f32_16x16x32_bf16 v[68:71], a[128:131], v[44:47], v[68:71]\n"
    "ds_read_b128 a[128:131], v4 offset:0\n"
    "v_mfma_f32_16x16x32_bf16 v[72:75], a[132:135], v[40:43], v[72:75]\n"
    "v_mfma_f32_16x16x32_bf16 v[76:79], a[132:135], v[44:47], v[76:79]\n"
    "ds_read_b128 a[132:135], v5 offset:0\n"
    "v_mfma_f32_16x16x32_bf16 v[80:83], a[136:139], v[40:43], v[80:83]\n"
    "v_mfma_f32_16x16x32_bf16 v[84:87], a[136:139], v[44:47], v[84:87]\n"
    "ds_read_b128 a[136:139], v6 offset:0\n"
    "v_mfma_f32_16x16x32_bf16 v[88:91], a[140:143], v[40:43], v[88:91]\n"
    "v_mfma_f32_16x16x32_bf16 v[92:95], a[140:143], v[44:47], v[92:95]\n"
    "ds_read_b128 a[140:143], v7 offset:0\n"
    "v_mfma_f32_16x16x32_bf16 v[96:99], a[144:147], v[40:43], v[96:99]\n"
    "v_mfma_f32_16x16x32_bf16 v[100:103], a[144:147], v[44:47], v[100:103]\n"
    "ds_read_b128 a[144:147], v4 offset:4096\n"
    "v_mfma_f32_16x16x32_bf16 v[104:107], a[148:151], v[40:43], v[104:107]\n"
    "v_mfma_f32_16x16x32_bf16 v[108:111], a[148:151], v[44:47], v[108:111]\n"
    "ds_read_b128 a[148:151], v5 offset:4096\n"
    "v_mfma_f32_16x16x32_bf16 v[112:115], a[152:155], v[40:43], v[112:115]\n"
    "v_mfma_f32_16x16x32_bf16 v[116:119], a[152:155], v[44:47], v[116:119]\n"
    "ds_read_b128 a[152:155], v6 offset:4096\n"
    "v_mfma_f32_16x16x32_bf16 v[120:123], a[156:159], v[40:43], v[120:123]\n"
    "v_mfma_f32_16x16x32_bf16 v[124:127], a[156:159], v[44:47], v[124:127]\n"
    "ds_read_b128 a[156:159], v7 offset:4096\n"
    "v_mfma_f32_16x16x32_bf16 v[128:131], a[160:163], v[40:43], v[128:131]\n"
    "v_mfma_f32_16x16x32_bf16 v[132:135], a[160:163], v[44:47], v[132:135]\n"
    "v_mfma_f32_16x16x32_bf16 v[136:139], a[164:167], v[40:43], v[136:139]\n"
    "v_mfma_f32_16x16x32_bf16 v[140:143], a[164:167], v[44:47], v[140:143]\n"
    "s_waitcnt lgkmcnt(11)\n"
    "v_mfma_f32_16x16x32_bf16 v[144:147], a[168:171], v[40:43], v[144:147]\n"
    "v_mfma_f32_16x16x32_bf16 v[148:151], a[168:171], v[44:47], v[148:151]\n"
    "v_mfma_f32_16x16x32_bf16 v[152:155], a[172:175], v[40:43], v[152:155]\n"
    "v_mfma_f32_16x16x32_bf16 v[156:159], a[172:175], v[44:47], v[156:159]\n"
    "v_mfma_f32_16x16x32_bf16 v[160:163], a[176:179], v[40:43], v[160:163]\n"
    "v_mfma_f32_16x16x32_bf16 v[164:167], a[176:179], v[44:47], v[164:167]\n"
    "v_mfma_f32_16x16x32_bf16 v[168:171], a[180:183], v[40:43], v[168:171]\n"
    "v_mfma_f32_16x16x32_bf16 v[172:175], a[180:183], v[44:47], v[172:175]\n"
    "s_waitcnt lgkmcnt(8)\n"
    "v_mfma_f32_16x16x32_bf16 v[176:179], a[184:187], v[40:43], v[176:179]\n"
    "v_mfma_f32_16x16x32_bf16 v[180:183], a[184:187], v[44:47], v[180:183]\n"
    "v_mfma_f32_16x16x32_bf16 v[184:187], a[188:191], v[40:43], v[184:187]\n"
    "v_mfma_f32_16x16x32_bf16 v[188:191], a[188:191], v[44:47], v[188:191]\n"
    "s_nop 7\n"
    "v_cvt_pk_bf16_f32 v192, v64, v65\n"
    "v_cvt_pk_bf16_f32 v193, v66, v67\n"
    "v_cvt_pk_bf16_f32 v194, v72, v73\n"
    "v_cvt_pk_bf16_f32 v195, v74, v75\n"
    "v_cvt_pk_bf16_f32 v196, v80, v81\n"
    "v_cvt_pk_bf16_f32 v197, v82, v83\n"
    "v_cvt_pk_bf16_f32 v198, v88, v89\n"
    "v_cvt_pk_bf16_f32 v199, v90, v91\n"
    "v_permlane32_swap_b32 v192, v194\n"
    "v_permlane32_swap_b32 v193, v195\n"
    "v_cvt_pk_bf16_f32 v208, v68, v69\n"
    "v_cvt_pk_bf16_f32 v209, v70, v71\n"
    "v_cvt_pk_bf16_f32 v210, v76, v77\n"
    "v_cvt_pk_bf16_f32 v211, v78, v79\n"
    "v_permlane32_swap_b32 v196, v198\n"
    "v_permlane32_swap_b32 v197, v199\n"
    "v_permlane16_swap_b32 v192, v194\n"
    "v_permlane16_swap_b32 v193, v195\n"
    "v_cvt_pk_bf16_f32 v212, v84, v85\n"
    "v_cvt_pk_bf16_f32 v213, v86, v87\n"
    "v_cvt_pk_bf16_f32 v214, v92, v93\n"
    "v_cvt_pk_bf16_f32 v215, v94, v95\n"
    "v_permlane32_swap_b32 v208, v210\n"
    "v_permlane32_swap_b32 v209, v211\n"
    "v_permlane16_swap_b32 v196, v198\n"
    "v_permlane16_swap_b32 v197, v199\n"
    "v_cvt_pk_bf16_f32 v200, v96, v97\n"
    "v_cvt_pk_bf16_f32 v201, v98, v99\n"
    "v_cvt_pk_bf16_f32 v202, v104, v105\n"
    "v_cvt_pk_bf16_f32 v203, v106, v107\n"
    "v_permlane32_swap_b32 v212, v214\n"
    "v_permlane32_swap_b32 v213, v215\n"
    "v_permlane16_swap_b32 v208, v210\n"
    "v_permlane16_swap_b32 v209, v211\n"
    "v_cvt_pk_bf16_f32 v204, v112, v113\n"
    "v_cvt_pk_bf16_f32 v205, v114, v115\n"
    "v_cvt_pk_bf16_f32 v206, v120, v121\n"
    "s_waitcnt lgkmcnt(0)\n"
    "v_mfma_f32_16x16x32_bf16 v[64:67], a[128:131], v[192:195], 0\n"
    "v_cvt_pk_bf16_f32 v207, v122, v123\n"
    "v_permlane32_swap_b32 v200, v202\n"
    "v_permlane32_swap_b32 v201, v203\n"
    "v_mfma_f32_16x16x32_bf16 v[68:71], a[144:147], v[192:195], 0\n"
    "v_permlane16_swap_b32 v212, v214\n"
    "v_permlane16_swap_b32 v213, v215\n"
    "v_mfma_f32_16x16x32_bf16 v[64:67], a[132:135], v[196:199], v[64:67]\n"
    "v_cvt_pk_bf16_f32 v216, v100, v101\n"
    "v_cvt_pk_bf16_f32 v217, v102, v103\n"
    "v_cvt_pk_bf16_f32 v218, v108, v109\n"
    "v_mfma_f32_16x16x32_bf16 v[68:71], a[148:151], v[196:199], v[68:71]\n"
    "v_cvt_pk_bf16_f32 v219, v110, v111\n"
    "v_permlane32_swap_b32 v204, v206\n"
    "v_permlane32_swap_b32 v205, v207\n"
    "v_mfma_f32_16x16x32_bf16 v[72:75], a[128:131], v[208:211], 0\n"
    "v_permlane16_swap_b32 v200, v202\n"
    "v_permlane16_swap_b32 v201, v203\n"
    "v_mfma_f32_16x16x32_bf16 v[76:79], a[144:147], v[208:211], 0\n"
    "v_cvt_pk_bf16_f32 v220, v116, v117\n"
    "v_cvt_pk_bf16_f32 v221, v118, v119\n"
    "v_cvt_pk_bf16_f32 v222, v124, v125\n"
    "v_mfma_f32_16x16x32_bf16 v[72:75], a[132:135], v[212:215], v[72:75]\n"
    "v_cvt_pk_bf16_f32 v223, v126, v127\n"
    "v_permlane32_swap_b32 v216, v218\n"
    "v_permlane32_swap_b32 v217, v219\n"
    "v_mfma_f32_16x16x32_bf16 v[76:79], a[148:151], v[212:215], v[76:79]\n"
    "v_permlane16_swap_b32 v204, v206\n"
    "v_permlane16_swap_b32 v205, v207\n"
    "v_mfma_f32_16x16x32_bf16 v[64:67], a[136:139], v[200:203], v[64:67]\n"
    "v_cvt_pk_bf16_f32 v224, v128, v129\n"
    "v_cvt_pk_bf16_f32 v225, v130, v131\n"
    "v_cvt_pk_bf16_f32 v226, v136, v137\n"
    "v_mfma_f32_16x16x32_bf16 v[68:71], a[152:155], v[200:203], v[68:71]\n"
    "v_cvt_pk_bf16_f32 v227, v138, v139\n"
    "v_permlane32_swap_b32 v220, v222\n"
    "v_permlane32_swap_b32 v221, v223\n"
    "v_mfma_f32_16x16x32_bf16 v[64:67], a[140:143], v[204:207], v[64:67]\n"
    "v_permlane16_swap_b32 v216, v218\n"
    "v_permlane16_swap_b32 v217, v219\n"
    "v_mfma_f32_16x16x32_bf16 v[68:71], a[156:159], v[204:207], v[68:71]\n"
    "v_cvt_pk_bf16_f32 v228, v144, v145\n"
    "v_cvt_pk_bf16_f32 v229, v146, v147\n"
    "v_cvt_pk_bf16_f32 v230, v152, v153\n"
    "v_mfma_f32_16x16x32_bf16 v[72:75], a[136:139], v[216:219], v[72:75]\n"
    "v_cvt_pk_bf16_f32 v231, v154, v155\n"
    "v_permlane32_swap_b32 v224, v226\n"
    "v_permlane32_swap_b32 v225, v227\n"
    "v_mfma_f32_16x16x32_bf16 v[76:79], a[152:155], v[216:219], v[76:79]\n"
    "v_permlane16_swap_b32 v220, v222\n"
    "v_permlane16_swap_b32 v221, v223\n"
    "v_cvt_pk_bf16_f32 v232, v160, v161\n"
    "v_cvt_pk_bf16_f32 v233, v162, v163\n"
    "v_cvt_pk_bf16_f32 v234, v168, v169\n"
    "v_mfma_f32_16x16x32_bf16 v[72:75], a[140:143], v[220:223], v[72:75]\n"
    "v_cvt_pk_bf16_f32 v235, v170, v171\n"
    "v_permlane32_swap_b32 v228, v230\n"
    "v_permlane32_swap_b32 v229, v231\n"
    "v_mfma_f32_16x16x32_bf16 v[76:79], a[156:159], v[220:223], v[76:79]\n"
    "v_permlane16_swap_b32 v224, v226\n"
    "v_permlane16_swap_b32 v225, v227\n"
    "v_cvt_pk_bf16_f32 v236, v176, v177\n"
    "v_cvt_pk_bf16_f32 v237, v178, v179\n"
    "v_cvt_pk_bf16_f32 v238, v184, v185\n"
    "v_mfma_f32_16x16x32_bf16 v[80:83], a[128:131], v[224:227], 0\n"
    "v_cvt_pk_bf16_f32 v239, v186, v187\n"
    "v_permlane32_swap_b32 v232, v234\n"
    "v_permlane32_swap_b32 v233, v235\n"
    "v_mfma_f32_16x16x32_bf16 v[84:87], a[144:147], v[224:227], 0\n"
    "v_permlane16_swap_b32 v228, v230\n"
    "v_permlane16_swap_b32 v229, v231\n"
    "v_cvt_pk_bf16_f32 v240, v132, v133\n"
    "v_cvt_pk_bf16_f32 v241, v134, v135\n"
    "v_cvt_pk_bf16_f32 v242, v140, v141\n"
    "v_mfma_f32_16x16x32_bf16 v[80:83], a[132:135], v[228:231], v[80:83]\n"
    "v_cvt_pk_bf16_f32 v243, v142, v143\n"
    "v_permlane32_swap_b32 v236, v238\n"
    "v_permlane32_swap_b32 v237, v239\n"
    "v_mfma_f32_16x16x32_bf16 v[84:87], a[148:151], v[228:231], v[84:87]\n"
    "v_permlane16_swap_b32 v232, v234\n"
    "v_permlane16_swap_b32 v233, v235\n"
    "v_cvt_pk_bf16_f32 v244, v148, v149\n"
    "v_cvt_pk_bf16_f32 v245, v150, v151\n"
    "v_cvt_pk_bf16_f32 v246, v156, v157\n"
    "v_mfma_f32_16x16x32_bf16 v[80:83], a[136:139], v[232:235], v[80:83]\n"
    "v_cvt_pk_bf16_f32 v247, v158, v159\n"
    "v_permlane32_swap_b32 v240, v242\n"
    "v_permlane32_swap_b32 v241, v243\n"
    "v_mfma_f32_16x16x32_bf16 v[84:87], a[152:155], v[232:235], v[84:87]\n"
    "v_permlane16_swap_b32 v236, v238\n"
    "v_permlane16_swap_b32 v237, v239\n"
    "v_cvt_pk_bf16_f32 v248, v164, v165\n"
    "v_cvt_pk_bf16_f32 v249, v166, v167\n"
    "v_cvt_pk_bf16_f32 v250, v172, v173\n"
    "v_mfma_f32_16x16x32_bf16 v[80:83], a[140:143], v[236:239], v[80:83]\n"
    "v_cvt_pk_bf16_f32 v251, v174, v175\n"
    "v_permlane32_swap_b32 v244, v246\n"
    "v_permlane32_swap_b32 v245, v247\n"
    "v_mfma_f32_16x16x32_bf16 v[84:87], a[156:159], v[236:239], v[84:87]\n"
    "v_permlane16_swap_b32 v240, v242\n"
    "v_permlane16_swap_b32 v241, v243\n"
    "v_cvt_pk_bf16_f32 v252, v180, v181\n"
    "v_cvt_pk_bf16_f32 v253, v182, v183\n"
    "v_cvt_pk_bf16_f32 v254, v188, v189\n"
    "v_mfma_f32_16x16x32_bf16 v[88:91], a[128:131], v[240:243], 0\n"
    "v_cvt_pk_bf16_f32 v255, v190, v191\n"
    "v_permlane32_swap_b32 v248, v250\n"
    "v_permlane32_swap_b32 v249, v251\n"
    "v_mfma_f32_16x16x32_bf16 v[92:95], a[144:147], v[240:243], 0\n"
    "v_permlane16_swap_b32 v244, v246\n"
    "v_permlane16_swap_b32 v245, v247\n"
    "v_permlane32_swap_b32 v252, v254\n"
    "v_permlane32_swap_b32 v253, v255\n"
    "v_permlane16_swap_b32 v248, v250\n"
    "v_mfma_f32_16x16x32_bf16 v[88:91], a[132:135], v[244:247], v[88:91]\n"
    "v_permlane16_swap_b32 v249, v251\n"
    "v_mfma_f32_16x16x32_bf16 v[92:95], a[148:151], v[244:247], v[92:95]\n"
    "v_permlane16_swap_b32 v252, v254\n"
    "v_permlane16_swap_b32 v253, v255\n"
    "v_mfma_f32_16x16x32_bf16 v[88:91], a[136:139], v[248:251], v[88:91]\n"
    "v_mfma_f32_16x16x32_bf16 v[92:95], a[152:155], v[248:251], v[92:95]\n"
    "v_mfma_f32_16x16x32_bf16 v[88:91], a[140:143], v[252:255], v[88:91]\n"
    "v_mfma_f32_16x16x32_bf16 v[92:95], a[156:159], v[252:255], v[92:95]\n"
    "v_mov_b32 v20, v2\n"
    "v_mov_b32 v21, v3\n"
    "v_mov_b32 v22, v31\n"
    "v_lshlrev_b32 v23, 7, v1\n"
    "s_mov_b32 s29, 0\n"
    "s_mov_b32 s99, 0x20000\n"
    "s_add_u32 s20, s26, 0xc000\n"
    "s_add_u32 s21, s26, 0x8000\n"
    "s_mov_b32 s56, 0x3db504f3\n"
    "s_mov_b32 s57, 0x3e0293ee\n"
    "ds_read_b128 a[128:131], v4 offset:8192\n"
    "ds_read_b128 a[132:135], v5 offset:8192\n"
    "ds_read_b128 a[136:139], v6 offset:8192\n"
    "ds_read_b128 a[140:143], v7 offset:8192\n"
    "ds_read_b128 a[144:147], v4 offset:12288\n"
    "ds_read_b128 a[148:151], v5 offset:12288\n"
    "ds_read_b128 a[152:155], v6 offset:12288\n"
    "ds_read_b128 a[156:159], v7 offset:12288\n"
    "s_nop 15\n"
    "v_cvt_pk_bf16_f32 v64, 0, v64\n"
    "v_cvt_pk_bf16_f32 v65, 0, v65\n"
    "v_cvt_pk_bf16_f32 v66, 0, v66\n"
    "v_cvt_pk_bf16_f32 v67, 0, v67\n"
    "v_cvt_pk_bf16_f32 v68, 0, v68\n"
    "v_cvt_pk_bf16_f32 v69, 0, v69\n"
    "v_cvt_pk_bf16_f32 v70, 0, v70\n"
    "v_cvt_pk_bf16_f32 v71, 0, v71\n"
    "v_cvt_pk_bf16_f32 v72, 0, v72\n"
    "v_cvt_pk_bf16_f32 v73, 0, v73\n"
    "v_cvt_pk_bf16_f32 v74, 0, v74\n"
    "v_cvt_pk_bf16_f32 v75, 0, v75\n"
    "v_cvt_pk_bf16_f32 v76, 0, v76\n"
    "v_cvt_pk_bf16_f32 v77, 0, v77\n"
    "v_cvt_pk_bf16_f32 v78, 0, v78\n"
    "v_cvt_pk_bf16_f32 v79, 0, v79\n"
    "v_cvt_pk_bf16_f32 v80, 0, v80\n"
    "v_cvt_pk_bf16_f32 v81, 0, v81\n"
    "v_cvt_pk_bf16_f32 v82, 0, v82\n"
    "v_cvt_pk_bf16_f32 v83, 0, v83\n"
    "v_cvt_pk_bf16_f32 v84, 0, v84\n"
    "v_cvt_pk_bf16_f32 v85, 0, v85\n"
    "v_cvt_pk_bf16_f32 v86, 0, v86\n"
    "v_cvt_pk_bf16_f32 v87, 0, v87\n"
    "v_cvt_pk_bf16_f32 v88, 0, v88\n"
    "v_cvt_pk_bf16_f32 v89, 0, v89\n"
    "v_cvt_pk_bf16_f32 v90, 0, v90\n"
    "v_cvt_pk_bf16_f32 v91, 0, v91\n"
    "v_cvt_pk_bf16_f32 v92, 0, v92\n"
    "v_cvt_pk_bf16_f32 v93, 0, v93\n"
    "v_cvt_pk_bf16_f32 v94, 0, v94\n"
    "v_cvt_pk_bf16_f32 v95, 0, v95\n"
    "v_max3_f32 v34, v64, v65, v66\n"
    "v_max3_f32 v34, v34, v67, v68\n"
    "v_max3_f32 v34, v34, v69, v70\n"
    "v_max_f32 v34, v34, v71\n"
    "v_mov_b32 v35, v34\n"
    "s_nop 1\n"
    "v_permlane16_swap_b32 v34, v35\n"
    "s_nop 0\n"
    "v_max_f32 v34, v34, v35\n"
    "v_mov_b32 v35, v34\n"
    "s_nop 1\n"
    "v_permlane32_swap_b32 v34, v35\n"
    "s_nop 0\n"
    "v_max_f32 v34, v34, v35\n"
    "v_mul_f32 v34, s57, v34\n"
    "v_add_f32 v28, s41, v34\n"
    "v_sub_f32 v160, 0, v28\n"
    "v_sub_f32 v161, 0, v28\n"
    "v_max3_f32 v34, v72, v73, v74\n"
    "v_max3_f32 v34, v34, v75, v76\n"
    "v_max3_f32 v34, v34, v77, v78\n"
    "v_max_f32 v34, v34, v79\n"
    "v_mov_b32 v35, v34\n"
    "s_nop 1\n"
    "v_permlane16_swap_b32 v34, v35\n"
    "s_nop 0\n"
    "v_max_f32 v34, v34, v35\n"
    "v_mov_b32 v35, v34\n"
    "s_nop 1\n"
    "v_permlane32_swap_b32 v34, v35\n"
    "s_nop 0\n"
    "v_max_f32 v34, v34, v35\n"
    "v_mul_f32 v34, s57, v34\n"
    "v_add_f32 v29, s41, v34\n"
    "v_sub_f32 v162, 0, v29\n"
    "v_sub_f32 v163, 0, v29\n"
    "v_max3_f32 v34, v80, v81, v82\n"
    "v_max3_f32 v34, v34, v83, v84\n"
    "v_max3_f32 v34, v34, v85, v86\n"
    "v_max_f32 v34, v34, v87\n"
    "v_mov_b32 v35, v34\n"
    "s_nop 1\n"
    "v_permlane16_swap_b32 v34, v35\n"
    "s_nop 0\n"
    "v_max_f32 v34, v34, v35\n"
    "v_mov_b32 v35, v34\n"
    "s_nop 1\n"
    "v_permlane32_swap_b32 v34, v35\n"
    "s_nop 0\n"
    "v_max_f32 v34, v34, v35\n"
    "v_mul_f32 v34, s57, v34\n"
    "v_add_f32 v30, s41, v34\n"
    "v_sub_f32 v164, 0, v30\n"
    "v_sub_f32 v165, 0, v30\n"
    "v_max3_f32 v34, v88, v89, v90\n"
    "v_max3_f32 v34, v34, v91, v92\n"
    "v_max3_f32 v34, v34, v93, v94\n"
    "v_max_f32 v34, v34, v95\n"
    "v_mov_b32 v35, v34\n"
    "s_nop 1\n"
    "v_permlane16_swap_b32 v34, v35\n"
    "s_nop 0\n"
    "v_max_f32 v34, v34, v35\n"
    "v_mov_b32 v35, v34\n"
    "s_nop 1\n"
    "v_permlane32_swap_b32 v34, v35\n"
    "s_nop 0\n"
    "v_max_f32 v34, v34, v35\n"
    "v_mul_f32 v34, s57, v34\n"
    "v_add_f32 v31, s41, v34\n"
    "v_sub_f32 v166, 0, v31\n"
    "v_sub_f32 v167, 0, v31\n"
    "v_mov_b32 v34, 0x3f803f80\n"
    "v_accvgpr_write_b32 a224, v34\n"
    "v_accvgpr_write_b32 a225, v34\n"
    "v_accvgpr_write_b32 a226, v34\n"
    "v_accvgpr_write_b32 a227, v34\n"
    "s_waitcnt lgkmcnt(0)\n"
    "v_mfma_f32_16x16x32_bf16 v[96:99], a[128:131], v[192:195], 0\n"
    "s_waitcnt vmcnt(8)\n"
    "s_barrier\n"
    "v_fma_f32 v64, v64, s57, v160\n"
    "v_mfma_f32_16x16x32_bf16 v[104:107], a[128:131], v[208:211], 0\n"
    "v_fma_f32 v65, v65, s57, v160\n"
    "v_exp_f32 v64, v64\n"
    "v_mfma_f32_16x16x32_bf16 v[112:115], a[128:131], v[224:227], 0\n"
    "v_exp_f32 v65, v65\n"
    "v_mfma_f32_16x16x32_bf16 v[120:123], a[128:131], v[240:243], 0\n"
    "v_fma_f32 v66, v66, s57, v160\n"
    "v_exp_f32 v66, v66\n"
    "ds_read_b128 a[128:131], v4 offset:16384\n"
    "v_cvt_pk_bf16_f32 v128, v64, v65\n"
    "v_fma_f32 v67, v67, s57, v160\n"
    "v_fma_f32 v68, v68, s57, v160\n"
    "v_exp_f32 v67, v67\n"
    "v_exp_f32 v68, v68\n"
    "v_cvt_pk_bf16_f32 v129, v66, v67\n"
    "v_mfma_f32_16x16x32_bf16 v[96:99], a[132:135], v[196:199], v[96:99]\n"
    "v_fma_f32 v69, v69, s57, v160\n"
    "v_mfma_f32_16x16x32_bf16 v[104:107], a[132:135], v[212:215], v[104:107]\n"
    "v_exp_f32 v69, v69\n"
    "v_mfma_f32_16x16x32_bf16 v[112:115], a[132:135], v[228:231], v[112:115]\n"
    "v_fma_f32 v70, v70, s57, v160\n"
    "v_exp_f32 v70, v70\n"
    "v_mfma_f32_16x16x32_bf16 v[120:123], a[132:135], v[244:247], v[120:123]\n"
    "v_fma_f32 v71, v71, s57, v160\n"
    "v_exp_f32 v71, v71\n"
    "v_cvt_pk_bf16_f32 v130, v68, v69\n"
    "v_cvt_pk_bf16_f32 v131, v70, v71\n"
    "ds_read_b64_tr_b16 a[160:161], v12 offset:0\n"
    "v_fma_f32 v72, v72, s57, v162\n"
    "ds_read_b64_tr_b16 a[162:163], v12 offset:4096\n"
    "v_fma_f32 v73, v73, s57, v162\n"
    "ds_read_b128 a[132:135], v5 offset:16384\n"
    "v_fma_f32 v74, v74, s57, v162\n"
    "v_mfma_f32_16x16x32_bf16 v[96:99], a[136:139], v[200:203], v[96:99]\n"
    "ds_read_b64_tr_b16 a[164:165], v12 offset:256\n"
    "v_exp_f32 v72, v72\n"
    "v_mfma_f32_16x16x32_bf16 v[104:107], a[136:139], v[216:219], v[104:107]\n"
    "v_fma_f32 v75, v75, s57, v162\n"
    "v_mfma_f32_16x16x32_bf16 v[176:179], a[224:227], v[128:131], 0\n"
    "ds_read_b64_tr_b16 a[166:167], v12 offset:4352\n"
    "v_exp_f32 v73, v73\n"
    "v_mfma_f32_16x16x32_bf16 v[112:115], a[136:139], v[232:235], v[112:115]\n"
    "s_add_u32 m0, s22, 0xc000\n"
    "v_fma_f32 v76, v76, s57, v162\n"
    "v_exp_f32 v74, v74\n"
    "v_mfma_f32_16x16x32_bf16 v[120:123], a[136:139], v[248:251], v[120:123]\n"
    "buffer_load_dwordx4 v13, s[8:11], s20 offen offset:0 lds\n"
    "v_cvt_pk_bf16_f32 v132, v72, v73\n"
    "v_fma_f32 v77, v77, s57, v162\n"
    "v_exp_f32 v75, v75\n"
    "v_exp_f32 v76, v76\n"
    "v_fma_f32 v78, v78, s57, v162\n"
    "v_cvt_pk_bf16_f32 v133, v74, v75\n"
    "v_exp_f32 v77, v77\n"
    "v_mfma_f32_16x16x32_bf16 v[96:99], a[140:143], v[204:207], v[96:99]\n"
    "v_fma_f32 v79, v79, s57, v162\n"
    "v_exp_f32 v78, v78\n"
    "v_mfma_f32_16x16x32_bf16 v[104:107], a[140:143], v[220:223], v[104:107]\n"
    "s_waitcnt lgkmcnt(0)\n"
    "v_exp_f32 v79, v79\n"
    "v_mfma_f32_16x16x32_bf16 v[112:115], a[140:143], v[236:239], v[112:115]\n"
    "v_cvt_pk_bf16_f32 v134, v76, v77\n"
    "v_cvt_pk_bf16_f32 v135, v78, v79\n"
    "v_mfma_f32_16x16x32_bf16 v[120:123], a[140:143], v[252:255], v[120:123]\n"
    "buffer_load_dwordx4 v14, s[8:11], s20 offen offset:1024 lds\n"
    "v_fma_f32 v80, v80, s57, v164\n"
    "ds_read_b128 a[136:139], v6 offset:16384\n"
    "v_fma_f32 v81, v81, s57, v164\n"
    "v_fma_f32 v82, v82, s57, v164\n"
    "ds_read_b64_tr_b16 a[168:169], v12 offset:512\n"
    "ds_read_b64_tr_b16 a[170:171], v12 offset:4608\n"
    "v_fma_f32 v83, v83, s57, v164\n"
    "v_mfma_f32_16x16x32_bf16 v[180:183], a[224:227], v[132:135], 0\n"
    "ds_read_b128 a[140:143], v7 offset:16384\n"
    "v_exp_f32 v80, v80\n"
    "v_cvt_pk_bf16_f32 v96, 0, v96\n"
    "v_fma_f32 v84, v84, s57, v164\n"
    "v_cvt_pk_bf16_f32 v97, 0, v97\n"
    "v_mfma_f32_16x16x32_bf16 v[100:103], a[144:147], v[192:195], 0\n"
    "v_exp_f32 v81, v81\n"
    "v_mfma_f32_16x16x32_bf16 v[108:111], a[144:147], v[208:211], 0\n"
    "v_fma_f32 v85, v85, s57, v164\n"
    "v_cvt_pk_bf16_f32 v136, v80, v81\n"
    "v_mfma_f32_16x16x32_bf16 v[116:119], a[144:147], v[224:227], 0\n"
    "buffer_load_dwordx4 v15, s[8:11], s20 offen offset:2048 lds\n"
    "v_exp_f32 v82, v82\n"
    "v_mfma_f32_16x16x32_bf16 v[124:127], a[144:147], v[240:243], 0\n"
    "v_fma_f32 v86, v86, s57, v164\n"
    "v_cvt_pk_bf16_f32 v98, 0, v98\n"
    "v_exp_f32 v83, v83\n"
    "v_fma_f32 v87, v87, s57, v164\n"
    "v_cvt_pk_bf16_f32 v137, v82, v83\n"
    "v_exp_f32 v84, v84\n"
    "v_fma_f32 v88, v88, s57, v166\n"
    "v_exp_f32 v85, v85\n"
    "v_mfma_f32_16x16x32_bf16 v[100:103], a[148:151], v[196:199], v[100:103]\n"
    "v_exp_f32 v86, v86\n"
    "v_exp_f32 v87, v87\n"
    "v_mfma_f32_16x16x32_bf16 v[108:111], a[148:151], v[212:215], v[108:111]\n"
    "ds_read_b64_tr_b16 a[172:173], v12 offset:768\n"
    "v_mfma_f32_16x16x32_bf16 v[116:119], a[148:151], v[228:231], v[116:119]\n"
    "v_cvt_pk_bf16_f32 v138, v84, v85\n"
    "v_cvt_pk_bf16_f32 v139, v86, v87\n"
    "v_mfma_f32_16x16x32_bf16 v[124:127], a[148:151], v[244:247], v[124:127]\n"
    "ds_read_b64_tr_b16 a[174:175], v12 offset:4864\n"
    "v_fma_f32 v89, v89, s57, v166\n"
    "v_mfma_f32_16x16x32_bf16 v[184:187], a[224:227], v[136:139], 0\n"
    "buffer_load_dwordx4 v16, s[8:11], s20 offen offset:3072 lds\n"
    "v_fma_f32 v90, v90, s57, v166\n"
    "ds_read_b128 a[144:147], v4 offset:20480\n"
    "v_fma_f32 v91, v91, s57, v166\n"
    "v_fma_f32 v92, v92, s57, v166\n"
    "s_add_u32 s20, s20, 0x4000\n"
    "ds_read_b64_tr_b16 a[176:177], v12 offset:1024\n"
    "v_cvt_pk_bf16_f32 v99, 0, v99\n"
    "ds_read_b64_tr_b16 a[178:179], v12 offset:5120\n"
    "v_fma_f32 v93, v93, s57, v166\n"
    "v_exp_f32 v88, v88\n"
    "v_mfma_f32_16x16x32_bf16 v[100:103], a[152:155], v[200:203], v[100:103]\n"
    "v_fma_f32 v94, v94, s57, v166\n"
    "v_exp_f32 v89, v89\n"
    "v_mfma_f32_16x16x32_bf16 v[108:111], a[152:155], v[216:219], v[108:111]\n"
    "v_exp_f32 v90, v90\n"
    "v_mfma_f32_16x16x32_bf16 v[116:119], a[152:155], v[232:235], v[116:119]\n"
    "v_fma_f32 v95, v95, s57, v166\n"
    "v_cvt_pk_bf16_f32 v140, v88, v89\n"
    "v_mfma_f32_16x16x32_bf16 v[124:127], a[152:155], v[248:251], v[124:127]\n"
    "v_exp_f32 v91, v91\n"
    "v_cvt_pk_bf16_f32 v104, 0, v104\n"
    "v_exp_f32 v92, v92\n"
    "v_exp_f32 v93, v93\n"
    "v_exp_f32 v94, v94\n"
    "v_exp_f32 v95, v95\n"
    "ds_read_b128 a[148:151], v5 offset:20480\n"
    "s_waitcnt lgkmcnt(1)\n"
    "v_cvt_pk_bf16_f32 v141, v90, v91\n"
    "v_cvt_pk_bf16_f32 v142, v92, v93\n"
    "v_cvt_pk_bf16_f32 v143, v94, v95\n"
    "v_mfma_f32_16x16x32_bf16 v[100:103], a[156:159], v[204:207], v[100:103]\n"
    "s_nop 0\n"
    "v_mfma_f32_16x16x32_bf16 v[188:191], a[224:227], v[140:143], 0\n"
    "ds_read_b64_tr_b16 a[180:181], v12 offset:1280\n"
    "v_cvt_pk_bf16_f32 v105, 0, v105\n"
    "v_cvt_pk_bf16_f32 v106, 0, v106\n"
    "v_mfma_f32_16x16x32_bf16 v[108:111], a[156:159], v[220:223], v[108:111]\n"
    "ds_read_b64_tr_b16 a[182:183], v12 offset:5376\n"
    "v_cvt_pk_bf16_f32 v107, 0, v107\n"
    "v_mfma_f32_16x16x32_bf16 v[116:119], a[156:159], v[236:239], v[116:119]\n"
    "ds_read_b128 a[152:155], v6 offset:20480\n"
    "v_cvt_pk_bf16_f32 v112, 0, v112\n"
    "v_mfma_f32_16x16x32_bf16 v[124:127], a[156:159], v[252:255], v[124:127]\n"
    "ds_read_b64_tr_b16 a[184:185], v12 offset:1536\n"
    "v_cvt_pk_bf16_f32 v113, 0, v113\n"
    "v_max3_f32 v19, v176, v180, v184\n"
    "v_cvt_pk_bf16_f32 v114, 0, v114\n"
    "v_max_f32 v19, v19, v188\n"
    "v_cvt_pk_bf16_f32 v115, 0, v115\n"
    "v_cvt_pk_bf16_f32 v120, 0, v120\n"
    "v_cmp_lt_f32 vcc, s25, v19\n"
    "v_cvt_pk_bf16_f32 v121, 0, v121\n"
    "ds_read_b64_tr_b16 a[186:187], v12 offset:5632\n"
    "ds_read_b128 a[156:159], v7 offset:20480\n"
    "ds_read_b64_tr_b16 a[188:189], v12 offset:1792\n"
    "ds_read_b64_tr_b16 a[190:191], v12 offset:5888\n"
    "v_cvt_pk_bf16_f32 v122, 0, v122\n"
    "v_cvt_pk_bf16_f32 v123, 0, v123\n"
    "s_cbranch_vccnz RARE_p0_%=\n"
    "RARE_p0_%=_ret:\n"
    "v_mfma_f32_16x16x32_bf16 v[64:67], a[128:131], v[192:195], 0\n"
    "v_cvt_pk_bf16_f32 v100, 0, v100\n"
    "v_cvt_pk_bf16_f32 v101, 0, v101\n"
    "v_mfma_f32_16x16x32_bf16 v[72:75], a[128:131], v[208:211], 0\n"
    "v_cvt_pk_bf16_f32 v102, 0, v102\n"
    "v_cvt_pk_bf16_f32 v103, 0, v103\n"
    "v_mfma_f32_16x16x32_bf16 v[80:83], a[128:131], v[224:227], 0\n"
    "v_fma_f32 v96, v96, s57, v160\n"
    "v_fma_f32 v97, v97, s57, v160\n"
    "v_mfma_f32_16x16x32_bf16 v[88:91], a[128:131], v[240:243], 0\n"
    "v_fma_f32 v98, v98, s57, v160\n"
    "v_exp_f32 v96, v96\n"
    "v_mfma_f32_16x16x32_bf16 a[0:3], a[160:163], v[128:131], 0\n"
    "v_exp_f32 v97, v97\n"
    "v_mfma_f32_16x16x32_bf16 a[4:7], a[160:163], v[132:135], 0\n"
    "v_fma_f32 v99, v99, s57, v160\n"
    "v_exp_f32 v98, v98\n"
    "v_mfma_f32_16x16x32_bf16 a[8:11], a[160:163], v[136:139], 0\n"
    "v_fma_f32 v100, v100, s57, v160\n"
    "v_exp_f32 v99, v99\n"
    "v_mfma_f32_16x16x32_bf16 a[12:15], a[160:163], v[140:143], 0\n"
    "v_exp_f32 v100, v100\n"
    "v_mfma_f32_16x16x32_bf16 v[64:67], a[132:135], v[196:199], v[64:67]\n"
    "v_fma_f32 v101, v101, s57, v160\n"
    "v_exp_f32 v101, v101\n"
    "v_mfma_f32_16x16x32_bf16 v[72:75], a[132:135], v[212:215], v[72:75]\n"
    "ds_read_b128 a[128:131], v4 offset:24576\n"
    "v_cvt_pk_bf16_f32 v144, v96, v97\n"
    "v_mfma_f32_16x16x32_bf16 v[80:83], a[132:135], v[228:231], v[80:83]\n"
    "v_fma_f32 v102, v102, s57, v160\n"
    "v_exp_f32 v102, v102\n"
    "v_mfma_f32_16x16x32_bf16 v[88:91], a[132:135], v[244:247], v[88:91]\n"
    "v_fma_f32 v103, v103, s57, v160\n"
    "v_cvt_pk_bf16_f32 v145, v98, v99\n"
    "v_mfma_f32_16x16x32_bf16 a[16:19], a[164:167], v[128:131], 0\n"
    "v_exp_f32 v103, v103\n"
    "v_cvt_pk_bf16_f32 v146, v100, v101\n"
    "v_mfma_f32_16x16x32_bf16 a[20:23], a[164:167], v[132:135], 0\n"
    "v_cvt_pk_bf16_f32 v147, v102, v103\n"
    "v_cvt_pk_bf16_f32 v108, 0, v108\n"
    "v_mfma_f32_16x16x32_bf16 a[24:27], a[164:167], v[136:139], 0\n"
    "ds_read_b64_tr_b16 a[160:161], v12 offset:8192\n"
    "v_cvt_pk_bf16_f32 v109, 0, v109\n"
    "v_cvt_pk_bf16_f32 v110, 0, v110\n"
    "v_mfma_f32_16x16x32_bf16 a[28:31], a[164:167], v[140:143], 0\n"
    "ds_read_b64_tr_b16 a[162:163], v12 offset:12288\n"
    "v_cvt_pk_bf16_f32 v111, 0, v111\n"
    "v_mfma_f32_16x16x32_bf16 v[64:67], a[136:139], v[200:203], v[64:67]\n"
    "ds_read_b128 a[132:135], v5 offset:24576\n"
    "v_fma_f32 v104, v104, s57, v162\n"
    "v_fma_f32 v105, v105, s57, v162\n"
    "v_mfma_f32_16x16x32_bf16 v[72:75], a[136:139], v[216:219], v[72:75]\n"
    "v_fma_f32 v106, v106, s57, v162\n"
    "v_fma_f32 v107, v107, s57, v162\n"
    "v_mfma_f32_16x16x32_bf16 v[40:43], a[224:227], v[144:147], v[176:179]\n"
    "ds_read_b64_tr_b16 a[164:165], v12 offset:8448\n"
    "v_exp_f32 v104, v104\n"
    "v_mfma_f32_16x16x32_bf16 v[80:83], a[136:139], v[232:235], v[80:83]\n"
    "s_add_u32 m0, s23, 0x8000\n"
    "v_fma_f32 v108, v108, s57, v162\n"
    "v_mfma_f32_16x16x32_bf16 v[88:91], a[136:139], v[248:251], v[88:91]\n"
    "buffer_load_dwordx4 v17, s[12:15], s21 offen offset:0 lds\n"
    "v_exp_f32 v105, v105\n"
    "v_cvt_pk_bf16_f32 v116, 0, v116\n"
    "v_mfma_f32_16x16x32_bf16 a[32:35], a[168:171], v[128:131], 0\n"
    "v_fma_f32 v109, v109, s57, v162\n"
    "v_cvt_pk_bf16_f32 v148, v104, v105\n"
    "v_mfma_f32_16x16x32_bf16 a[36:39], a[168:171], v[132:135], 0\n"
    "ds_read_b64_tr_b16 a[166:167], v12 offset:12544\n"
    "v_exp_f32 v106, v106\n"
    "v_mfma_f32_16x16x32_bf16 a[40:43], a[168:171], v[136:139], 0\n"
    "v_fma_f32 v110, v110, s57, v162\n"
    "v_cvt_pk_bf16_f32 v117, 0, v117\n"
    "v_mfma_f32_16x16x32_bf16 a[44:47], a[168:171], v[140:143], 0\n"
    "v_exp_f32 v107, v107\n"
    "v_cvt_pk_bf16_f32 v118, 0, v118\n"
    "v_mfma_f32_16x16x32_bf16 v[64:67], a[140:143], v[204:207], v[64:67]\n"
    "v_fma_f32 v111, v111, s57, v162\n"
    "v_cvt_pk_bf16_f32 v149, v106, v107\n"
    "v_mfma_f32_16x16x32_bf16 v[72:75], a[140:143], v[220:223], v[72:75]\n"
    "s_waitcnt lgkmcnt(1)\n"
    "v_exp_f32 v108, v108\n"
    "v_cvt_pk_bf16_f32 v119, 0, v119\n"
    "v_mfma_f32_16x16x32_bf16 v[80:83], a[140:143], v[236:239], v[80:83]\n"
    "v_exp_f32 v109, v109\n"
    "v_mfma_f32_16x16x32_bf16 v[88:91], a[140:143], v[252:255], v[88:91]\n"
    "v_exp_f32 v110, v110\n"
    "v_exp_f32 v111, v111\n"
    "v_mfma_f32_16x16x32_bf16 a[48:51], a[172:175], v[128:131], 0\n"
    "v_cvt_pk_bf16_f32 v150, v108, v109\n"
    "v_mfma_f32_16x16x32_bf16 a[52:55], a[172:175], v[132:135], 0\n"
    "buffer_load_dwordx4 v18, s[12:15], s21 offen offset:1024 lds\n"
    "v_cvt_pk_bf16_f32 v151, v110, v111\n"
    "v_fma_f32 v112, v112, s57, v164\n"
    "v_mfma_f32_16x16x32_bf16 a[56:59], a[172:175], v[136:139], 0\n"
    "ds_read_b128 a[136:139], v6 offset:24576\n"
    "v_fma_f32 v113, v113, s57, v164\n"
    "v_mfma_f32_16x16x32_bf16 v[44:47], a[224:227], v[148:151], v[180:183]\n"
    "ds_read_b64_tr_b16 a[168:169], v12 offset:8704\n"
    "v_fma_f32 v114, v114, s57, v164\n"
    "v_fma_f32 v115, v115, s57, v164\n"
    "v_mfma_f32_16x16x32_bf16 a[60:63], a[172:175], v[140:143], 0\n"
    "ds_read_b64_tr_b16 a[170:171], v12 offset:12800\n"
    "v_fma_f32 v116, v116, s57, v164\n"
    "v_mfma_f32_16x16x32_bf16 v[68:71], a[144:147], v[192:195], 0\n"
    "ds_read_b128 a[140:143], v7 offset:24576\n"
    "v_exp_f32 v112, v112\n"
    "v_mfma_f32_16x16x32_bf16 v[76:79], a[144:147], v[208:211], 0\n"
    "v_fma_f32 v117, v117, s57, v164\n"
    "v_exp_f32 v113, v113\n"
    "v_mfma_f32_16x16x32_bf16 v[84:87], a[144:147], v[224:227], 0\n"
    "buffer_load_dwordx4 v17, s[12:15], s21 offen offset:2048 lds\n"
    "v_cvt_pk_bf16_f32 v152, v112, v113\n"
    "v_mfma_f32_16x16x32_bf16 v[92:95], a[144:147], v[240:243], 0\n"
    "v_fma_f32 v118, v118, s57, v164\n"
    "v_exp_f32 v114, v114\n"
    "v_mfma_f32_16x16x32_bf16 a[64:67], a[176:179], v[128:131], 0\n"
    "v_exp_f32 v115, v115\n"
    "v_mfma_f32_16x16x32_bf16 a[68:71], a[176:179], v[132:135], 0\n"
    "v_fma_f32 v119, v119, s57, v164\n"
    "v_exp_f32 v116, v116\n"
    "v_mfma_f32_16x16x32_bf16 a[72:75], a[176:179], v[136:139], 0\n"
    "v_exp_f32 v117, v117\n"
    "v_mfma_f32_16x16x32_bf16 a[76:79], a[176:179], v[140:143], 0\n"
    "v_exp_f32 v118, v118\n"
    "v_cvt_pk_bf16_f32 v153, v114, v115\n"
    "v_mfma_f32_16x16x32_bf16 v[68:71], a[148:151], v[196:199], v[68:71]\n"
    "v_exp_f32 v119, v119\n"
    "v_mfma_f32_16x16x32_bf16 v[76:79], a[148:151], v[212:215], v[76:79]\n"
    "v_cvt_pk_bf16_f32 v154, v116, v117\n"
    "v_cvt_pk_bf16_f32 v155, v118, v119\n"
    "v_cvt_pk_bf16_f32 v124, 0, v124\n"
    "v_mfma_f32_16x16x32_bf16 v[84:87], a[148:151], v[228:231], v[84:87]\n"
    "ds_read_b64_tr_b16 a[172:173], v12 offset:8960\n"
    "v_cvt_pk_bf16_f32 v125, 0, v125\n"
    "v_mfma_f32_16x16x32_bf16 v[92:95], a[148:151], v[244:247], v[92:95]\n"
    "ds_read_b64_tr_b16 a[174:175], v12 offset:13056\n"
    "v_cvt_pk_bf16_f32 v126, 0, v126\n"
    "v_cvt_pk_bf16_f32 v127, 0, v127\n"
    "v_mfma_f32_16x16x32_bf16 v[48:51], a[224:227], v[152:155], v[184:187]\n"
    "buffer_load_dwordx4 v18, s[12:15], s21 offen offset:3072 lds\n"
    "v_fma_f32 v120, v120, s57, v166\n"
    "v_mfma_f32_16x16x32_bf16 a[80:83], a[180:183], v[128:131], 0\n"
    "ds_read_b128 a[144:147], v4 offset:28672\n"
    "v_fma_f32 v121, v121, s57, v166\n"
    "v_fma_f32 v122, v122, s57, v166\n"
    "v_mfma_f32_16x16x32_bf16 a[84:87], a[180:183], v[132:135], 0\n"
    "s_add_u32 s21, s21, 0x4000\n"
    "ds_read_b64_tr_b16 a[176:177], v12 offset:9216\n"
    "v_fma_f32 v123, v123, s57, v166\n"
    "v_mfma_f32_16x16x32_bf16 a[88:91], a[180:183], v[136:139], 0\n"
    "ds_read_b64_tr_b16 a[178:179], v12 offset:13312\n"
    "v_fma_f32 v124, v124, s57, v166\n"
    "v_fma_f32 v125, v125, s57, v166\n"
    "v_mfma_f32_16x16x32_bf16 a[92:95], a[180:183], v[140:143], 0\n"
    "v_exp_f32 v120, v120\n"
    "v_mfma_f32_16x16x32_bf16 v[68:71], a[152:155], v[200:203], v[68:71]\n"
    "v_fma_f32 v126, v126, s57, v166\n"
    "v_exp_f32 v121, v121\n"
    "v_mfma_f32_16x16x32_bf16 v[76:79], a[152:155], v[216:219], v[76:79]\n"
    "v_exp_f32 v122, v122\n"
    "v_cvt_pk_bf16_f32 v156, v120, v121\n"
    "v_mfma_f32_16x16x32_bf16 v[84:87], a[152:155], v[232:235], v[84:87]\n"
    "v_fma_f32 v127, v127, s57, v166\n"
    "v_cvt_pk_bf16_f32 v64, 0, v64\n"
    "v_mfma_f32_16x16x32_bf16 v[92:95], a[152:155], v[248:251], v[92:95]\n"
    "v_exp_f32 v123, v123\n"
    "v_mfma_f32_16x16x32_bf16 a[96:99], a[184:187], v[128:131], 0\n"
    "v_exp_f32 v124, v124\n"
    "v_cvt_pk_bf16_f32 v157, v122, v123\n"
    "v_mfma_f32_16x16x32_bf16 a[100:103], a[184:187], v[132:135], 0\n"
    "v_exp_f32 v125, v125\n"
    "v_exp_f32 v126, v126\n"
    "v_exp_f32 v127, v127\n"
    "v_mfma_f32_16x16x32_bf16 a[104:107], a[184:187], v[136:139], 0\n"
    "ds_read_b128 a[148:151], v5 offset:28672\n"
    "v_mfma_f32_16x16x32_bf16 a[108:111], a[184:187], v[140:143], 0\n"
    "s_waitcnt lgkmcnt(1)\n"
    "v_cvt_pk_bf16_f32 v158, v124, v125\n"
    "v_cvt_pk_bf16_f32 v159, v126, v127\n"
    "v_mfma_f32_16x16x32_bf16 v[68:71], a[156:159], v[204:207], v[68:71]\n"
    "ds_read_b64_tr_b16 a[180:181], v12 offset:9472\n"
    "v_cvt_pk_bf16_f32 v65, 0, v65\n"
    "v_mfma_f32_16x16x32_bf16 v[52:55], a[224:227], v[156:159], v[188:191]\n"
    "ds_read_b64_tr_b16 a[182:183], v12 offset:13568\n"
    "v_cvt_pk_bf16_f32 v66, 0, v66\n"
    "v_cvt_pk_bf16_f32 v67, 0, v67\n"
    "v_mfma_f32_16x16x32_bf16 v[76:79], a[156:159], v[220:223], v[76:79]\n"
    "ds_read_b128 a[152:155], v6 offset:28672\n"
    "v_cvt_pk_bf16_f32 v72, 0, v72\n"
    "v_mfma_f32_16x16x32_bf16 v[84:87], a[156:159], v[236:239], v[84:87]\n"
    "ds_read_b64_tr_b16 a[184:185], v12 offset:9728\n"
    "v_cvt_pk_bf16_f32 v73, 0, v73\n"
    "v_cvt_pk_bf16_f32 v74, 0, v74\n"
    "v_mfma_f32_16x16x32_bf16 v[92:95], a[156:159], v[252:255], v[92:95]\n"
    "ds_read_b64_tr_b16 a[186:187], v12 offset:13824\n"
    "v_cvt_pk_bf16_f32 v75, 0, v75\n"
    "v_mfma_f32_16x16x32_bf16 a[112:115], a[188:191], v[128:131], 0\n"
    "v_max3_f32 v19, v40, v44, v48\n"
    "v_cvt_pk_bf16_f32 v80, 0, v80\n"
    "v_cvt_pk_bf16_f32 v81, 0, v81\n"
    "v_mfma_f32_16x16x32_bf16 a[116:119], a[188:191], v[132:135], 0\n"
    "v_max_f32 v19, v19, v52\n"
    "v_cvt_pk_bf16_f32 v82, 0, v82\n"
    "v_mfma_f32_16x16x32_bf16 a[120:123], a[188:191], v[136:139], 0\n"
    "v_cmp_lt_f32 vcc, s25, v19\n"
    "v_cvt_pk_bf16_f32 v83, 0, v83\n"
    "v_mfma_f32_16x16x32_bf16 a[124:127], a[188:191], v[140:143], 0\n"
    "ds_read_b128 a[156:159], v7 offset:28672\n"
    "ds_read_b64_tr_b16 a[188:189], v12 offset:9984\n"
    "ds_read_b64_tr_b16 a[190:191], v12 offset:14080\n"
    "v_cvt_pk_bf16_f32 v88, 0, v88\n"
    "v_cvt_pk_bf16_f32 v89, 0, v89\n"
    "v_cvt_pk_bf16_f32 v90, 0, v90\n"
    "v_cvt_pk_bf16_f32 v91, 0, v91\n"
    "s_cbranch_vccnz RARE_p1_%=\n"
    "RARE_p1_%=_ret:\n"
    "s_movk_i32 s24, 7\n"
    "LOOP_%=:\n"
    "v_mfma_f32_16x16x32_bf16 v[96:99], a[128:131], v[192:195], 0\n"
    "s_waitcnt vmcnt(8)\n"
    "s_barrier\n"
    "v_cvt_pk_bf16_f32 v68, 0, v68\n"
    "v_cvt_pk_bf16_f32 v69, 0, v69\n"
    "v_mfma_f32_16x16x32_bf16 v[104:107], a[128:131], v[208:211], 0\n"
    "v_cvt_pk_bf16_f32 v70, 0, v70\n"
    "v_cvt_pk_bf16_f32 v71, 0, v71\n"
    "v_mfma_f32_16x16x32_bf16 v[112:115], a[128:131], v[224:227], 0\n"
    "v_fma_f32 v64, v64, s57, v160\n"
    "v_fma_f32 v65, v65, s57, v160\n"
    "v_mfma_f32_16x16x32_bf16 v[120:123], a[128:131], v[240:243], 0\n"
    "v_fma_f32 v66, v66, s57, v160\n"
    "v_exp_f32 v64, v64\n"
    "v_mfma_f32_16x16x32_bf16 a[0:3], a[160:163], v[144:147], a[0:3]\n"
    "v_exp_f32 v65, v65\n"
    "v_mfma_f32_16x16x32_bf16 a[4:7], a[160:163], v[148:151], a[4:7]\n"
    "v_fma_f32 v67, v67, s57, v160\n"
    "v_exp_f32 v66, v66\n"
    "v_mfma_f32_16x16x32_bf16 a[8:11], a[160:163], v[152:155], a[8:11]\n"
    "v_fma_f32 v68, v68, s57, v160\n"
    "v_exp_f32 v67, v67\n"
    "v_mfma_f32_16x16x32_bf16 a[12:15], a[160:163], v[156:159], a[12:15]\n"
    "v_exp_f32 v68, v68\n"
    "v_mfma_f32_16x16x32_bf16 v[96:99], a[132:135], v[196:199], v[96:99]\n"
    "v_fma_f32 v69, v69, s57, v160\n"
    "v_exp_f32 v69, v69\n"
    "v_mfma_f32_16x16x32_bf16 v[104:107], a[132:135], v[212:215], v[104:107]\n"
    "ds_read_b128 a[128:131], v4 offset:32768\n"
    "v_cvt_pk_bf16_f32 v128, v64, v65\n"
    "v_mfma_f32_16x16x32_bf16 v[112:115], a[132:135], v[228:231], v[112:115]\n"
    "v_fma_f32 v70, v70, s57, v160\n"
    "v_exp_f32 v70, v70\n"
    "v_mfma_f32_16x16x32_bf16 v[120:123], a[132:135], v[244:247], v[120:123]\n"
    "v_fma_f32 v71, v71, s57, v160\n"
    "v_cvt_pk_bf16_f32 v129, v66, v67\n"
    "v_mfma_f32_16x16x32_bf16 a[16:19], a[164:167], v[144:147], a[16:19]\n"
    "v_exp_f32 v71, v71\n"
    "v_cvt_pk_bf16_f32 v130, v68, v69\n"
    "v_mfma_f32_16x16x32_bf16 a[20:23], a[164:167], v[148:151], a[20:23]\n"
    "v_cvt_pk_bf16_f32 v131, v70, v71\n"
    "v_cvt_pk_bf16_f32 v76, 0, v76\n"
    "v_mfma_f32_16x16x32_bf16 a[24:27], a[164:167], v[152:155], a[24:27]\n"
    "ds_read_b64_tr_b16 a[160:161], v12 offset:16384\n"
    "v_cvt_pk_bf16_f32 v77, 0, v77\n"
    "v_cvt_pk_bf16_f32 v78, 0, v78\n"
    "v_mfma_f32_16x16x32_bf16 a[28:31], a[164:167], v[156:159], a[28:31]\n"
    "ds_read_b64_tr_b16 a[162:163], v12 offset:20480\n"
    "v_cvt_pk_bf16_f32 v79, 0, v79\n"
    "v_mfma_f32_16x16x32_bf16 v[96:99], a[136:139], v[200:203], v[96:99]\n"
    "ds_read_b128 a[132:135], v5 offset:32768\n"
    "v_fma_f32 v72, v72, s57, v162\n"
    "v_fma_f32 v73, v73, s57, v162\n"
    "v_mfma_f32_16x16x32_bf16 v[104:107], a[136:139], v[216:219], v[104:107]\n"
    "v_fma_f32 v74, v74, s57, v162\n"
    "v_fma_f32 v75, v75, s57, v162\n"
    "v_mfma_f32_16x16x32_bf16 v[176:179], a[224:227], v[128:131], v[40:43]\n"
    "ds_read_b64_tr_b16 a[164:165], v12 offset:16640\n"
    "v_exp_f32 v72, v72\n"
    "v_mfma_f32_16x16x32_bf16 v[112:115], a[136:139], v[232:235], v[112:115]\n"
    "s_add_u32 m0, s22, 0x0\n"
    "v_fma_f32 v76, v76, s57, v162\n"
    "v_mfma_f32_16x16x32_bf16 v[120:123], a[136:139], v[248:251], v[120:123]\n"
    "buffer_load_dwordx4 v13, s[8:11], s20 offen offset:0 lds\n"
    "v_exp_f32 v73, v73\n"
    "v_cvt_pk_bf16_f32 v84, 0, v84\n"
    "v_mfma_f32_16x16x32_bf16 a[32:35], a[168:171], v[144:147], a[32:35]\n"
    "v_fma_f32 v77, v77, s57, v162\n"
    "v_cvt_pk_bf16_f32 v132, v72, v73\n"
    "v_mfma_f32_16x16x32_bf16 a[36:39], a[168:171], v[148:151], a[36:39]\n"
    "ds_read_b64_tr_b16 a[166:167], v12 offset:20736\n"
    "v_exp_f32 v74, v74\n"
    "v_mfma_f32_16x16x32_bf16 a[40:43], a[168:171], v[152:155], a[40:43]\n"
    "v_fma_f32 v78, v78, s57, v162\n"
    "v_cvt_pk_bf16_f32 v85, 0, v85\n"
    "v_mfma_f32_16x16x32_bf16 a[44:47], a[168:171], v[156:159], a[44:47]\n"
    "v_exp_f32 v75, v75\n"
    "v_cvt_pk_bf16_f32 v86, 0, v86\n"
    "v_mfma_f32_16x16x32_bf16 v[96:99], a[140:143], v[204:207], v[96:99]\n"
    "v_fma_f32 v79, v79, s57, v162\n"
    "v_cvt_pk_bf16_f32 v133, v74, v75\n"
    "v_mfma_f32_16x16x32_bf16 v[104:107], a[140:143], v[220:223], v[104:107]\n"
    "s_waitcnt lgkmcnt(1)\n"
    "v_exp_f32 v76, v76\n"
    "v_cvt_pk_bf16_f32 v87, 0, v87\n"
    "v_mfma_f32_16x16x32_bf16 v[112:115], a[140:143], v[236:239], v[112:115]\n"
    "v_exp_f32 v77, v77\n"
    "v_mfma_f32_16x16x32_bf16 v[120:123], a[140:143], v[252:255], v[120:123]\n"
    "v_exp_f32 v78, v78\n"
    "v_exp_f32 v79, v79\n"
    "v_mfma_f32_16x16x32_bf16 a[48:51], a[172:175], v[144:147], a[48:51]\n"
    "v_cvt_pk_bf16_f32 v134, v76, v77\n"
    "v_mfma_f32_16x16x32_bf16 a[52:55], a[172:175], v[148:151], a[52:55]\n"
    "buffer_load_dwordx4 v14, s[8:11], s20 offen offset:1024 lds\n"
    "v_cvt_pk_bf16_f32 v135, v78, v79\n"
    "v_fma_f32 v80, v80, s57, v164\n"
    "v_mfma_f32_16x16x32_bf16 a[56:59], a[172:175], v[152:155], a[56:59]\n"
    "ds_read_b128 a[136:139], v6 offset:32768\n"
    "v_fma_f32 v81, v81, s57, v164\n"
    "v_mfma_f32_16x16x32_bf16 v[180:183], a[224:227], v[132:135], v[44:47]\n"
    "ds_read_b64_tr_b16 a[168:169], v12 offset:16896\n"
    "v_fma_f32 v82, v82, s57, v164\n"
    "v_fma_f32 v83, v83, s57, v164\n"
    "v_mfma_f32_16x16x32_bf16 a[60:63], a[172:175], v[156:159], a[60:63]\n"
    "ds_read_b64_tr_b16 a[170:171], v12 offset:20992\n"
    "v_fma_f32 v84, v84, s57, v164\n"
    "v_mfma_f32_16x16x32_bf16 v[100:103], a[144:147], v[192:195], 0\n"
    "ds_read_b128 a[140:143], v7 offset:32768\n"
    "v_exp_f32 v80, v80\n"
    "v_mfma_f32_16x16x32_bf16 v[108:111], a[144:147], v[208:211], 0\n"
    "v_fma_f32 v85, v85, s57, v164\n"
    "v_exp_f32 v81, v81\n"
    "v_mfma_f32_16x16x32_bf16 v[116:119], a[144:147], v[224:227], 0\n"
    "buffer_load_dwordx4 v15, s[8:11], s20 offen offset:2048 lds\n"
    "v_cvt_pk_bf16_f32 v136, v80, v81\n"
    "v_mfma_f32_16x16x32_bf16 v[124:127], a[144:147], v[240:243], 0\n"
    "v_fma_f32 v86, v86, s57, v164\n"
    "v_exp_f32 v82, v82\n"
    "v_mfma_f32_16x16x32_bf16 a[64:67], a[176:179], v[144:147], a[64:67]\n"
    "v_exp_f32 v83, v83\n"
    "v_mfma_f32_16x16x32_bf16 a[68:71], a[176:179], v[148:151], a[68:71]\n"
    "v_fma_f32 v87, v87, s57, v164\n"
    "v_exp_f32 v84, v84\n"
    "v_mfma_f32_16x16x32_bf16 a[72:75], a[176:179], v[152:155], a[72:75]\n"
    "v_exp_f32 v85, v85\n"
    "v_mfma_f32_16x16x32_bf16 a[76:79], a[176:179], v[156:159], a[76:79]\n"
    "v_exp_f32 v86, v86\n"
    "v_cvt_pk_bf16_f32 v137, v82, v83\n"
    "v_mfma_f32_16x16x32_bf16 v[100:103], a[148:151], v[196:199], v[100:103]\n"
    "v_exp_f32 v87, v87\n"
    "v_mfma_f32_16x16x32_bf16 v[108:111], a[148:151], v[212:215], v[108:111]\n"
    "v_cvt_pk_bf16_f32 v138, v84, v85\n"
    "v_cvt_pk_bf16_f32 v139, v86, v87\n"
    "v_cvt_pk_bf16_f32 v92, 0, v92\n"
    "v_mfma_f32_16x16x32_bf16 v[116:119], a[148:151], v[228:231], v[116:119]\n"
    "ds_read_b64_tr_b16 a[172:173], v12 offset:17152\n"
    "v_cvt_pk_bf16_f32 v93, 0, v93\n"
    "v_mfma_f32_16x16x32_bf16 v[124:127], a[148:151], v[244:247], v[124:127]\n"
    "ds_read_b64_tr_b16 a[174:175], v12 offset:21248\n"
    "v_cvt_pk_bf16_f32 v94, 0, v94\n"
    "v_cvt_pk_bf16_f32 v95, 0, v95\n"
    "v_mfma_f32_16x16x32_bf16 v[184:187], a[224:227], v[136:139], v[48:51]\n"
    "buffer_load_dwordx4 v16, s[8:11], s20 offen offset:3072 lds\n"
    "v_fma_f32 v88, v88, s57, v166\n"
    "v_mfma_f32_16x16x32_bf16 a[80:83], a[180:183], v[144:147], a[80:83]\n"
    "ds_read_b128 a[144:147], v4 offset:36864\n"
    "v_fma_f32 v89, v89, s57, v166\n"
    "v_fma_f32 v90, v90, s57, v166\n"
    "v_mfma_f32_16x16x32_bf16 a[84:87], a[180:183], v[148:151], a[84:87]\n"
    "s_add_u32 s20, s20, 0x4000\n"
    "ds_read_b64_tr_b16 a[176:177], v12 offset:17408\n"
    "v_fma_f32 v91, v91, s57, v166\n"
    "v_mfma_f32_16x16x32_bf16 a[88:91], a[180:183], v[152:155], a[88:91]\n"
    "ds_read_b64_tr_b16 a[178:179], v12 offset:21504\n"
    "v_fma_f32 v92, v92, s57, v166\n"
    "v_fma_f32 v93, v93, s57, v166\n"
    "v_mfma_f32_16x16x32_bf16 a[92:95], a[180:183], v[156:159], a[92:95]\n"
    "v_exp_f32 v88, v88\n"
    "v_mfma_f32_16x16x32_bf16 v[100:103], a[152:155], v[200:203], v[100:103]\n"
    "v_fma_f32 v94, v94, s57, v166\n"
    "v_exp_f32 v89, v89\n"
    "v_mfma_f32_16x16x32_bf16 v[108:111], a[152:155], v[216:219], v[108:111]\n"
    "v_exp_f32 v90, v90\n"
    "v_cvt_pk_bf16_f32 v140, v88, v89\n"
    "v_mfma_f32_16x16x32_bf16 v[116:119], a[152:155], v[232:235], v[116:119]\n"
    "v_fma_f32 v95, v95, s57, v166\n"
    "v_cvt_pk_bf16_f32 v96, 0, v96\n"
    "v_mfma_f32_16x16x32_bf16 v[124:127], a[152:155], v[248:251], v[124:127]\n"
    "v_exp_f32 v91, v91\n"
    "v_mfma_f32_16x16x32_bf16 a[96:99], a[184:187], v[144:147], a[96:99]\n"
    "v_exp_f32 v92, v92\n"
    "v_cvt_pk_bf16_f32 v141, v90, v91\n"
    "v_mfma_f32_16x16x32_bf16 a[100:103], a[184:187], v[148:151], a[100:103]\n"
    "v_exp_f32 v93, v93\n"
    "v_exp_f32 v94, v94\n"
    "v_exp_f32 v95, v95\n"
    "v_mfma_f32_16x16x32_bf16 a[104:107], a[184:187], v[152:155], a[104:107]\n"
    "ds_read_b128 a[148:151], v5 offset:36864\n"
    "v_mfma_f32_16x16x32_bf16 a[108:111], a[184:187], v[156:159], a[108:111]\n"
    "s_waitcnt lgkmcnt(1)\n"
    "v_cvt_pk_bf16_f32 v142, v92, v93\n"
    "v_cvt_pk_bf16_f32 v143, v94, v95\n"
    "v_mfma_f32_16x16x32_bf16 v[100:103], a[156:159], v[204:207], v[100:103]\n"
    "ds_read_b64_tr_b16 a[180:181], v12 offset:17664\n"
    "v_cvt_pk_bf16_f32 v97, 0, v97\n"
    "v_mfma_f32_16x16x32_bf16 v[188:191], a[224:227], v[140:143], v[52:55]\n"
    "ds_read_b64_tr_b16 a[182:183], v12 offset:21760\n"
    "v_cvt_pk_bf16_f32 v98, 0, v98\n"
    "v_cvt_pk_bf16_f32 v99, 0, v99\n"
    "v_mfma_f32_16x16x32_bf16 v[108:111], a[156:159], v[220:223], v[108:111]\n"
    "ds_read_b128 a[152:155], v6 offset:36864\n"
    "v_cvt_pk_bf16_f32 v104, 0, v104\n"
    "v_mfma_f32_16x16x32_bf16 v[116:119], a[156:159], v[236:239], v[116:119]\n"
    "ds_read_b64_tr_b16 a[184:185], v12 offset:17920\n"
    "v_cvt_pk_bf16_f32 v105, 0, v105\n"
    "v_cvt_pk_bf16_f32 v106, 0, v106\n"
    "v_mfma_f32_16x16x32_bf16 v[124:127], a[156:159], v[252:255], v[124:127]\n"
    "ds_read_b64_tr_b16 a[186:187], v12 offset:22016\n"
    "v_cvt_pk_bf16_f32 v107, 0, v107\n"
    "v_mfma_f32_16x16x32_bf16 a[112:115], a[188:191], v[144:147], a[112:115]\n"
    "v_max3_f32 v19, v176, v180, v184\n"
    "v_cvt_pk_bf16_f32 v112, 0, v112\n"
    "v_cvt_pk_bf16_f32 v113, 0, v113\n"
    "v_mfma_f32_16x16x32_bf16 a[116:119], a[188:191], v[148:151], a[116:119]\n"
    "v_max_f32 v19, v19, v188\n"
    "v_cvt_pk_bf16_f32 v114, 0, v114\n"
    "v_mfma_f32_16x16x32_bf16 a[120:123], a[188:191], v[152:155], a[120:123]\n"
    "v_cmp_lt_f32 vcc, s25, v19\n"
    "v_cvt_pk_bf16_f32 v115, 0, v115\n"
    "v_mfma_f32_16x16x32_bf16 a[124:127], a[188:191], v[156:159], a[124:127]\n"
    "ds_read_b128 a[156:159], v7 offset:36864\n"
    "ds_read_b64_tr_b16 a[188:189], v12 offset:18176\n"
    "ds_read_b64_tr_b16 a[190:191], v12 offset:22272\n"
    "v_cvt_pk_bf16_f32 v120, 0, v120\n"
    "v_cvt_pk_bf16_f32 v121, 0, v121\n"
    "v_cvt_pk_bf16_f32 v122, 0, v122\n"
    "v_cvt_pk_bf16_f32 v123, 0, v123\n"
    "s_cbranch_vccnz RARE_b2_%=\n"
    "RARE_b2_%=_ret:\n"
    "v_mfma_f32_16x16x32_bf16 v[64:67], a[128:131], v[192:195], 0\n"
    "v_cvt_pk_bf16_f32 v100, 0, v100\n"
    "v_cvt_pk_bf16_f32 v101, 0, v101\n"
    "v_mfma_f32_16x16x32_bf16 v[72:75], a[128:131], v[208:211], 0\n"
    "v_cvt_pk_bf16_f32 v102, 0, v102\n"
    "v_cvt_pk_bf16_f32 v103, 0, v103\n"
    "v_mfma_f32_16x16x32_bf16 v[80:83], a[128:131], v[224:227], 0\n"
    "v_fma_f32 v96, v96, s57, v160\n"
    "v_fma_f32 v97, v97, s57, v160\n"
    "v_mfma_f32_16x16x32_bf16 v[88:91], a[128:131], v[240:243], 0\n"
    "v_fma_f32 v98, v98, s57, v160\n"
    "v_exp_f32 v96, v96\n"
    "v_mfma_f32_16x16x32_bf16 a[0:3], a[160:163], v[128:131], a[0:3]\n"
    "v_exp_f32 v97, v97\n"
    "v_mfma_f32_16x16x32_bf16 a[4:7], a[160:163], v[132:135], a[4:7]\n"
    "v_fma_f32 v99, v99, s57, v160\n"
    "v_exp_f32 v98, v98\n"
    "v_cvt_pk_bf16_f32 v144, v96, v97\n"
    "v_mfma_f32_16x16x32_bf16 a[8:11], a[160:163], v[136:139], a[8:11]\n"
    "v_fma_f32 v100, v100, s57, v160\n"
    "v_cvt_pk_bf16_f32 v108, 0, v108\n"
    "v_mfma_f32_16x16x32_bf16 a[12:15], a[160:163], v[140:143], a[12:15]\n"
    "v_exp_f32 v99, v99\n"
    "v_cvt_pk_bf16_f32 v109, 0, v109\n"
    "v_mfma_f32_16x16x32_bf16 v[64:67], a[132:135], v[196:199], v[64:67]\n"
    "v_fma_f32 v101, v101, s57, v160\n"
    "v_cvt_pk_bf16_f32 v145, v98, v99\n"
    "v_mfma_f32_16x16x32_bf16 v[72:75], a[132:135], v[212:215], v[72:75]\n"
    "v_exp_f32 v100, v100\n"
    "v_cvt_pk_bf16_f32 v110, 0, v110\n"
    "v_mfma_f32_16x16x32_bf16 v[80:83], a[132:135], v[228:231], v[80:83]\n"
    "v_fma_f32 v102, v102, s57, v160\n"
    "v_cvt_pk_bf16_f32 v111, 0, v111\n"
    "v_mfma_f32_16x16x32_bf16 v[88:91], a[132:135], v[244:247], v[88:91]\n"
    "v_fma_f32 v103, v103, s57, v160\n"
    "v_exp_f32 v101, v101\n"
    "v_mfma_f32_16x16x32_bf16 a[16:19], a[164:167], v[128:131], a[16:19]\n"
    "v_exp_f32 v102, v102\n"
    "v_mfma_f32_16x16x32_bf16 a[20:23], a[164:167], v[132:135], a[20:23]\n"
    "v_exp_f32 v103, v103\n"
    "v_cvt_pk_bf16_f32 v146, v100, v101\n"
    "v_mfma_f32_16x16x32_bf16 a[24:27], a[164:167], v[136:139], a[24:27]\n"
    "ds_read_b128 a[128:131], v4 offset:40960\n"
    "v_cvt_pk_bf16_f32 v147, v102, v103\n"
    "v_mfma_f32_16x16x32_bf16 a[28:31], a[164:167], v[140:143], a[28:31]\n"
    "ds_read_b64_tr_b16 a[160:161], v12 offset:24576\n"
    "v_fma_f32 v104, v104, s57, v162\n"
    "v_fma_f32 v105, v105, s57, v162\n"
    "v_mfma_f32_16x16x32_bf16 v[64:67], a[136:139], v[200:203], v[64:67]\n"
    "ds_read_b64_tr_b16 a[162:163], v12 offset:28672\n"
    "v_fma_f32 v106, v106, s57, v162\n"
    "v_mfma_f32_16x16x32_bf16 v[72:75], a[136:139], v[216:219], v[72:75]\n"
    "ds_read_b128 a[132:135], v5 offset:40960\n"
    "v_fma_f32 v107, v107, s57, v162\n"
    "v_mfma_f32_16x16x32_bf16 v[40:43], a[224:227], v[144:147], v[176:179]\n"
    "ds_read_b64_tr_b16 a[164:165], v12 offset:24832\n"
    "v_exp_f32 v104, v104\n"
    "v_mfma_f32_16x16x32_bf16 v[80:83], a[136:139], v[232:235], v[80:83]\n"
    "s_add_u32 m0, s23, 0xc000\n"
    "v_fma_f32 v108, v108, s57, v162\n"
    "v_exp_f32 v105, v105\n"
    "v_mfma_f32_16x16x32_bf16 v[88:91], a[136:139], v[248:251], v[88:91]\n"
    "buffer_load_dwordx4 v17, s[12:15], s21 offen offset:0 lds\n"
    "v_exp_f32 v106, v106\n"
    "v_mfma_f32_16x16x32_bf16 a[32:35], a[168:171], v[128:131], a[32:35]\n"
    "v_fma_f32 v109, v109, s57, v162\n"
    "v_exp_f32 v107, v107\n"
    "v_mfma_f32_16x16x32_bf16 a[36:39], a[168:171], v[132:135], a[36:39]\n"
    "ds_read_b64_tr_b16 a[166:167], v12 offset:28928\n"
    "v_exp_f32 v108, v108\n"
    "v_mfma_f32_16x16x32_bf16 a[40:43], a[168:171], v[136:139], a[40:43]\n"
    "v_fma_f32 v110, v110, s57, v162\n"
    "v_cvt_pk_bf16_f32 v148, v104, v105\n"
    "v_mfma_f32_16x16x32_bf16 a[44:47], a[168:171], v[140:143], a[44:47]\n"
    "v_exp_f32 v109, v109\n"
    "v_cvt_pk_bf16_f32 v149, v106, v107\n"
    "v_mfma_f32_16x16x32_bf16 v[64:67], a[140:143], v[204:207], v[64:67]\n"
    "v_fma_f32 v111, v111, s57, v162\n"
    "v_cvt_pk_bf16_f32 v150, v108, v109\n"
    "v_mfma_f32_16x16x32_bf16 v[72:75], a[140:143], v[220:223], v[72:75]\n"
    "s_waitcnt lgkmcnt(1)\n"
    "v_exp_f32 v110, v110\n"
    "v_cvt_pk_bf16_f32 v116, 0, v116\n"
    "v_mfma_f32_16x16x32_bf16 v[80:83], a[140:143], v[236:239], v[80:83]\n"
    "v_exp_f32 v111, v111\n"
    "v_mfma_f32_16x16x32_bf16 v[88:91], a[140:143], v[252:255], v[88:91]\n"
    "v_cvt_pk_bf16_f32 v151, v110, v111\n"
    "v_cvt_pk_bf16_f32 v117, 0, v117\n"
    "v_cvt_pk_bf16_f32 v118, 0, v118\n"
    "v_mfma_f32_16x16x32_bf16 a[48:51], a[172:175], v[128:131], a[48:51]\n"
    "buffer_load_dwordx4 v18, s[12:15], s21 offen offset:1024 lds\n"
    "v_cvt_pk_bf16_f32 v119, 0, v119\n"
    "v_mfma_f32_16x16x32_bf16 a[52:55], a[172:175], v[132:135], a[52:55]\n"
    "ds_read_b128 a[136:139], v6 offset:40960\n"
    "v_fma_f32 v112, v112, s57, v164\n"
    "v_fma_f32 v113, v113, s57, v164\n"
    "v_mfma_f32_16x16x32_bf16 a[56:59], a[172:175], v[136:139], a[56:59]\n"
    "ds_read_b64_tr_b16 a[168:169], v12 offset:25088\n"
    "v_fma_f32 v114, v114, s57, v164\n"
    "v_mfma_f32_16x16x32_bf16 v[44:47], a[224:227], v[148:151], v[180:183]\n"
    "ds_read_b64_tr_b16 a[170:171], v12 offset:29184\n"
    "v_fma_f32 v115, v115, s57, v164\n"
    "v_cvt_pk_bf16_f32 v64, 0, v64\n"
    "v_mfma_f32_16x16x32_bf16 a[60:63], a[172:175], v[140:143], a[60:63]\n"
    "ds_read_b128 a[140:143], v7 offset:40960\n"
    "v_fma_f32 v116, v116, s57, v164\n"
    "v_mfma_f32_16x16x32_bf16 v[68:71], a[144:147], v[192:195], 0\n"
    "v_exp_f32 v112, v112\n"
    "v_cvt_pk_bf16_f32 v124, 0, v124\n"
    "v_mfma_f32_16x16x32_bf16 v[76:79], a[144:147], v[208:211], 0\n"
    "v_fma_f32 v117, v117, s57, v164\n"
    "v_exp_f32 v113, v113\n"
    "v_mfma_f32_16x16x32_bf16 v[84:87], a[144:147], v[224:227], 0\n"
    "buffer_load_dwordx4 v17, s[12:15], s21 offen offset:2048 lds\n"
    "v_cvt_pk_bf16_f32 v152, v112, v113\n"
    "v_mfma_f32_16x16x32_bf16 v[92:95], a[144:147], v[240:243], 0\n"
    "v_fma_f32 v118, v118, s57, v164\n"
    "v_exp_f32 v114, v114\n"
    "v_mfma_f32_16x16x32_bf16 a[64:67], a[176:179], v[128:131], a[64:67]\n"
    "v_exp_f32 v115, v115\n"
    "v_mfma_f32_16x16x32_bf16 a[68:71], a[176:179], v[132:135], a[68:71]\n"
    "v_fma_f32 v119, v119, s57, v164\n"
    "v_exp_f32 v116, v116\n"
    "v_mfma_f32_16x16x32_bf16 a[72:75], a[176:179], v[136:139], a[72:75]\n"
    "v_exp_f32 v117, v117\n"
    "v_cvt_pk_bf16_f32 v153, v114, v115\n"
    "v_mfma_f32_16x16x32_bf16 a[76:79], a[176:179], v[140:143], a[76:79]\n"
    "v_exp_f32 v118, v118\n"
    "v_mfma_f32_16x16x32_bf16 v[68:71], a[148:151], v[196:199], v[68:71]\n"
    "v_exp_f32 v119, v119\n"
    "v_cvt_pk_bf16_f32 v154, v116, v117\n"
    "v_mfma_f32_16x16x32_bf16 v[76:79], a[148:151], v[212:215], v[76:79]\n"
    "v_cvt_pk_bf16_f32 v155, v118, v119\n"
    "v_cvt_pk_bf16_f32 v125, 0, v125\n"
    "v_mfma_f32_16x16x32_bf16 v[84:87], a[148:151], v[228:231], v[84:87]\n"
    "ds_read_b64_tr_b16 a[172:173], v12 offset:25344\n"
    "v_cvt_pk_bf16_f32 v126, 0, v126\n"
    "v_cvt_pk_bf16_f32 v127, 0, v127\n"
    "v_mfma_f32_16x16x32_bf16 v[92:95], a[148:151], v[244:247], v[92:95]\n"
    "ds_read_b64_tr_b16 a[174:175], v12 offset:29440\n"
    "v_fma_f32 v120, v120, s57, v166\n"
    "v_mfma_f32_16x16x32_bf16 v[48:51], a[224:227], v[152:155], v[184:187]\n"
    "buffer_load_dwordx4 v18, s[12:15], s21 offen offset:3072 lds\n"
    "v_fma_f32 v121, v121, s57, v166\n"
    "v_fma_f32 v122, v122, s57, v166\n"
    "v_mfma_f32_16x16x32_bf16 a[80:83], a[180:183], v[128:131], a[80:83]\n"
    "ds_read_b128 a[144:147], v4 offset:45056\n"
    "v_readlane_b32 s96, v20, s29\n"
    "v_fma_f32 v123, v123, s57, v166\n"
    "v_mfma_f32_16x16x32_bf16 a[84:87], a[180:183], v[132:135], a[84:87]\n"
    "s_add_u32 s21, s21, 0x4000\n"
    "v_readlane_b32 s97, v21, s29\n"
    "v_fma_f32 v124, v124, s57, v166\n"
    "v_mfma_f32_16x16x32_bf16 a[88:91], a[180:183], v[136:139], a[88:91]\n"
    "ds_read_b64_tr_b16 a[176:177], v12 offset:25600\n"
    "v_readlane_b32 s98, v22, s29\n"
    "v_fma_f32 v125, v125, s57, v166\n"
    "v_mfma_f32_16x16x32_bf16 a[92:95], a[180:183], v[140:143], a[92:95]\n"
    "ds_read_b64_tr_b16 a[178:179], v12 offset:29696\n"
    "v_cvt_pk_bf16_f32 v65, 0, v65\n"
    "v_mfma_f32_16x16x32_bf16 v[68:71], a[152:155], v[200:203], v[68:71]\n"
    "v_fma_f32 v126, v126, s57, v166\n"
    "v_exp_f32 v120, v120\n"
    "v_mfma_f32_16x16x32_bf16 v[76:79], a[152:155], v[216:219], v[76:79]\n"
    "v_exp_f32 v121, v121\n"
    "v_mfma_f32_16x16x32_bf16 v[84:87], a[152:155], v[232:235], v[84:87]\n"
    "v_fma_f32 v127, v127, s57, v166\n"
    "v_exp_f32 v122, v122\n"
    "v_mfma_f32_16x16x32_bf16 v[92:95], a[152:155], v[248:251], v[92:95]\n"
    "v_exp_f32 v123, v123\n"
    "v_cvt_pk_bf16_f32 v156, v120, v121\n"
    "v_mfma_f32_16x16x32_bf16 a[96:99], a[184:187], v[128:131], a[96:99]\n"
    "v_exp_f32 v124, v124\n"
    "v_mfma_f32_16x16x32_bf16 a[100:103], a[184:187], v[132:135], a[100:103]\n"
    "v_exp_f32 v125, v125\n"
    "v_exp_f32 v126, v126\n"
    "v_exp_f32 v127, v127\n"
    "v_mfma_f32_16x16x32_bf16 a[104:107], a[184:187], v[136:139], a[104:107]\n"
    "s_add_u32 s29, s29, 1\n"
    "ds_read_b128 a[148:151], v5 offset:45056\n"
    "v_mfma_f32_16x16x32_bf16 a[108:111], a[184:187], v[140:143], a[108:111]\n"
    "s_waitcnt lgkmcnt(1)\n"
    "v_cvt_pk_bf16_f32 v157, v122, v123\n"
    "v_cvt_pk_bf16_f32 v158, v124, v125\n"
    "v_cvt_pk_bf16_f32 v159, v126, v127\n"
    "v_mfma_f32_16x16x32_bf16 v[68:71], a[156:159], v[204:207], v[68:71]\n"
    "buffer_load_dword a250, v23, s[96:99], 0 offen\n"
    "v_mfma_f32_16x16x32_bf16 v[52:55], a[224:227], v[156:159], v[188:191]\n"
    "ds_read_b64_tr_b16 a[180:181], v12 offset:25856\n"
    "v_cvt_pk_bf16_f32 v66, 0, v66\n"
    "v_cvt_pk_bf16_f32 v67, 0, v67\n"
    "v_mfma_f32_16x16x32_bf16 v[76:79], a[156:159], v[220:223], v[76:79]\n"
    "ds_read_b64_tr_b16 a[182:183], v12 offset:29952\n"
    "v_cvt_pk_bf16_f32 v72, 0, v72\n"
    "v_mfma_f32_16x16x32_bf16 v[84:87], a[156:159], v[236:239], v[84:87]\n"
    "ds_read_b128 a[152:155], v6 offset:45056\n"
    "v_cvt_pk_bf16_f32 v73, 0, v73\n"
    "v_cvt_pk_bf16_f32 v74, 0, v74\n"
    "v_mfma_f32_16x16x32_bf16 v[92:95], a[156:159], v[252:255], v[92:95]\n"
    "ds_read_b64_tr_b16 a[184:185], v12 offset:26112\n"
    "v_cvt_pk_bf16_f32 v75, 0, v75\n"
    "v_cvt_pk_bf16_f32 v80, 0, v80\n"
    "v_mfma_f32_16x16x32_bf16 a[112:115], a[188:191], v[128:131], a[112:115]\n"
    "v_max3_f32 v19, v40, v44, v48\n"
    "v_cvt_pk_bf16_f32 v81, 0, v81\n"
    "v_mfma_f32_16x16x32_bf16 a[116:119], a[188:191], v[132:135], a[116:119]\n"
    "v_max_f32 v19, v19, v52\n"
    "v_cvt_pk_bf16_f32 v82, 0, v82\n"
    "v_mfma_f32_16x16x32_bf16 a[120:123], a[188:191], v[136:139], a[120:123]\n"
    "v_cmp_lt_f32 vcc, s25, v19\n"
    "v_cvt_pk_bf16_f32 v83, 0, v83\n"
    "v_cvt_pk_bf16_f32 v88, 0, v88\n"
    "v_mfma_f32_16x16x32_bf16 a[124:127], a[188:191], v[140:143], a[124:127]\n"
    "ds_read_b64_tr_b16 a[186:187], v12 offset:30208\n"
    "ds_read_b128 a[156:159], v7 offset:45056\n"
    "ds_read_b64_tr_b16 a[188:189], v12 offset:26368\n"
    "ds_read_b64_tr_b16 a[190:191], v12 offset:30464\n"
    "v_cvt_pk_bf16_f32 v89, 0, v89\n"
    "v_cvt_pk_bf16_f32 v90, 0, v90\n"
    "v_cvt_pk_bf16_f32 v91, 0, v91\n"
    "s_cbranch_vccnz RARE_b3_%=\n"
    "RARE_b3_%=_ret:\n"
    "v_mfma_f32_16x16x32_bf16 v[96:99], a[128:131], v[192:195], 0\n"
    "s_waitcnt vmcnt(9)\n"
    "s_barrier\n"
    "v_cvt_pk_bf16_f32 v68, 0, v68\n"
    "v_cvt_pk_bf16_f32 v69, 0, v69\n"
    "v_mfma_f32_16x16x32_bf16 v[104:107], a[128:131], v[208:211], 0\n"
    "v_cvt_pk_bf16_f32 v70, 0, v70\n"
    "v_cvt_pk_bf16_f32 v71, 0, v71\n"
    "v_mfma_f32_16x16x32_bf16 v[112:115], a[128:131], v[224:227], 0\n"
    "v_fma_f32 v64, v64, s57, v160\n"
    "v_fma_f32 v65, v65, s57, v160\n"
    "v_mfma_f32_16x16x32_bf16 v[120:123], a[128:131], v[240:243], 0\n"
    "v_fma_f32 v66, v66, s57, v160\n"
    "v_exp_f32 v64, v64\n"
    "v_mfma_f32_16x16x32_bf16 a[0:3], a[160:163], v[144:147], a[0:3]\n"
    "v_exp_f32 v65, v65\n"
    "v_mfma_f32_16x16x32_bf16 a[4:7], a[160:163], v[148:151], a[4:7]\n"
    "v_fma_f32 v67, v67, s57, v160\n"
    "v_exp_f32 v66, v66\n"
    "v_mfma_f32_16x16x32_bf16 a[8:11], a[160:163], v[152:155], a[8:11]\n"
    "v_fma_f32 v68, v68, s57, v160\n"
    "v_exp_f32 v67, v67\n"
    "v_mfma_f32_16x16x32_bf16 a[12:15], a[160:163], v[156:159], a[12:15]\n"
    "v_exp_f32 v68, v68\n"
    "v_mfma_f32_16x16x32_bf16 v[96:99], a[132:135], v[196:199], v[96:99]\n"
    "v_fma_f32 v69, v69, s57, v160\n"
    "v_exp_f32 v69, v69\n"
    "v_mfma_f32_16x16x32_bf16 v[104:107], a[132:135], v[212:215], v[104:107]\n"
    "ds_read_b128 a[128:131], v4 offset:49152\n"
    "v_cvt_pk_bf16_f32 v128, v64, v65\n"
    "v_mfma_f32_16x16x32_bf16 v[112:115], a[132:135], v[228:231], v[112:115]\n"
    "v_fma_f32 v70, v70, s57, v160\n"
    "v_exp_f32 v70, v70\n"
    "v_mfma_f32_16x16x32_bf16 v[120:123], a[132:135], v[244:247], v[120:123]\n"
    "v_fma_f32 v71, v71, s57, v160\n"
    "v_cvt_pk_bf16_f32 v129, v66, v67\n"
    "v_mfma_f32_16x16x32_bf16 a[16:19], a[164:167], v[144:147], a[16:19]\n"
    "v_exp_f32 v71, v71\n"
    "v_cvt_pk_bf16_f32 v130, v68, v69\n"
    "v_mfma_f32_16x16x32_bf16 a[20:23], a[164:167], v[148:151], a[20:23]\n"
    "v_cvt_pk_bf16_f32 v131, v70, v71\n"
    "v_cvt_pk_bf16_f32 v76, 0, v76\n"
    "v_mfma_f32_16x16x32_bf16 a[24:27], a[164:167], v[152:155], a[24:27]\n"
    "ds_read_b64_tr_b16 a[160:161], v12 offset:32768\n"
    "v_cvt_pk_bf16_f32 v77, 0, v77\n"
    "v_cvt_pk_bf16_f32 v78, 0, v78\n"
    "v_mfma_f32_16x16x32_bf16 a[28:31], a[164:167], v[156:159], a[28:31]\n"
    "ds_read_b64_tr_b16 a[162:163], v12 offset:36864\n"
    "v_cvt_pk_bf16_f32 v79, 0, v79\n"
    "v_mfma_f32_16x16x32_bf16 v[96:99], a[136:139], v[200:203], v[96:99]\n"
    "ds_read_b128 a[132:135], v5 offset:49152\n"
    "v_fma_f32 v72, v72, s57, v162\n"
    "v_fma_f32 v73, v73, s57, v162\n"
    "v_mfma_f32_16x16x32_bf16 v[104:107], a[136:139], v[216:219], v[104:107]\n"
    "v_fma_f32 v74, v74, s57, v162\n"
    "v_fma_f32 v75, v75, s57, v162\n"
    "v_mfma_f32_16x16x32_bf16 v[176:179], a[224:227], v[128:131], v[40:43]\n"
    "ds_read_b64_tr_b16 a[164:165], v12 offset:33024\n"
    "v_exp_f32 v72, v72\n"
    "v_mfma_f32_16x16x32_bf16 v[112:115], a[136:139], v[232:235], v[112:115]\n"
    "s_add_u32 m0, s22, 0x4000\n"
    "v_fma_f32 v76, v76, s57, v162\n"
    "v_mfma_f32_16x16x32_bf16 v[120:123], a[136:139], v[248:251], v[120:123]\n"
    "buffer_load_dwordx4 v13, s[8:11], s20 offen offset:0 lds\n"
    "v_exp_f32 v73, v73\n"
    "v_cvt_pk_bf16_f32 v84, 0, v84\n"
    "v_mfma_f32_16x16x32_bf16 a[32:35], a[168:171], v[144:147], a[32:35]\n"
    "v_fma_f32 v77, v77, s57, v162\n"
    "v_cvt_pk_bf16_f32 v132, v72, v73\n"
    "v_mfma_f32_16x16x32_bf16 a[36:39], a[168:171], v[148:151], a[36:39]\n"
    "ds_read_b64_tr_b16 a[166:167], v12 offset:37120\n"
    "v_exp_f32 v74, v74\n"
    "v_mfma_f32_16x16x32_bf16 a[40:43], a[168:171], v[152:155], a[40:43]\n"
    "v_fma_f32 v78, v78, s57, v162\n"
    "v_cvt_pk_bf16_f32 v85, 0, v85\n"
    "v_mfma_f32_16x16x32_bf16 a[44:47], a[168:171], v[156:159], a[44:47]\n"
    "v_exp_f32 v75, v75\n"
    "v_cvt_pk_bf16_f32 v86, 0, v86\n"
    "v_mfma_f32_16x16x32_bf16 v[96:99], a[140:143], v[204:207], v[96:99]\n"
    "v_fma_f32 v79, v79, s57, v162\n"
    "v_cvt_pk_bf16_f32 v133, v74, v75\n"
    "v_mfma_f32_16x16x32_bf16 v[104:107], a[140:143], v[220:223], v[104:107]\n"
    "s_waitcnt lgkmcnt(1)\n"
    "v_exp_f32 v76, v76\n"
    "v_cvt_pk_bf16_f32 v87, 0, v87\n"
    "v_mfma_f32_16x16x32_bf16 v[112:115], a[140:143], v[236:239], v[112:115]\n"
    "v_exp_f32 v77, v77\n"
    "v_mfma_f32_16x16x32_bf16 v[120:123], a[140:143], v[252:255], v[120:123]\n"
    "v_exp_f32 v78, v78\n"
    "v_exp_f32 v79, v79\n"
    "v_mfma_f32_16x16x32_bf16 a[48:51], a[172:175], v[144:147], a[48:51]\n"
    "v_cvt_pk_bf16_f32 v134, v76, v77\n"
    "v_mfma_f32_16x16x32_bf16 a[52:55], a[172:175], v[148:151], a[52:55]\n"
    "buffer_load_dwordx4 v14, s[8:11], s20 offen offset:1024 lds\n"
    "v_cvt_pk_bf16_f32 v135, v78, v79\n"
    "v_fma_f32 v80, v80, s57, v164\n"
    "v_mfma_f32_16x16x32_bf16 a[56:59], a[172:175], v[152:155], a[56:59]\n"
    "ds_read_b128 a[136:139], v6 offset:49152\n"
    "v_fma_f32 v81, v81, s57, v164\n"
    "v_mfma_f32_16x16x32_bf16 v[180:183], a[224:227], v[132:135], v[44:47]\n"
    "ds_read_b64_tr_b16 a[168:169], v12 offset:33280\n"
    "v_fma_f32 v82, v82, s57, v164\n"
    "v_fma_f32 v83, v83, s57, v164\n"
    "v_mfma_f32_16x16x32_bf16 a[60:63], a[172:175], v[156:159], a[60:63]\n"
    "ds_read_b64_tr_b16 a[170:171], v12 offset:37376\n"
    "v_fma_f32 v84, v84, s57, v164\n"
    "v_mfma_f32_16x16x32_bf16 v[100:103], a[144:147], v[192:195], 0\n"
    "ds_read_b128 a[140:143], v7 offset:49152\n"
    "v_exp_f32 v80, v80\n"
    "v_mfma_f32_16x16x32_bf16 v[108:111], a[144:147], v[208:211], 0\n"
    "v_fma_f32 v85, v85, s57, v164\n"
    "v_exp_f32 v81, v81\n"
    "v_mfma_f32_16x16x32_bf16 v[116:119], a[144:147], v[224:227], 0\n"
    "buffer_load_dwordx4 v15, s[8:11], s20 offen offset:2048 lds\n"
    "v_cvt_pk_bf16_f32 v136, v80, v81\n"
    "v_mfma_f32_16x16x32_bf16 v[124:127], a[144:147], v[240:243], 0\n"
    "v_fma_f32 v86, v86, s57, v164\n"
    "v_exp_f32 v82, v82\n"
    "v_mfma_f32_16x16x32_bf16 a[64:67], a[176:179], v[144:147], a[64:67]\n"
    "v_exp_f32 v83, v83\n"
    "v_mfma_f32_16x16x32_bf16 a[68:71], a[176:179], v[148:151], a[68:71]\n"
    "v_fma_f32 v87, v87, s57, v164\n"
    "v_exp_f32 v84, v84\n"
    "v_mfma_f32_16x16x32_bf16 a[72:75], a[176:179], v[152:155], a[72:75]\n"
    "v_exp_f32 v85, v85\n"
    "v_mfma_f32_16x16x32_bf16 a[76:79], a[176:179], v[156:159], a[76:79]\n"
    "v_exp_f32 v86, v86\n"
    "v_cvt_pk_bf16_f32 v137, v82, v83\n"
    "v_mfma_f32_16x16x32_bf16 v[100:103], a[148:151], v[196:199], v[100:103]\n"
    "v_exp_f32 v87, v87\n"
    "v_mfma_f32_16x16x32_bf16 v[108:111], a[148:151], v[212:215], v[108:111]\n"
    "v_cvt_pk_bf16_f32 v138, v84, v85\n"
    "v_cvt_pk_bf16_f32 v139, v86, v87\n"
    "v_cvt_pk_bf16_f32 v92, 0, v92\n"
    "v_mfma_f32_16x16x32_bf16 v[116:119], a[148:151], v[228:231], v[116:119]\n"
    "ds_read_b64_tr_b16 a[172:173], v12 offset:33536\n"
    "v_cvt_pk_bf16_f32 v93, 0, v93\n"
    "v_mfma_f32_16x16x32_bf16 v[124:127], a[148:151], v[244:247], v[124:127]\n"
    "ds_read_b64_tr_b16 a[174:175], v12 offset:37632\n"
    "v_cvt_pk_bf16_f32 v94, 0, v94\n"
    "v_cvt_pk_bf16_f32 v95, 0, v95\n"
    "v_mfma_f32_16x16x32_bf16 v[184:187], a[224:227], v[136:139], v[48:51]\n"
    "buffer_load_dwordx4 v16, s[8:11], s20 offen offset:3072 lds\n"
    "v_fma_f32 v88, v88, s57, v166\n"
    "v_mfma_f32_16x16x32_bf16 a[80:83], a[180:183], v[144:147], a[80:83]\n"
    "ds_read_b128 a[144:147], v4 offset:53248\n"
    "v_fma_f32 v89, v89, s57, v166\n"
    "v_fma_f32 v90, v90, s57, v166\n"
    "v_mfma_f32_16x16x32_bf16 a[84:87], a[180:183], v[148:151], a[84:87]\n"
    "s_add_u32 s20, s20, 0x4000\n"
    "ds_read_b64_tr_b16 a[176:177], v12 offset:33792\n"
    "v_fma_f32 v91, v91, s57, v166\n"
    "v_mfma_f32_16x16x32_bf16 a[88:91], a[180:183], v[152:155], a[88:91]\n"
    "ds_read_b64_tr_b16 a[178:179], v12 offset:37888\n"
    "v_fma_f32 v92, v92, s57, v166\n"
    "v_fma_f32 v93, v93, s57, v166\n"
    "v_mfma_f32_16x16x32_bf16 a[92:95], a[180:183], v[156:159], a[92:95]\n"
    "v_exp_f32 v88, v88\n"
    "v_mfma_f32_16x16x32_bf16 v[100:103], a[152:155], v[200:203], v[100:103]\n"
    "v_fma_f32 v94, v94, s57, v166\n"
    "v_exp_f32 v89, v89\n"
    "v_mfma_f32_16x16x32_bf16 v[108:111], a[152:155], v[216:219], v[108:111]\n"
    "v_exp_f32 v90, v90\n"
    "v_cvt_pk_bf16_f32 v140, v88, v89\n"
    "v_mfma_f32_16x16x32_bf16 v[116:119], a[152:155], v[232:235], v[116:119]\n"
    "v_fma_f32 v95, v95, s57, v166\n"
    "v_cvt_pk_bf16_f32 v96, 0, v96\n"
    "v_mfma_f32_16x16x32_bf16 v[124:127], a[152:155], v[248:251], v[124:127]\n"
    "v_exp_f32 v91, v91\n"
    "v_mfma_f32_16x16x32_bf16 a[96:99], a[184:187], v[144:147], a[96:99]\n"
    "v_exp_f32 v92, v92\n"
    "v_cvt_pk_bf16_f32 v141, v90, v91\n"
    "v_mfma_f32_16x16x32_bf16 a[100:103], a[184:187], v[148:151], a[100:103]\n"
    "v_exp_f32 v93, v93\n"
    "v_exp_f32 v94, v94\n"
    "v_exp_f32 v95, v95\n"
    "v_mfma_f32_16x16x32_bf16 a[104:107], a[184:187], v[152:155], a[104:107]\n"
    "ds_read_b128 a[148:151], v5 offset:53248\n"
    "v_mfma_f32_16x16x32_bf16 a[108:111], a[184:187], v[156:159], a[108:111]\n"
    "s_waitcnt lgkmcnt(1)\n"
    "v_cvt_pk_bf16_f32 v142, v92, v93\n"
    "v_cvt_pk_bf16_f32 v143, v94, v95\n"
    "v_mfma_f32_16x16x32_bf16 v[100:103], a[156:159], v[204:207], v[100:103]\n"
    "ds_read_b64_tr_b16 a[180:181], v12 offset:34048\n"
    "v_cvt_pk_bf16_f32 v97, 0, v97\n"
    "v_mfma_f32_16x16x32_bf16 v[188:191], a[224:227], v[140:143], v[52:55]\n"
    "ds_read_b64_tr_b16 a[182:183], v12 offset:38144\n"
    "v_cvt_pk_bf16_f32 v98, 0, v98\n"
    "v_cvt_pk_bf16_f32 v99, 0, v99\n"
    "v_mfma_f32_16x16x32_bf16 v[108:111], a[156:159], v[220:223], v[108:111]\n"
    "ds_read_b128 a[152:155], v6 offset:53248\n"
    "v_cvt_pk_bf16_f32 v104, 0, v104\n"
    "v_mfma_f32_16x16x32_bf16 v[116:119], a[156:159], v[236:239], v[116:119]\n"
    "ds_read_b64_tr_b16 a[184:185], v12 offset:34304\n"
    "v_cvt_pk_bf16_f32 v105, 0, v105\n"
    "v_cvt_pk_bf16_f32 v106, 0, v106\n"
    "v_mfma_f32_16x16x32_bf16 v[124:127], a[156:159], v[252:255], v[124:127]\n"
    "ds_read_b64_tr_b16 a[186:187], v12 offset:38400\n"
    "v_cvt_pk_bf16_f32 v107, 0, v107\n"
    "v_mfma_f32_16x16x32_bf16 a[112:115], a[188:191], v[144:147], a[112:115]\n"
    "v_max3_f32 v19, v176, v180, v184\n"
    "v_cvt_pk_bf16_f32 v112, 0, v112\n"
    "v_cvt_pk_bf16_f32 v113, 0, v113\n"
    "v_mfma_f32_16x16x32_bf16 a[116:119], a[188:191], v[148:151], a[116:119]\n"
    "v_max_f32 v19, v19, v188\n"
    "v_cvt_pk_bf16_f32 v114, 0, v114\n"
    "v_mfma_f32_16x16x32_bf16 a[120:123], a[188:191], v[152:155], a[120:123]\n"
    "v_cmp_lt_f32 vcc, s25, v19\n"
    "v_cvt_pk_bf16_f32 v115, 0, v115\n"
    "v_mfma_f32_16x16x32_bf16 a[124:127], a[188:191], v[156:159], a[124:127]\n"
    "ds_read_b128 a[156:159], v7 offset:53248\n"
    "ds_read_b64_tr_b16 a[188:189], v12 offset:34560\n"
    "ds_read_b64_tr_b16 a[190:191], v12 offset:38656\n"
    "v_cvt_pk_bf16_f32 v120, 0, v120\n"
    "v_cvt_pk_bf16_f32 v121, 0, v121\n"
    "v_cvt_pk_bf16_f32 v122, 0, v122\n"
    "v_cvt_pk_bf16_f32 v123, 0, v123\n"
    "s_cbranch_vccnz RARE_b4_%=\n"
    "RARE_b4_%=_ret:\n"
    "v_mfma_f32_16x16x32_bf16 v[64:67], a[128:131], v[192:195], 0\n"
    "v_cvt_pk_bf16_f32 v100, 0, v100\n"
    "v_cvt_pk_bf16_f32 v101, 0, v101\n"
    "v_mfma_f32_16x16x32_bf16 v[72:75], a[128:131], v[208:211], 0\n"
    "v_cvt_pk_bf16_f32 v102, 0, v102\n"
    "v_cvt_pk_bf16_f32 v103, 0, v103\n"
    "v_mfma_f32_16x16x32_bf16 v[80:83], a[128:131], v[224:227], 0\n"
    "v_fma_f32 v96, v96, s57, v160\n"
    "v_fma_f32 v97, v97, s57, v160\n"
    "v_mfma_f32_16x16x32_bf16 v[88:91], a[128:131], v[240:243], 0\n"
    "v_fma_f32 v98, v98, s57, v160\n"
    "v_exp_f32 v96, v96\n"
    "v_mfma_f32_16x16x32_bf16 a[0:3], a[160:163], v[128:131], a[0:3]\n"
    "v_exp_f32 v97, v97\n"
    "v_mfma_f32_16x16x32_bf16 a[4:7], a[160:163], v[132:135], a[4:7]\n"
    "v_fma_f32 v99, v99, s57, v160\n"
    "v_exp_f32 v98, v98\n"
    "v_mfma_f32_16x16x32_bf16 a[8:11], a[160:163], v[136:139], a[8:11]\n"
    "v_fma_f32 v100, v100, s57, v160\n"
    "v_exp_f32 v99, v99\n"
    "v_mfma_f32_16x16x32_bf16 a[12:15], a[160:163], v[140:143], a[12:15]\n"
    "v_exp_f32 v100, v100\n"
    "v_mfma_f32_16x16x32_bf16 v[64:67], a[132:135], v[196:199], v[64:67]\n"
    "v_fma_f32 v101, v101, s57, v160\n"
    "v_exp_f32 v101, v101\n"
    "v_mfma_f32_16x16x32_bf16 v[72:75], a[132:135], v[212:215], v[72:75]\n"
    "ds_read_b128 a[128:131], v4 offset:57344\n"
    "v_cvt_pk_bf16_f32 v144, v96, v97\n"
    "v_mfma_f32_16x16x32_bf16 v[80:83], a[132:135], v[228:231], v[80:83]\n"
    "v_fma_f32 v102, v102, s57, v160\n"
    "v_exp_f32 v102, v102\n"
    "v_mfma_f32_16x16x32_bf16 v[88:91], a[132:135], v[244:247], v[88:91]\n"
    "v_fma_f32 v103, v103, s57, v160\n"
    "v_cvt_pk_bf16_f32 v145, v98, v99\n"
    "v_mfma_f32_16x16x32_bf16 a[16:19], a[164:167], v[128:131], a[16:19]\n"
    "v_exp_f32 v103, v103\n"
    "v_cvt_pk_bf16_f32 v146, v100, v101\n"
    "v_mfma_f32_16x16x32_bf16 a[20:23], a[164:167], v[132:135], a[20:23]\n"
    "v_cvt_pk_bf16_f32 v147, v102, v103\n"
    "v_cvt_pk_bf16_f32 v108, 0, v108\n"
    "v_mfma_f32_16x16x32_bf16 a[24:27], a[164:167], v[136:139], a[24:27]\n"
    "ds_read_b64_tr_b16 a[160:161], v12 offset:40960\n"
    "v_cvt_pk_bf16_f32 v109, 0, v109\n"
    "v_cvt_pk_bf16_f32 v110, 0, v110\n"
    "v_mfma_f32_16x16x32_bf16 a[28:31], a[164:167], v[140:143], a[28:31]\n"
    "ds_read_b64_tr_b16 a[162:163], v12 offset:45056\n"
    "v_cvt_pk_bf16_f32 v111, 0, v111\n"
    "v_mfma_f32_16x16x32_bf16 v[64:67], a[136:139], v[200:203], v[64:67]\n"
    "ds_read_b128 a[132:135], v5 offset:57344\n"
    "v_fma_f32 v104, v104, s57, v162\n"
    "v_fma_f32 v105, v105, s57, v162\n"
    "v_mfma_f32_16x16x32_bf16 v[72:75], a[136:139], v[216:219], v[72:75]\n"
    "v_fma_f32 v106, v106, s57, v162\n"
    "v_fma_f32 v107, v107, s57, v162\n"
    "v_mfma_f32_16x16x32_bf16 v[40:43], a[224:227], v[144:147], v[176:179]\n"
    "ds_read_b64_tr_b16 a[164:165], v12 offset:41216\n"
    "v_exp_f32 v104, v104\n"
    "v_mfma_f32_16x16x32_bf16 v[80:83], a[136:139], v[232:235], v[80:83]\n"
    "s_add_u32 m0, s23, 0x0\n"
    "v_fma_f32 v108, v108, s57, v162\n"
    "v_mfma_f32_16x16x32_bf16 v[88:91], a[136:139], v[248:251], v[88:91]\n"
    "buffer_load_dwordx4 v17, s[12:15], s21 offen offset:0 lds\n"
    "v_exp_f32 v105, v105\n"
    "v_cvt_pk_bf16_f32 v116, 0, v116\n"
    "v_mfma_f32_16x16x32_bf16 a[32:35], a[168:171], v[128:131], a[32:35]\n"
    "v_fma_f32 v109, v109, s57, v162\n"
    "v_cvt_pk_bf16_f32 v148, v104, v105\n"
    "v_mfma_f32_16x16x32_bf16 a[36:39], a[168:171], v[132:135], a[36:39]\n"
    "ds_read_b64_tr_b16 a[166:167], v12 offset:45312\n"
    "v_exp_f32 v106, v106\n"
    "v_mfma_f32_16x16x32_bf16 a[40:43], a[168:171], v[136:139], a[40:43]\n"
    "v_fma_f32 v110, v110, s57, v162\n"
    "v_cvt_pk_bf16_f32 v117, 0, v117\n"
    "v_mfma_f32_16x16x32_bf16 a[44:47], a[168:171], v[140:143], a[44:47]\n"
    "v_exp_f32 v107, v107\n"
    "v_cvt_pk_bf16_f32 v118, 0, v118\n"
    "v_mfma_f32_16x16x32_bf16 v[64:67], a[140:143], v[204:207], v[64:67]\n"
    "v_fma_f32 v111, v111, s57, v162\n"
    "v_cvt_pk_bf16_f32 v149, v106, v107\n"
    "v_mfma_f32_16x16x32_bf16 v[72:75], a[140:143], v[220:223], v[72:75]\n"
    "s_waitcnt lgkmcnt(1)\n"
    "v_exp_f32 v108, v108\n"
    "v_cvt_pk_bf16_f32 v119, 0, v119\n"
    "v_mfma_f32_16x16x32_bf16 v[80:83], a[140:143], v[236:239], v[80:83]\n"
    "v_exp_f32 v109, v109\n"
    "v_mfma_f32_16x16x32_bf16 v[88:91], a[140:143], v[252:255], v[88:91]\n"
    "v_exp_f32 v110, v110\n"
    "v_exp_f32 v111, v111\n"
    "v_mfma_f32_16x16x32_bf16 a[48:51], a[172:175], v[128:131], a[48:51]\n"
    "v_cvt_pk_bf16_f32 v150, v108, v109\n"
    "v_mfma_f32_16x16x32_bf16 a[52:55], a[172:175], v[132:135], a[52:55]\n"
    "buffer_load_dwordx4 v18, s[12:15], s21 offen offset:1024 lds\n"
    "v_cvt_pk_bf16_f32 v151, v110, v111\n"
    "v_fma_f32 v112, v112, s57, v164\n"
    "v_mfma_f32_16x16x32_bf16 a[56:59], a[172:175], v[136:139], a[56:59]\n"
    "ds_read_b128 a[136:139], v6 offset:57344\n"
    "v_fma_f32 v113, v113, s57, v164\n"
    "v_mfma_f32_16x16x32_bf16 v[44:47], a[224:227], v[148:151], v[180:183]\n"
    "ds_read_b64_tr_b16 a[168:169], v12 offset:41472\n"
    "v_fma_f32 v114, v114, s57, v164\n"
    "v_fma_f32 v115, v115, s57, v164\n"
    "v_mfma_f32_16x16x32_bf16 a[60:63], a[172:175], v[140:143], a[60:63]\n"
    "ds_read_b64_tr_b16 a[170:171], v12 offset:45568\n"
    "v_fma_f32 v116, v116, s57, v164\n"
    "v_mfma_f32_16x16x32_bf16 v[68:71], a[144:147], v[192:195], 0\n"
    "ds_read_b128 a[140:143], v7 offset:57344\n"
    "v_exp_f32 v112, v112\n"
    "v_mfma_f32_16x16x32_bf16 v[76:79], a[144:147], v[208:211], 0\n"
    "v_fma_f32 v117, v117, s57, v164\n"
    "v_exp_f32 v113, v113\n"
    "v_mfma_f32_16x16x32_bf16 v[84:87], a[144:147], v[224:227], 0\n"
    "buffer_load_dwordx4 v17, s[12:15], s21 offen offset:2048 lds\n"
    "v_cvt_pk_bf16_f32 v152, v112, v113\n"
    "v_mfma_f32_16x16x32_bf16 v[92:95], a[144:147], v[240:243], 0\n"
    "v_fma_f32 v118, v118, s57, v164\n"
    "v_exp_f32 v114, v114\n"
    "v_mfma_f32_16x16x32_bf16 a[64:67], a[176:179], v[128:131], a[64:67]\n"
    "v_exp_f32 v115, v115\n"
    "v_mfma_f32_16x16x32_bf16 a[68:71], a[176:179], v[132:135], a[68:71]\n"
    "v_fma_f32 v119, v119, s57, v164\n"
    "v_exp_f32 v116, v116\n"
    "v_mfma_f32_16x16x32_bf16 a[72:75], a[176:179], v[136:139], a[72:75]\n"
    "v_exp_f32 v117, v117\n"
    "v_mfma_f32_16x16x32_bf16 a[76:79], a[176:179], v[140:143], a[76:79]\n"
    "v_exp_f32 v118, v118\n"
    "v_cvt_pk_bf16_f32 v153, v114, v115\n"
    "v_mfma_f32_16x16x32_bf16 v[68:71], a[148:151], v[196:199], v[68:71]\n"
    "v_exp_f32 v119, v119\n"
    "v_mfma_f32_16x16x32_bf16 v[76:79], a[148:151], v[212:215], v[76:79]\n"
    "v_cvt_pk_bf16_f32 v154, v116, v117\n"
    "v_cvt_pk_bf16_f32 v155, v118, v119\n"
    "v_cvt_pk_bf16_f32 v124, 0, v124\n"
    "v_mfma_f32_16x16x32_bf16 v[84:87], a[148:151], v[228:231], v[84:87]\n"
    "ds_read_b64_tr_b16 a[172:173], v12 offset:41728\n"
    "v_cvt_pk_bf16_f32 v125, 0, v125\n"
    "v_mfma_f32_16x16x32_bf16 v[92:95], a[148:151], v[244:247], v[92:95]\n"
    "ds_read_b64_tr_b16 a[174:175], v12 offset:45824\n"
    "v_cvt_pk_bf16_f32 v126, 0, v126\n"
    "v_cvt_pk_bf16_f32 v127, 0, v127\n"
    "v_mfma_f32_16x16x32_bf16 v[48:51], a[224:227], v[152:155], v[184:187]\n"
    "buffer_load_dwordx4 v18, s[12:15], s21 offen offset:3072 lds\n"
    "v_fma_f32 v120, v120, s57, v166\n"
    "v_mfma_f32_16x16x32_bf16 a[80:83], a[180:183], v[128:131], a[80:83]\n"
    "ds_read_b128 a[144:147], v4 offset:61440\n"
    "v_fma_f32 v121, v121, s57, v166\n"
    "v_fma_f32 v122, v122, s57, v166\n"
    "v_mfma_f32_16x16x32_bf16 a[84:87], a[180:183], v[132:135], a[84:87]\n"
    "s_add_u32 s21, s21, 0x4000\n"
    "ds_read_b64_tr_b16 a[176:177], v12 offset:41984\n"
    "v_fma_f32 v123, v123, s57, v166\n"
    "v_mfma_f32_16x16x32_bf16 a[88:91], a[180:183], v[136:139], a[88:91]\n"
    "ds_read_b64_tr_b16 a[178:179], v12 offset:46080\n"
    "v_fma_f32 v124, v124, s57, v166\n"
    "v_fma_f32 v125, v125, s57, v166\n"
    "v_mfma_f32_16x16x32_bf16 a[92:95], a[180:183], v[140:143], a[92:95]\n"
    "v_exp_f32 v120, v120\n"
    "v_mfma_f32_16x16x32_bf16 v[68:71], a[152:155], v[200:203], v[68:71]\n"
    "v_fma_f32 v126, v126, s57, v166\n"
    "v_exp_f32 v121, v121\n"
    "v_mfma_f32_16x16x32_bf16 v[76:79], a[152:155], v[216:219], v[76:79]\n"
    "v_exp_f32 v122, v122\n"
    "v_cvt_pk_bf16_f32 v156, v120, v121\n"
    "v_mfma_f32_16x16x32_bf16 v[84:87], a[152:155], v[232:235], v[84:87]\n"
    "v_fma_f32 v127, v127, s57, v166\n"
    "v_cvt_pk_bf16_f32 v64, 0, v64\n"
    "v_mfma_f32_16x16x32_bf16 v[92:95], a[152:155], v[248:251], v[92:95]\n"
    "v_exp_f32 v123, v123\n"
    "v_mfma_f32_16x16x32_bf16 a[96:99], a[184:187], v[128:131], a[96:99]\n"
    "v_exp_f32 v124, v124\n"
    "v_cvt_pk_bf16_f32 v157, v122, v123\n"
    "v_mfma_f32_16x16x32_bf16 a[100:103], a[184:187], v[132:135], a[100:103]\n"
    "v_exp_f32 v125, v125\n"
    "v_exp_f32 v126, v126\n"
    "v_exp_f32 v127, v127\n"
    "v_mfma_f32_16x16x32_bf16 a[104:107], a[184:187], v[136:139], a[104:107]\n"
    "ds_read_b128 a[148:151], v5 offset:61440\n"
    "v_mfma_f32_16x16x32_bf16 a[108:111], a[184:187], v[140:143], a[108:111]\n"
    "s_waitcnt lgkmcnt(1)\n"
    "v_cvt_pk_bf16_f32 v158, v124, v125\n"
    "v_cvt_pk_bf16_f32 v159, v126, v127\n"
    "v_mfma_f32_16x16x32_bf16 v[68:71], a[156:159], v[204:207], v[68:71]\n"
    "ds_read_b64_tr_b16 a[180:181], v12 offset:42240\n"
    "v_cvt_pk_bf16_f32 v65, 0, v65\n"
    "v_mfma_f32_16x16x32_bf16 v[52:55], a[224:227], v[156:159], v[188:191]\n"
    "ds_read_b64_tr_b16 a[182:183], v12 offset:46336\n"
    "v_cvt_pk_bf16_f32 v66, 0, v66\n"
    "v_cvt_pk_bf16_f32 v67, 0, v67\n"
    "v_mfma_f32_16x16x32_bf16 v[76:79], a[156:159], v[220:223], v[76:79]\n"
    "ds_read_b128 a[152:155], v6 offset:61440\n"
    "v_cvt_pk_bf16_f32 v72, 0, v72\n"
    "v_mfma_f32_16x16x32_bf16 v[84:87], a[156:159], v[236:239], v[84:87]\n"
    "ds_read_b64_tr_b16 a[184:185], v12 offset:42496\n"
    "v_cvt_pk_bf16_f32 v73, 0, v73\n"
    "v_cvt_pk_bf16_f32 v74, 0, v74\n"
    "v_mfma_f32_16x16x32_bf16 v[92:95], a[156:159], v[252:255], v[92:95]\n"
    "ds_read_b64_tr_b16 a[186:187], v12 offset:46592\n"
    "v_cvt_pk_bf16_f32 v75, 0, v75\n"
    "v_mfma_f32_16x16x32_bf16 a[112:115], a[188:191], v[128:131], a[112:115]\n"
    "v_max3_f32 v19, v40, v44, v48\n"
    "v_cvt_pk_bf16_f32 v80, 0, v80\n"
    "v_cvt_pk_bf16_f32 v81, 0, v81\n"
    "v_mfma_f32_16x16x32_bf16 a[116:119], a[188:191], v[132:135], a[116:119]\n"
    "v_max_f32 v19, v19, v52\n"
    "v_cvt_pk_bf16_f32 v82, 0, v82\n"
    "v_mfma_f32_16x16x32_bf16 a[120:123], a[188:191], v[136:139], a[120:123]\n"
    "v_cmp_lt_f32 vcc, s25, v19\n"
    "v_cvt_pk_bf16_f32 v83, 0, v83\n"
    "v_mfma_f32_16x16x32_bf16 a[124:127], a[188:191], v[140:143], a[124:127]\n"
    "ds_read_b128 a[156:159], v7 offset:61440\n"
    "ds_read_b64_tr_b16 a[188:189], v12 offset:42752\n"
    "ds_read_b64_tr_b16 a[190:191], v12 offset:46848\n"
    "v_cvt_pk_bf16_f32 v88, 0, v88\n"
    "v_cvt_pk_bf16_f32 v89, 0, v89\n"
    "v_cvt_pk_bf16_f32 v90, 0, v90\n"
    "v_cvt_pk_bf16_f32 v91, 0, v91\n"
    "s_cbranch_vccnz RARE_b5_%=\n"
    "RARE_b5_%=_ret:\n"
    "v_mfma_f32_16x16x32_bf16 v[96:99], a[128:131], v[192:195], 0\n"
    "s_waitcnt vmcnt(9)\n"
    "s_barrier\n"
    "v_cvt_pk_bf16_f32 v68, 0, v68\n"
    "v_cvt_pk_bf16_f32 v69, 0, v69\n"
    "v_mfma_f32_16x16x32_bf16 v[104:107], a[128:131], v[208:211], 0\n"
    "v_cvt_pk_bf16_f32 v70, 0, v70\n"
    "v_cvt_pk_bf16_f32 v71, 0, v71\n"
    "v_mfma_f32_16x16x32_bf16 v[112:115], a[128:131], v[224:227], 0\n"
    "v_fma_f32 v64, v64, s57, v160\n"
    "v_fma_f32 v65, v65, s57, v160\n"
    "v_mfma_f32_16x16x32_bf16 v[120:123], a[128:131], v[240:243], 0\n"
    "v_fma_f32 v66, v66, s57, v160\n"
    "v_exp_f32 v64, v64\n"
    "v_mfma_f32_16x16x32_bf16 a[0:3], a[160:163], v[144:147], a[0:3]\n"
    "v_exp_f32 v65, v65\n"
    "v_mfma_f32_16x16x32_bf16 a[4:7], a[160:163], v[148:151], a[4:7]\n"
    "v_fma_f32 v67, v67, s57, v160\n"
    "v_exp_f32 v66, v66\n"
    "v_mfma_f32_16x16x32_bf16 a[8:11], a[160:163], v[152:155], a[8:11]\n"
    "v_fma_f32 v68, v68, s57, v160\n"
    "v_exp_f32 v67, v67\n"
    "v_mfma_f32_16x16x32_bf16 a[12:15], a[160:163], v[156:159], a[12:15]\n"
    "v_exp_f32 v68, v68\n"
    "v_mfma_f32_16x16x32_bf16 v[96:99], a[132:135], v[196:199], v[96:99]\n"
    "v_fma_f32 v69, v69, s57, v160\n"
    "v_exp_f32 v69, v69\n"
    "v_mfma_f32_16x16x32_bf16 v[104:107], a[132:135], v[212:215], v[104:107]\n"
    "ds_read_b128 a[128:131], v4 offset:0\n"
    "v_cvt_pk_bf16_f32 v128, v64, v65\n"
    "v_mfma_f32_16x16x32_bf16 v[112:115], a[132:135], v[228:231], v[112:115]\n"
    "v_fma_f32 v70, v70, s57, v160\n"
    "v_exp_f32 v70, v70\n"
    "v_mfma_f32_16x16x32_bf16 v[120:123], a[132:135], v[244:247], v[120:123]\n"
    "v_fma_f32 v71, v71, s57, v160\n"
    "v_cvt_pk_bf16_f32 v129, v66, v67\n"
    "v_mfma_f32_16x16x32_bf16 a[16:19], a[164:167], v[144:147], a[16:19]\n"
    "v_exp_f32 v71, v71\n"
    "v_cvt_pk_bf16_f32 v130, v68, v69\n"
    "v_mfma_f32_16x16x32_bf16 a[20:23], a[164:167], v[148:151], a[20:23]\n"
    "v_cvt_pk_bf16_f32 v131, v70, v71\n"
    "v_cvt_pk_bf16_f32 v76, 0, v76\n"
    "v_mfma_f32_16x16x32_bf16 a[24:27], a[164:167], v[152:155], a[24:27]\n"
    "ds_read_b64_tr_b16 a[160:161], v12 offset:49152\n"
    "v_cvt_pk_bf16_f32 v77, 0, v77\n"
    "v_cvt_pk_bf16_f32 v78, 0, v78\n"
    "v_mfma_f32_16x16x32_bf16 a[28:31], a[164:167], v[156:159], a[28:31]\n"
    "ds_read_b64_tr_b16 a[162:163], v12 offset:53248\n"
    "v_cvt_pk_bf16_f32 v79, 0, v79\n"
    "v_mfma_f32_16x16x32_bf16 v[96:99], a[136:139], v[200:203], v[96:99]\n"
    "ds_read_b128 a[132:135], v5 offset:0\n"
    "v_fma_f32 v72, v72, s57, v162\n"
    "v_fma_f32 v73, v73, s57, v162\n"
    "v_mfma_f32_16x16x32_bf16 v[104:107], a[136:139], v[216:219], v[104:107]\n"
    "v_fma_f32 v74, v74, s57, v162\n"
    "v_fma_f32 v75, v75, s57, v162\n"
    "v_mfma_f32_16x16x32_bf16 v[176:179], a[224:227], v[128:131], v[40:43]\n"
    "ds_read_b64_tr_b16 a[164:165], v12 offset:49408\n"
    "v_exp_f32 v72, v72\n"
    "v_mfma_f32_16x16x32_bf16 v[112:115], a[136:139], v[232:235], v[112:115]\n"
    "s_add_u32 m0, s22, 0x8000\n"
    "v_fma_f32 v76, v76, s57, v162\n"
    "v_mfma_f32_16x16x32_bf16 v[120:123], a[136:139], v[248:251], v[120:123]\n"
    "buffer_load_dwordx4 v13, s[8:11], s20 offen offset:0 lds\n"
    "v_exp_f32 v73, v73\n"
    "v_cvt_pk_bf16_f32 v84, 0, v84\n"
    "v_mfma_f32_16x16x32_bf16 a[32:35], a[168:171], v[144:147], a[32:35]\n"
    "v_fma_f32 v77, v77, s57, v162\n"
    "v_cvt_pk_bf16_f32 v132, v72, v73\n"
    "v_mfma_f32_16x16x32_bf16 a[36:39], a[168:171], v[148:151], a[36:39]\n"
    "ds_read_b64_tr_b16 a[166:167], v12 offset:53504\n"
    "v_exp_f32 v74, v74\n"
    "v_mfma_f32_16x16x32_bf16 a[40:43], a[168:171], v[152:155], a[40:43]\n"
    "v_fma_f32 v78, v78, s57, v162\n"
    "v_cvt_pk_bf16_f32 v85, 0, v85\n"
    "v_mfma_f32_16x16x32_bf16 a[44:47], a[168:171], v[156:159], a[44:47]\n"
    "v_exp_f32 v75, v75\n"
    "v_cvt_pk_bf16_f32 v86, 0, v86\n"
    "v_mfma_f32_16x16x32_bf16 v[96:99], a[140:143], v[204:207], v[96:99]\n"
    "v_fma_f32 v79, v79, s57, v162\n"
    "v_cvt_pk_bf16_f32 v133, v74, v75\n"
    "v_mfma_f32_16x16x32_bf16 v[104:107], a[140:143], v[220:223], v[104:107]\n"
    "s_waitcnt lgkmcnt(1)\n"
    "v_exp_f32 v76, v76\n"
    "v_cvt_pk_bf16_f32 v87, 0, v87\n"
    "v_mfma_f32_16x16x32_bf16 v[112:115], a[140:143], v[236:239], v[112:115]\n"
    "v_exp_f32 v77, v77\n"
    "v_mfma_f32_16x16x32_bf16 v[120:123], a[140:143], v[252:255], v[120:123]\n"
    "v_exp_f32 v78, v78\n"
    "v_exp_f32 v79, v79\n"
    "v_mfma_f32_16x16x32_bf16 a[48:51], a[172:175], v[144:147], a[48:51]\n"
    "v_cvt_pk_bf16_f32 v134, v76, v77\n"
    "v_mfma_f32_16x16x32_bf16 a[52:55], a[172:175], v[148:151], a[52:55]\n"
    "buffer_load_dwordx4 v14, s[8:11], s20 offen offset:1024 lds\n"
    "v_cvt_pk_bf16_f32 v135, v78, v79\n"
    "v_fma_f32 v80, v80, s57, v164\n"
    "v_mfma_f32_16x16x32_bf16 a[56:59], a[172:175], v[152:155], a[56:59]\n"
    "ds_read_b128 a[136:139], v6 offset:0\n"
    "v_fma_f32 v81, v81, s57, v164\n"
    "v_mfma_f32_16x16x32_bf16 v[180:183], a[224:227], v[132:135], v[44:47]\n"
    "ds_read_b64_tr_b16 a[168:169], v12 offset:49664\n"
    "v_fma_f32 v82, v82, s57, v164\n"
    "v_fma_f32 v83, v83, s57, v164\n"
    "v_mfma_f32_16x16x32_bf16 a[60:63], a[172:175], v[156:159], a[60:63]\n"
    "ds_read_b64_tr_b16 a[170:171], v12 offset:53760\n"
    "v_fma_f32 v84, v84, s57, v164\n"
    "v_mfma_f32_16x16x32_bf16 v[100:103], a[144:147], v[192:195], 0\n"
    "ds_read_b128 a[140:143], v7 offset:0\n"
    "v_exp_f32 v80, v80\n"
    "v_mfma_f32_16x16x32_bf16 v[108:111], a[144:147], v[208:211], 0\n"
    "v_fma_f32 v85, v85, s57, v164\n"
    "v_exp_f32 v81, v81\n"
    "v_mfma_f32_16x16x32_bf16 v[116:119], a[144:147], v[224:227], 0\n"
    "buffer_load_dwordx4 v15, s[8:11], s20 offen offset:2048 lds\n"
    "v_cvt_pk_bf16_f32 v136, v80, v81\n"
    "v_mfma_f32_16x16x32_bf16 v[124:127], a[144:147], v[240:243], 0\n"
    "v_fma_f32 v86, v86, s57, v164\n"
    "v_exp_f32 v82, v82\n"
    "v_mfma_f32_16x16x32_bf16 a[64:67], a[176:179], v[144:147], a[64:67]\n"
    "v_exp_f32 v83, v83\n"
    "v_mfma_f32_16x16x32_bf16 a[68:71], a[176:179], v[148:151], a[68:71]\n"
    "v_fma_f32 v87, v87, s57, v164\n"
    "v_exp_f32 v84, v84\n"
    "v_mfma_f32_16x16x32_bf16 a[72:75], a[176:179], v[152:155], a[72:75]\n"
    "v_exp_f32 v85, v85\n"
    "v_mfma_f32_16x16x32_bf16 a[76:79], a[176:179], v[156:159], a[76:79]\n"
    "v_exp_f32 v86, v86\n"
    "v_cvt_pk_bf16_f32 v137, v82, v83\n"
    "v_mfma_f32_16x16x32_bf16 v[100:103], a[148:151], v[196:199], v[100:103]\n"
    "v_exp_f32 v87, v87\n"
    "v_mfma_f32_16x16x32_bf16 v[108:111], a[148:151], v[212:215], v[108:111]\n"
    "v_cvt_pk_bf16_f32 v138, v84, v85\n"
    "v_cvt_pk_bf16_f32 v139, v86, v87\n"
    "v_cvt_pk_bf16_f32 v92, 0, v92\n"
    "v_mfma_f32_16x16x32_bf16 v[116:119], a[148:151], v[228:231], v[116:119]\n"
    "ds_read_b64_tr_b16 a[172:173], v12 offset:49920\n"
    "v_cvt_pk_bf16_f32 v93, 0, v93\n"
    "v_mfma_f32_16x16x32_bf16 v[124:127], a[148:151], v[244:247], v[124:127]\n"
    "ds_read_b64_tr_b16 a[174:175], v12 offset:54016\n"
    "v_cvt_pk_bf16_f32 v94, 0, v94\n"
    "v_cvt_pk_bf16_f32 v95, 0, v95\n"
    "v_mfma_f32_16x16x32_bf16 v[184:187], a[224:227], v[136:139], v[48:51]\n"
    "buffer_load_dwordx4 v16, s[8:11], s20 offen offset:3072 lds\n"
    "v_fma_f32 v88, v88, s57, v166\n"
    "v_mfma_f32_16x16x32_bf16 a[80:83], a[180:183], v[144:147], a[80:83]\n"
    "ds_read_b128 a[144:147], v4 offset:4096\n"
    "v_fma_f32 v89, v89, s57, v166\n"
    "v_fma_f32 v90, v90, s57, v166\n"
    "v_mfma_f32_16x16x32_bf16 a[84:87], a[180:183], v[148:151], a[84:87]\n"
    "s_add_u32 s20, s20, 0x4000\n"
    "ds_read_b64_tr_b16 a[176:177], v12 offset:50176\n"
    "v_fma_f32 v91, v91, s57, v166\n"
    "v_mfma_f32_16x16x32_bf16 a[88:91], a[180:183], v[152:155], a[88:91]\n"
    "ds_read_b64_tr_b16 a[178:179], v12 offset:54272\n"
    "v_fma_f32 v92, v92, s57, v166\n"
    "v_fma_f32 v93, v93, s57, v166\n"
    "v_mfma_f32_16x16x32_bf16 a[92:95], a[180:183], v[156:159], a[92:95]\n"
    "v_exp_f32 v88, v88\n"
    "v_mfma_f32_16x16x32_bf16 v[100:103], a[152:155], v[200:203], v[100:103]\n"
    "v_fma_f32 v94, v94, s57, v166\n"
    "v_exp_f32 v89, v89\n"
    "v_mfma_f32_16x16x32_bf16 v[108:111], a[152:155], v[216:219], v[108:111]\n"
    "v_exp_f32 v90, v90\n"
    "v_cvt_pk_bf16_f32 v140, v88, v89\n"
    "v_mfma_f32_16x16x32_bf16 v[116:119], a[152:155], v[232:235], v[116:119]\n"
    "v_fma_f32 v95, v95, s57, v166\n"
    "v_cvt_pk_bf16_f32 v96, 0, v96\n"
    "v_mfma_f32_16x16x32_bf16 v[124:127], a[152:155], v[248:251], v[124:127]\n"
    "v_exp_f32 v91, v91\n"
    "v_mfma_f32_16x16x32_bf16 a[96:99], a[184:187], v[144:147], a[96:99]\n"
    "v_exp_f32 v92, v92\n"
    "v_cvt_pk_bf16_f32 v141, v90, v91\n"
    "v_mfma_f32_16x16x32_bf16 a[100:103], a[184:187], v[148:151], a[100:103]\n"
    "v_exp_f32 v93, v93\n"
    "v_exp_f32 v94, v94\n"
    "v_exp_f32 v95, v95\n"
    "v_mfma_f32_16x16x32_bf16 a[104:107], a[184:187], v[152:155], a[104:107]\n"
    "ds_read_b128 a[148:151], v5 offset:4096\n"
    "v_mfma_f32_16x16x32_bf16 a[108:111], a[184:187], v[156:159], a[108:111]\n"
    "s_waitcnt lgkmcnt(1)\n"
    "v_cvt_pk_bf16_f32 v142, v92, v93\n"
    "v_cvt_pk_bf16_f32 v143, v94, v95\n"
    "v_mfma_f32_16x16x32_bf16 v[100:103], a[156:159], v[204:207], v[100:103]\n"
    "ds_read_b64_tr_b16 a[180:181], v12 offset:50432\n"
    "v_cvt_pk_bf16_f32 v97, 0, v97\n"
    "v_mfma_f32_16x16x32_bf16 v[188:191], a[224:227], v[140:143], v[52:55]\n"
    "ds_read_b64_tr_b16 a[182:183], v12 offset:54528\n"
    "v_cvt_pk_bf16_f32 v98, 0, v98\n"
    "v_cvt_pk_bf16_f32 v99, 0, v99\n"
    "v_mfma_f32_16x16x32_bf16 v[108:111], a[156:159], v[220:223], v[108:111]\n"
    "ds_read_b128 a[152:155], v6 offset:4096\n"
    "v_cvt_pk_bf16_f32 v104, 0, v104\n"
    "v_mfma_f32_16x16x32_bf16 v[116:119], a[156:159], v[236:239], v[116:119]\n"
    "ds_read_b64_tr_b16 a[184:185], v12 offset:50688\n"
    "v_cvt_pk_bf16_f32 v105, 0, v105\n"
    "v_cvt_pk_bf16_f32 v106, 0, v106\n"
    "v_mfma_f32_16x16x32_bf16 v[124:127], a[156:159], v[252:255], v[124:127]\n"
    "ds_read_b64_tr_b16 a[186:187], v12 offset:54784\n"
    "v_cvt_pk_bf16_f32 v107, 0, v107\n"
    "v_mfma_f32_16x16x32_bf16 a[112:115], a[188:191], v[144:147], a[112:115]\n"
    "v_max3_f32 v19, v176, v180, v184\n"
    "v_cvt_pk_bf16_f32 v112, 0, v112\n"
    "v_cvt_pk_bf16_f32 v113, 0, v113\n"
    "v_mfma_f32_16x16x32_bf16 a[116:119], a[188:191], v[148:151], a[116:119]\n"
    "v_max_f32 v19, v19, v188\n"
    "v_cvt_pk_bf16_f32 v114, 0, v114\n"
    "v_mfma_f32_16x16x32_bf16 a[120:123], a[188:191], v[152:155], a[120:123]\n"
    "v_cmp_lt_f32 vcc, s25, v19\n"
    "v_cvt_pk_bf16_f32 v115, 0, v115\n"
    "v_mfma_f32_16x16x32_bf16 a[124:127], a[188:191], v[156:159], a[124:127]\n"
    "ds_read_b128 a[156:159], v7 offset:4096\n"
    "ds_read_b64_tr_b16 a[188:189], v12 offset:50944\n"
    "ds_read_b64_tr_b16 a[190:191], v12 offset:55040\n"
    "v_cvt_pk_bf16_f32 v120, 0, v120\n"
    "v_cvt_pk_bf16_f32 v121, 0, v121\n"
    "v_cvt_pk_bf16_f32 v122, 0, v122\n"
    "v_cvt_pk_bf16_f32 v123, 0, v123\n"
    "s_cbranch_vccnz RARE_b6_%=\n"
    "RARE_b6_%=_ret:\n"
    "v_mfma_f32_16x16x32_bf16 v[64:67], a[128:131], v[192:195], 0\n"
    "v_cvt_pk_bf16_f32 v100, 0, v100\n"
    "v_cvt_pk_bf16_f32 v101, 0, v101\n"
    "v_mfma_f32_16x16x32_bf16 v[72:75], a[128:131], v[208:211], 0\n"
    "v_cvt_pk_bf16_f32 v102, 0, v102\n"
    "v_cvt_pk_bf16_f32 v103, 0, v103\n"
    "v_mfma_f32_16x16x32_bf16 v[80:83], a[128:131], v[224:227], 0\n"
    "v_fma_f32 v96, v96, s57, v160\n"
    "v_fma_f32 v97, v97, s57, v160\n"
    "v_mfma_f32_16x16x32_bf16 v[88:91], a[128:131], v[240:243], 0\n"
    "v_fma_f32 v98, v98, s57, v160\n"
    "v_exp_f32 v96, v96\n"
    "v_mfma_f32_16x16x32_bf16 a[0:3], a[160:163], v[128:131], a[0:3]\n"
    "v_exp_f32 v97, v97\n"
    "v_mfma_f32_16x16x32_bf16 a[4:7], a[160:163], v[132:135], a[4:7]\n"
    "v_fma_f32 v99, v99, s57, v160\n"
    "v_exp_f32 v98, v98\n"
    "v_mfma_f32_16x16x32_bf16 a[8:11], a[160:163], v[136:139], a[8:11]\n"
    "v_fma_f32 v100, v100, s57, v160\n"
    "v_exp_f32 v99, v99\n"
    "v_mfma_f32_16x16x32_bf16 a[12:15], a[160:163], v[140:143], a[12:15]\n"
    "v_exp_f32 v100, v100\n"
    "v_mfma_f32_16x16x32_bf16 v[64:67], a[132:135], v[196:199], v[64:67]\n"
    "v_fma_f32 v101, v101, s57, v160\n"
    "v_exp_f32 v101, v101\n"
    "v_mfma_f32_16x16x32_bf16 v[72:75], a[132:135], v[212:215], v[72:75]\n"
    "ds_read_b128 a[128:131], v4 offset:8192\n"
    "v_cvt_pk_bf16_f32 v144, v96, v97\n"
    "v_mfma_f32_16x16x32_bf16 v[80:83], a[132:135], v[228:231], v[80:83]\n"
    "v_fma_f32 v102, v102, s57, v160\n"
    "v_exp_f32 v102, v102\n"
    "v_mfma_f32_16x16x32_bf16 v[88:91], a[132:135], v[244:247], v[88:91]\n"
    "v_fma_f32 v103, v103, s57, v160\n"
    "v_cvt_pk_bf16_f32 v145, v98, v99\n"
    "v_mfma_f32_16x16x32_bf16 a[16:19], a[164:167], v[128:131], a[16:19]\n"
    "v_exp_f32 v103, v103\n"
    "v_cvt_pk_bf16_f32 v146, v100, v101\n"
    "v_mfma_f32_16x16x32_bf16 a[20:23], a[164:167], v[132:135], a[20:23]\n"
    "v_cvt_pk_bf16_f32 v147, v102, v103\n"
    "v_cvt_pk_bf16_f32 v108, 0, v108\n"
    "v_mfma_f32_16x16x32_bf16 a[24:27], a[164:167], v[136:139], a[24:27]\n"
    "ds_read_b64_tr_b16 a[160:161], v12 offset:57344\n"
    "v_cvt_pk_bf16_f32 v109, 0, v109\n"
    "v_cvt_pk_bf16_f32 v110, 0, v110\n"
    "v_mfma_f32_16x16x32_bf16 a[28:31], a[164:167], v[140:143], a[28:31]\n"
    "ds_read_b64_tr_b16 a[162:163], v12 offset:61440\n"
    "v_cvt_pk_bf16_f32 v111, 0, v111\n"
    "v_mfma_f32_16x16x32_bf16 v[64:67], a[136:139], v[200:203], v[64:67]\n"
    "ds_read_b128 a[132:135], v5 offset:8192\n"
    "v_fma_f32 v104, v104, s57, v162\n"
    "v_fma_f32 v105, v105, s57, v162\n"
    "v_mfma_f32_16x16x32_bf16 v[72:75], a[136:139], v[216:219], v[72:75]\n"
    "v_fma_f32 v106, v106, s57, v162\n"
    "v_fma_f32 v107, v107, s57, v162\n"
    "v_mfma_f32_16x16x32_bf16 v[40:43], a[224:227], v[144:147], v[176:179]\n"
    "ds_read_b64_tr_b16 a[164:165], v12 offset:57600\n"
    "v_exp_f32 v104, v104\n"
    "v_mfma_f32_16x16x32_bf16 v[80:83], a[136:139], v[232:235], v[80:83]\n"
    "s_add_u32 m0, s23, 0x4000\n"
    "v_fma_f32 v108, v108, s57, v162\n"
    "v_mfma_f32_16x16x32_bf16 v[88:91], a[136:139], v[248:251], v[88:91]\n"
    "buffer_load_dwordx4 v17, s[12:15], s21 offen offset:0 lds\n"
    "v_exp_f32 v105, v105\n"
    "v_cvt_pk_bf16_f32 v116, 0, v116\n"
    "v_mfma_f32_16x16x32_bf16 a[32:35], a[168:171], v[128:131], a[32:35]\n"
    "v_fma_f32 v109, v109, s57, v162\n"
    "v_cvt_pk_bf16_f32 v148, v104, v105\n"
    "v_mfma_f32_16x16x32_bf16 a[36:39], a[168:171], v[132:135], a[36:39]\n"
    "ds_read_b64_tr_b16 a[166:167], v12 offset:61696\n"
    "v_exp_f32 v106, v106\n"
    "v_mfma_f32_16x16x32_bf16 a[40:43], a[168:171], v[136:139], a[40:43]\n"
    "v_fma_f32 v110, v110, s57, v162\n"
    "v_cvt_pk_bf16_f32 v117, 0, v117\n"
    "v_mfma_f32_16x16x32_bf16 a[44:47], a[168:171], v[140:143], a[44:47]\n"
    "v_exp_f32 v107, v107\n"
    "v_cvt_pk_bf16_f32 v118, 0, v118\n"
    "v_mfma_f32_16x16x32_bf16 v[64:67], a[140:143], v[204:207], v[64:67]\n"
    "v_fma_f32 v111, v111, s57, v162\n"
    "v_cvt_pk_bf16_f32 v149, v106, v107\n"
    "v_mfma_f32_16x16x32_bf16 v[72:75], a[140:143], v[220:223], v[72:75]\n"
    "s_waitcnt lgkmcnt(1)\n"
    "v_exp_f32 v108, v108\n"
    "v_cvt_pk_bf16_f32 v119, 0, v119\n"
    "v_mfma_f32_16x16x32_bf16 v[80:83], a[140:143], v[236:239], v[80:83]\n"
    "v_exp_f32 v109, v109\n"
    "v_mfma_f32_16x16x32_bf16 v[88:91], a[140:143], v[252:255], v[88:91]\n"
    "v_exp_f32 v110, v110\n"
    "v_exp_f32 v111, v111\n"
    "v_mfma_f32_16x16x32_bf16 a[48:51], a[172:175], v[128:131], a[48:51]\n"
    "v_cvt_pk_bf16_f32 v150, v108, v109\n"
    "v_mfma_f32_16x16x32_bf16 a[52:55], a[172:175], v[132:135], a[52:55]\n"
    "buffer_load_dwordx4 v18, s[12:15], s21 offen offset:1024 lds\n"
    "v_cvt_pk_bf16_f32 v151, v110, v111\n"
    "v_fma_f32 v112, v112, s57, v164\n"
    "v_mfma_f32_16x16x32_bf16 a[56:59], a[172:175], v[136:139], a[56:59]\n"
    "ds_read_b128 a[136:139], v6 offset:8192\n"
    "v_fma_f32 v113, v113, s57, v164\n"
    "v_mfma_f32_16x16x32_bf16 v[44:47], a[224:227], v[148:151], v[180:183]\n"
    "ds_read_b64_tr_b16 a[168:169], v12 offset:57856\n"
    "v_fma_f32 v114, v114, s57, v164\n"
    "v_fma_f32 v115, v115, s57, v164\n"
    "v_mfma_f32_16x16x32_bf16 a[60:63], a[172:175], v[140:143], a[60:63]\n"
    "ds_read_b64_tr_b16 a[170:171], v12 offset:61952\n"
    "v_fma_f32 v116, v116, s57, v164\n"
    "v_mfma_f32_16x16x32_bf16 v[68:71], a[144:147], v[192:195], 0\n"
    "ds_read_b128 a[140:143], v7 offset:8192\n"
    "v_exp_f32 v112, v112\n"
    "v_mfma_f32_16x16x32_bf16 v[76:79], a[144:147], v[208:211], 0\n"
    "v_fma_f32 v117, v117, s57, v164\n"
    "v_exp_f32 v113, v113\n"
    "v_mfma_f32_16x16x32_bf16 v[84:87], a[144:147], v[224:227], 0\n"
    "buffer_load_dwordx4 v17, s[12:15], s21 offen offset:2048 lds\n"
    "v_cvt_pk_bf16_f32 v152, v112, v113\n"
    "v_mfma_f32_16x16x32_bf16 v[92:95], a[144:147], v[240:243], 0\n"
    "v_fma_f32 v118, v118, s57, v164\n"
    "v_exp_f32 v114, v114\n"
    "v_mfma_f32_16x16x32_bf16 a[64:67], a[176:179], v[128:131], a[64:67]\n"
    "v_exp_f32 v115, v115\n"
    "v_mfma_f32_16x16x32_bf16 a[68:71], a[176:179], v[132:135], a[68:71]\n"
    "v_fma_f32 v119, v119, s57, v164\n"
    "v_exp_f32 v116, v116\n"
    "v_mfma_f32_16x16x32_bf16 a[72:75], a[176:179], v[136:139], a[72:75]\n"
    "v_exp_f32 v117, v117\n"
    "v_mfma_f32_16x16x32_bf16 a[76:79], a[176:179], v[140:143], a[76:79]\n"
    "v_exp_f32 v118, v118\n"
    "v_cvt_pk_bf16_f32 v153, v114, v115\n"
    "v_mfma_f32_16x16x32_bf16 v[68:71], a[148:151], v[196:199], v[68:71]\n"
    "v_exp_f32 v119, v119\n"
    "v_mfma_f32_16x16x32_bf16 v[76:79], a[148:151], v[212:215], v[76:79]\n"
    "v_cvt_pk_bf16_f32 v154, v116, v117\n"
    "v_cvt_pk_bf16_f32 v155, v118, v119\n"
    "v_cvt_pk_bf16_f32 v124, 0, v124\n"
    "v_mfma_f32_16x16x32_bf16 v[84:87], a[148:151], v[228:231], v[84:87]\n"
    "ds_read_b64_tr_b16 a[172:173], v12 offset:58112\n"
    "v_cvt_pk_bf16_f32 v125, 0, v125\n"
    "v_mfma_f32_16x16x32_bf16 v[92:95], a[148:151], v[244:247], v[92:95]\n"
    "ds_read_b64_tr_b16 a[174:175], v12 offset:62208\n"
    "v_cvt_pk_bf16_f32 v126, 0, v126\n"
    "v_cvt_pk_bf16_f32 v127, 0, v127\n"
    "v_mfma_f32_16x16x32_bf16 v[48:51], a[224:227], v[152:155], v[184:187]\n"
    "buffer_load_dwordx4 v18, s[12:15], s21 offen offset:3072 lds\n"
    "v_fma_f32 v120, v120, s57, v166\n"
    "v_mfma_f32_16x16x32_bf16 a[80:83], a[180:183], v[128:131], a[80:83]\n"
    "ds_read_b128 a[144:147], v4 offset:12288\n"
    "v_fma_f32 v121, v121, s57, v166\n"
    "v_fma_f32 v122, v122, s57, v166\n"
    "v_mfma_f32_16x16x32_bf16 a[84:87], a[180:183], v[132:135], a[84:87]\n"
    "s_add_u32 s21, s21, 0x4000\n"
    "ds_read_b64_tr_b16 a[176:177], v12 offset:58368\n"
    "v_fma_f32 v123, v123, s57, v166\n"
    "v_mfma_f32_16x16x32_bf16 a[88:91], a[180:183], v[136:139], a[88:91]\n"
    "ds_read_b64_tr_b16 a[178:179], v12 offset:62464\n"
    "v_fma_f32 v124, v124, s57, v166\n"
    "v_fma_f32 v125, v125, s57, v166\n"
    "v_mfma_f32_16x16x32_bf16 a[92:95], a[180:183], v[140:143], a[92:95]\n"
    "v_exp_f32 v120, v120\n"
    "v_mfma_f32_16x16x32_bf16 v[68:71], a[152:155], v[200:203], v[68:71]\n"
    "v_fma_f32 v126, v126, s57, v166\n"
    "v_exp_f32 v121, v121\n"
    "v_mfma_f32_16x16x32_bf16 v[76:79], a[152:155], v[216:219], v[76:79]\n"
    "v_exp_f32 v122, v122\n"
    "v_cvt_pk_bf16_f32 v156, v120, v121\n"
    "v_mfma_f32_16x16x32_bf16 v[84:87], a[152:155], v[232:235], v[84:87]\n"
    "v_fma_f32 v127, v127, s57, v166\n"
    "v_cvt_pk_bf16_f32 v64, 0, v64\n"
    "v_mfma_f32_16x16x32_bf16 v[92:95], a[152:155], v[248:251], v[92:95]\n"
    "v_exp_f32 v123, v123\n"
    "v_mfma_f32_16x16x32_bf16 a[96:99], a[184:187], v[128:131], a[96:99]\n"
    "v_exp_f32 v124, v124\n"
    "v_cvt_pk_bf16_f32 v157, v122, v123\n"
    "v_mfma_f32_16x16x32_bf16 a[100:103], a[184:187], v[132:135], a[100:103]\n"
    "v_exp_f32 v125, v125\n"
    "v_exp_f32 v126, v126\n"
    "v_exp_f32 v127, v127\n"
    "v_mfma_f32_16x16x32_bf16 a[104:107], a[184:187], v[136:139], a[104:107]\n"
    "ds_read_b128 a[148:151], v5 offset:12288\n"
    "v_mfma_f32_16x16x32_bf16 a[108:111], a[184:187], v[140:143], a[108:111]\n"
    "s_waitcnt lgkmcnt(1)\n"
    "v_cvt_pk_bf16_f32 v158, v124, v125\n"
    "v_cvt_pk_bf16_f32 v159, v126, v127\n"
    "v_mfma_f32_16x16x32_bf16 v[68:71], a[156:159], v[204:207], v[68:71]\n"
    "ds_read_b64_tr_b16 a[180:181], v12 offset:58624\n"
    "v_cvt_pk_bf16_f32 v65, 0, v65\n"
    "v_mfma_f32_16x16x32_bf16 v[52:55], a[224:227], v[156:159], v[188:191]\n"
    "ds_read_b64_tr_b16 a[182:183], v12 offset:62720\n"
    "v_cvt_pk_bf16_f32 v66, 0, v66\n"
    "v_cvt_pk_bf16_f32 v67, 0, v67\n"
    "v_mfma_f32_16x16x32_bf16 v[76:79], a[156:159], v[220:223], v[76:79]\n"
    "ds_read_b128 a[152:155], v6 offset:12288\n"
    "v_cvt_pk_bf16_f32 v72, 0, v72\n"
    "v_mfma_f32_16x16x32_bf16 v[84:87], a[156:159], v[236:239], v[84:87]\n"
    "ds_read_b64_tr_b16 a[184:185], v12 offset:58880\n"
    "v_cvt_pk_bf16_f32 v73, 0, v73\n"
    "v_cvt_pk_bf16_f32 v74, 0, v74\n"
    "v_mfma_f32_16x16x32_bf16 v[92:95], a[156:159], v[252:255], v[92:95]\n"
    "ds_read_b64_tr_b16 a[186:187], v12 offset:62976\n"
    "v_cvt_pk_bf16_f32 v75, 0, v75\n"
    "v_mfma_f32_16x16x32_bf16 a[112:115], a[188:191], v[128:131], a[112:115]\n"
    "v_max3_f32 v19, v40, v44, v48\n"
    "v_cvt_pk_bf16_f32 v80, 0, v80\n"
    "v_cvt_pk_bf16_f32 v81, 0, v81\n"
    "v_mfma_f32_16x16x32_bf16 a[116:119], a[188:191], v[132:135], a[116:119]\n"
    "v_max_f32 v19, v19, v52\n"
    "v_cvt_pk_bf16_f32 v82, 0, v82\n"
    "v_mfma_f32_16x16x32_bf16 a[120:123], a[188:191], v[136:139], a[120:123]\n"
    "v_cmp_lt_f32 vcc, s25, v19\n"
    "v_cvt_pk_bf16_f32 v83, 0, v83\n"
    "v_mfma_f32_16x16x32_bf16 a[124:127], a[188:191], v[140:143], a[124:127]\n"
    "ds_read_b128 a[156:159], v7 offset:12288\n"
    "ds_read_b64_tr_b16 a[188:189], v12 offset:59136\n"
    "ds_read_b64_tr_b16 a[190:191], v12 offset:63232\n"
    "v_cvt_pk_bf16_f32 v88, 0, v88\n"
    "v_cvt_pk_bf16_f32 v89, 0, v89\n"
    "v_cvt_pk_bf16_f32 v90, 0, v90\n"
    "v_cvt_pk_bf16_f32 v91, 0, v91\n"
    "s_cbranch_vccnz RARE_b7_%=\n"
    "RARE_b7_%=_ret:\n"
    "v_mfma_f32_16x16x32_bf16 v[96:99], a[128:131], v[192:195], 0\n"
    "s_waitcnt vmcnt(8)\n"
    "s_barrier\n"
    "v_cvt_pk_bf16_f32 v68, 0, v68\n"
    "v_cvt_pk_bf16_f32 v69, 0, v69\n"
    "v_mfma_f32_16x16x32_bf16 v[104:107], a[128:131], v[208:211], 0\n"
    "v_cvt_pk_bf16_f32 v70, 0, v70\n"
    "v_cvt_pk_bf16_f32 v71, 0, v71\n"
    "v_mfma_f32_16x16x32_bf16 v[112:115], a[128:131], v[224:227], 0\n"
    "v_fma_f32 v64, v64, s57, v160\n"
    "v_fma_f32 v65, v65, s57, v160\n"
    "v_mfma_f32_16x16x32_bf16 v[120:123], a[128:131], v[240:243], 0\n"
    "v_fma_f32 v66, v66, s57, v160\n"
    "v_exp_f32 v64, v64\n"
    "v_mfma_f32_16x16x32_bf16 a[0:3], a[160:163], v[144:147], a[0:3]\n"
    "v_exp_f32 v65, v65\n"
    "v_mfma_f32_16x16x32_bf16 a[4:7], a[160:163], v[148:151], a[4:7]\n"
    "v_fma_f32 v67, v67, s57, v160\n"
    "v_exp_f32 v66, v66\n"
    "v_mfma_f32_16x16x32_bf16 a[8:11], a[160:163], v[152:155], a[8:11]\n"
    "v_fma_f32 v68, v68, s57, v160\n"
    "v_exp_f32 v67, v67\n"
    "v_mfma_f32_16x16x32_bf16 a[12:15], a[160:163], v[156:159], a[12:15]\n"
    "v_exp_f32 v68, v68\n"
    "v_mfma_f32_16x16x32_bf16 v[96:99], a[132:135], v[196:199], v[96:99]\n"
    "v_fma_f32 v69, v69, s57, v160\n"
    "v_exp_f32 v69, v69\n"
    "v_mfma_f32_16x16x32_bf16 v[104:107], a[132:135], v[212:215], v[104:107]\n"
    "ds_read_b128 a[128:131], v4 offset:16384\n"
    "v_cvt_pk_bf16_f32 v128, v64, v65\n"
    "v_mfma_f32_16x16x32_bf16 v[112:115], a[132:135], v[228:231], v[112:115]\n"
    "v_fma_f32 v70, v70, s57, v160\n"
    "v_exp_f32 v70, v70\n"
    "v_mfma_f32_16x16x32_bf16 v[120:123], a[132:135], v[244:247], v[120:123]\n"
    "v_fma_f32 v71, v71, s57, v160\n"
    "v_cvt_pk_bf16_f32 v129, v66, v67\n"
    "v_mfma_f32_16x16x32_bf16 a[16:19], a[164:167], v[144:147], a[16:19]\n"
    "v_exp_f32 v71, v71\n"
    "v_cvt_pk_bf16_f32 v130, v68, v69\n"
    "v_mfma_f32_16x16x32_bf16 a[20:23], a[164:167], v[148:151], a[20:23]\n"
    "v_cvt_pk_bf16_f32 v131, v70, v71\n"
    "v_cvt_pk_bf16_f32 v76, 0, v76\n"
    "v_mfma_f32_16x16x32_bf16 a[24:27], a[164:167], v[152:155], a[24:27]\n"
    "ds_read_b64_tr_b16 a[160:161], v12 offset:0\n"
    "v_cvt_pk_bf16_f32 v77, 0, v77\n"
    "v_cvt_pk_bf16_f32 v78, 0, v78\n"
    "v_mfma_f32_16x16x32_bf16 a[28:31], a[164:167], v[156:159], a[28:31]\n"
    "ds_read_b64_tr_b16 a[162:163], v12 offset:4096\n"
    "v_cvt_pk_bf16_f32 v79, 0, v79\n"
    "v_mfma_f32_16x16x32_bf16 v[96:99], a[136:139], v[200:203], v[96:99]\n"
    "ds_read_b128 a[132:135], v5 offset:16384\n"
    "v_fma_f32 v72, v72, s57, v162\n"
    "v_fma_f32 v73, v73, s57, v162\n"
    "v_mfma_f32_16x16x32_bf16 v[104:107], a[136:139], v[216:219], v[104:107]\n"
    "v_fma_f32 v74, v74, s57, v162\n"
    "v_fma_f32 v75, v75, s57, v162\n"
    "v_mfma_f32_16x16x32_bf16 v[176:179], a[224:227], v[128:131], v[40:43]\n"
    "ds_read_b64_tr_b16 a[164:165], v12 offset:256\n"
    "v_exp_f32 v72, v72\n"
    "v_mfma_f32_16x16x32_bf16 v[112:115], a[136:139], v[232:235], v[112:115]\n"
    "s_add_u32 m0, s22, 0xc000\n"
    "v_fma_f32 v76, v76, s57, v162\n"
    "v_mfma_f32_16x16x32_bf16 v[120:123], a[136:139], v[248:251], v[120:123]\n"
    "buffer_load_dwordx4 v13, s[8:11], s20 offen offset:0 lds\n"
    "v_exp_f32 v73, v73\n"
    "v_cvt_pk_bf16_f32 v84, 0, v84\n"
    "v_mfma_f32_16x16x32_bf16 a[32:35], a[168:171], v[144:147], a[32:35]\n"
    "v_fma_f32 v77, v77, s57, v162\n"
    "v_cvt_pk_bf16_f32 v132, v72, v73\n"
    "v_mfma_f32_16x16x32_bf16 a[36:39], a[168:171], v[148:151], a[36:39]\n"
    "ds_read_b64_tr_b16 a[166:167], v12 offset:4352\n"
    "v_exp_f32 v74, v74\n"
    "v_mfma_f32_16x16x32_bf16 a[40:43], a[168:171], v[152:155], a[40:43]\n"
    "v_fma_f32 v78, v78, s57, v162\n"
    "v_cvt_pk_bf16_f32 v85, 0, v85\n"
    "v_mfma_f32_16x16x32_bf16 a[44:47], a[168:171], v[156:159], a[44:47]\n"
    "v_exp_f32 v75, v75\n"
    "v_cvt_pk_bf16_f32 v86, 0, v86\n"
    "v_mfma_f32_16x16x32_bf16 v[96:99], a[140:143], v[204:207], v[96:99]\n"
    "v_fma_f32 v79, v79, s57, v162\n"
    "v_cvt_pk_bf16_f32 v133, v74, v75\n"
    "v_mfma_f32_16x16x32_bf16 v[104:107], a[140:143], v[220:223], v[104:107]\n"
    "s_waitcnt lgkmcnt(1)\n"
    "v_exp_f32 v76, v76\n"
    "v_cvt_pk_bf16_f32 v87, 0, v87\n"
    "v_mfma_f32_16x16x32_bf16 v[112:115], a[140:143], v[236:239], v[112:115]\n"
    "v_exp_f32 v77, v77\n"
    "v_mfma_f32_16x16x32_bf16 v[120:123], a[140:143], v[252:255], v[120:123]\n"
    "v_exp_f32 v78, v78\n"
    "v_exp_f32 v79, v79\n"
    "v_mfma_f32_16x16x32_bf16 a[48:51], a[172:175], v[144:147], a[48:51]\n"
    "v_cvt_pk_bf16_f32 v134, v76, v77\n"
    "v_mfma_f32_16x16x32_bf16 a[52:55], a[172:175], v[148:151], a[52:55]\n"
    "buffer_load_dwordx4 v14, s[8:11], s20 offen offset:1024 lds\n"
    "v_cvt_pk_bf16_f32 v135, v78, v79\n"
    "v_fma_f32 v80, v80, s57, v164\n"
    "v_mfma_f32_16x16x32_bf16 a[56:59], a[172:175], v[152:155], a[56:59]\n"
    "ds_read_b128 a[136:139], v6 offset:16384\n"
    "v_fma_f32 v81, v81, s57, v164\n"
    "v_mfma_f32_16x16x32_bf16 v[180:183], a[224:227], v[132:135], v[44:47]\n"
    "ds_read_b64_tr_b16 a[168:169], v12 offset:512\n"
    "v_fma_f32 v82, v82, s57, v164\n"
    "v_fma_f32 v83, v83, s57, v164\n"
    "v_mfma_f32_16x16x32_bf16 a[60:63], a[172:175], v[156:159], a[60:63]\n"
    "ds_read_b64_tr_b16 a[170:171], v12 offset:4608\n"
    "v_fma_f32 v84, v84, s57, v164\n"
    "v_mfma_f32_16x16x32_bf16 v[100:103], a[144:147], v[192:195], 0\n"
    "ds_read_b128 a[140:143], v7 offset:16384\n"
    "v_exp_f32 v80, v80\n"
    "v_mfma_f32_16x16x32_bf16 v[108:111], a[144:147], v[208:211], 0\n"
    "v_fma_f32 v85, v85, s57, v164\n"
    "v_exp_f32 v81, v81\n"
    "v_mfma_f32_16x16x32_bf16 v[116:119], a[144:147], v[224:227], 0\n"
    "buffer_load_dwordx4 v15, s[8:11], s20 offen offset:2048 lds\n"
    "v_cvt_pk_bf16_f32 v136, v80, v81\n"
    "v_mfma_f32_16x16x32_bf16 v[124:127], a[144:147], v[240:243], 0\n"
    "v_fma_f32 v86, v86, s57, v164\n"
    "v_exp_f32 v82, v82\n"
    "v_mfma_f32_16x16x32_bf16 a[64:67], a[176:179], v[144:147], a[64:67]\n"
    "v_exp_f32 v83, v83\n"
    "v_mfma_f32_16x16x32_bf16 a[68:71], a[176:179], v[148:151], a[68:71]\n"
    "v_fma_f32 v87, v87, s57, v164\n"
    "v_exp_f32 v84, v84\n"
    "v_mfma_f32_16x16x32_bf16 a[72:75], a[176:179], v[152:155], a[72:75]\n"
    "v_exp_f32 v85, v85\n"
    "v_mfma_f32_16x16x32_bf16 a[76:79], a[176:179], v[156:159], a[76:79]\n"
    "v_exp_f32 v86, v86\n"
    "v_cvt_pk_bf16_f32 v137, v82, v83\n"
    "v_mfma_f32_16x16x32_bf16 v[100:103], a[148:151], v[196:199], v[100:103]\n"
    "v_exp_f32 v87, v87\n"
    "v_mfma_f32_16x16x32_bf16 v[108:111], a[148:151], v[212:215], v[108:111]\n"
    "v_cvt_pk_bf16_f32 v138, v84, v85\n"
    "v_cvt_pk_bf16_f32 v139, v86, v87\n"
    "v_cvt_pk_bf16_f32 v92, 0, v92\n"
    "v_mfma_f32_16x16x32_bf16 v[116:119], a[148:151], v[228:231], v[116:119]\n"
    "ds_read_b64_tr_b16 a[172:173], v12 offset:768\n"
    "v_cvt_pk_bf16_f32 v93, 0, v93\n"
    "v_mfma_f32_16x16x32_bf16 v[124:127], a[148:151], v[244:247], v[124:127]\n"
    "ds_read_b64_tr_b16 a[174:175], v12 offset:4864\n"
    "v_cvt_pk_bf16_f32 v94, 0, v94\n"
    "v_cvt_pk_bf16_f32 v95, 0, v95\n"
    "v_mfma_f32_16x16x32_bf16 v[184:187], a[224:227], v[136:139], v[48:51]\n"
    "buffer_load_dwordx4 v16, s[8:11], s20 offen offset:3072 lds\n"
    "v_fma_f32 v88, v88, s57, v166\n"
    "v_mfma_f32_16x16x32_bf16 a[80:83], a[180:183], v[144:147], a[80:83]\n"
    "ds_read_b128 a[144:147], v4 offset:20480\n"
    "v_fma_f32 v89, v89, s57, v166\n"
    "v_fma_f32 v90, v90, s57, v166\n"
    "v_mfma_f32_16x16x32_bf16 a[84:87], a[180:183], v[148:151], a[84:87]\n"
    "s_add_u32 s20, s20, 0x4000\n"
    "ds_read_b64_tr_b16 a[176:177], v12 offset:1024\n"
    "v_fma_f32 v91, v91, s57, v166\n"
    "v_mfma_f32_16x16x32_bf16 a[88:91], a[180:183], v[152:155], a[88:91]\n"
    "ds_read_b64_tr_b16 a[178:179], v12 offset:5120\n"
    "v_fma_f32 v92, v92, s57, v166\n"
    "v_fma_f32 v93, v93, s57, v166\n"
    "v_mfma_f32_16x16x32_bf16 a[92:95], a[180:183], v[156:159], a[92:95]\n"
    "v_exp_f32 v88, v88\n"
    "v_mfma_f32_16x16x32_bf16 v[100:103], a[152:155], v[200:203], v[100:103]\n"
    "v_fma_f32 v94, v94, s57, v166\n"
    "v_exp_f32 v89, v89\n"
    "v_mfma_f32_16x16x32_bf16 v[108:111], a[152:155], v[216:219], v[108:111]\n"
    "v_exp_f32 v90, v90\n"
    "v_cvt_pk_bf16_f32 v140, v88, v89\n"
    "v_mfma_f32_16x16x32_bf16 v[116:119], a[152:155], v[232:235], v[116:119]\n"
    "v_fma_f32 v95, v95, s57, v166\n"
    "v_cvt_pk_bf16_f32 v96, 0, v96\n"
    "v_mfma_f32_16x16x32_bf16 v[124:127], a[152:155], v[248:251], v[124:127]\n"
    "v_exp_f32 v91, v91\n"
    "v_mfma_f32_16x16x32_bf16 a[96:99], a[184:187], v[144:147], a[96:99]\n"
    "v_exp_f32 v92, v92\n"
    "v_cvt_pk_bf16_f32 v141, v90, v91\n"
    "v_mfma_f32_16x16x32_bf16 a[100:103], a[184:187], v[148:151], a[100:103]\n"
    "v_exp_f32 v93, v93\n"
    "v_exp_f32 v94, v94\n"
    "v_exp_f32 v95, v95\n"
    "v_mfma_f32_16x16x32_bf16 a[104:107], a[184:187], v[152:155], a[104:107]\n"
    "ds_read_b128 a[148:151], v5 offset:20480\n"
    "v_mfma_f32_16x16x32_bf16 a[108:111], a[184:187], v[156:159], a[108:111]\n"
    "s_waitcnt lgkmcnt(1)\n"
    "v_cvt_pk_bf16_f32 v142, v92, v93\n"
    "v_cvt_pk_bf16_f32 v143, v94, v95\n"
    "v_mfma_f32_16x16x32_bf16 v[100:103], a[156:159], v[204:207], v[100:103]\n"
    "ds_read_b64_tr_b16 a[180:181], v12 offset:1280\n"
    "v_cvt_pk_bf16_f32 v97, 0, v97\n"
    "v_mfma_f32_16x16x32_bf16 v[188:191], a[224:227], v[140:143], v[52:55]\n"
    "ds_read_b64_tr_b16 a[182:183], v12 offset:5376\n"
    "v_cvt_pk_bf16_f32 v98, 0, v98\n"
    "v_cvt_pk_bf16_f32 v99, 0, v99\n"
    "v_mfma_f32_16x16x32_bf16 v[108:111], a[156:159], v[220:223], v[108:111]\n"
    "ds_read_b128 a[152:155], v6 offset:20480\n"
    "v_cvt_pk_bf16_f32 v104, 0, v104\n"
    "v_mfma_f32_16x16x32_bf16 v[116:119], a[156:159], v[236:239], v[116:119]\n"
    "ds_read_b64_tr_b16 a[184:185], v12 offset:1536\n"
    "v_cvt_pk_bf16_f32 v105, 0, v105\n"
    "v_cvt_pk_bf16_f32 v106, 0, v106\n"
    "v_mfma_f32_16x16x32_bf16 v[124:127], a[156:159], v[252:255], v[124:127]\n"
    "ds_read_b64_tr_b16 a[186:187], v12 offset:5632\n"
    "v_cvt_pk_bf16_f32 v107, 0, v107\n"
    "v_mfma_f32_16x16x32_bf16 a[112:115], a[188:191], v[144:147], a[112:115]\n"
    "v_max3_f32 v19, v176, v180, v184\n"
    "v_cvt_pk_bf16_f32 v112, 0, v112\n"
    "v_cvt_pk_bf16_f32 v113, 0, v113\n"
    "v_mfma_f32_16x16x32_bf16 a[116:119], a[188:191], v[148:151], a[116:119]\n"
    "v_max_f32 v19, v19, v188\n"
    "v_cvt_pk_bf16_f32 v114, 0, v114\n"
    "v_mfma_f32_16x16x32_bf16 a[120:123], a[188:191], v[152:155], a[120:123]\n"
    "v_cmp_lt_f32 vcc, s25, v19\n"
    "v_cvt_pk_bf16_f32 v115, 0, v115\n"
    "v_mfma_f32_16x16x32_bf16 a[124:127], a[188:191], v[156:159], a[124:127]\n"
    "ds_read_b128 a[156:159], v7 offset:20480\n"
    "ds_read_b64_tr_b16 a[188:189], v12 offset:1792\n"
    "ds_read_b64_tr_b16 a[190:191], v12 offset:5888\n"
    "v_cvt_pk_bf16_f32 v120, 0, v120\n"
    "v_cvt_pk_bf16_f32 v121, 0, v121\n"
    "v_cvt_pk_bf16_f32 v122, 0, v122\n"
    "v_cvt_pk_bf16_f32 v123, 0, v123\n"
    "s_cbranch_vccnz RARE_b8_%=\n"
    "RARE_b8_%=_ret:\n"
    "v_mfma_f32_16x16x32_bf16 v[64:67], a[128:131], v[192:195], 0\n"
    "v_cvt_pk_bf16_f32 v100, 0, v100\n"
    "v_cvt_pk_bf16_f32 v101, 0, v101\n"
    "v_mfma_f32_16x16x32_bf16 v[72:75], a[128:131], v[208:211], 0\n"
    "v_cvt_pk_bf16_f32 v102, 0, v102\n"
    "v_cvt_pk_bf16_f32 v103, 0, v103\n"
    "v_mfma_f32_16x16x32_bf16 v[80:83], a[128:131], v[224:227], 0\n"
    "v_fma_f32 v96, v96, s57, v160\n"
    "v_fma_f32 v97, v97, s57, v160\n"
    "v_mfma_f32_16x16x32_bf16 v[88:91], a[128:131], v[240:243], 0\n"
    "v_fma_f32 v98, v98, s57, v160\n"
    "v_exp_f32 v96, v96\n"
    "v_mfma_f32_16x16x32_bf16 a[0:3], a[160:163], v[128:131], a[0:3]\n"
    "v_exp_f32 v97, v97\n"
    "v_mfma_f32_16x16x32_bf16 a[4:7], a[160:163], v[132:135], a[4:7]\n"
    "v_fma_f32 v99, v99, s57, v160\n"
    "v_exp_f32 v98, v98\n"
    "v_mfma_f32_16x16x32_bf16 a[8:11], a[160:163], v[136:139], a[8:11]\n"
    "v_fma_f32 v100, v100, s57, v160\n"
    "v_exp_f32 v99, v99\n"
    "v_mfma_f32_16x16x32_bf16 a[12:15], a[160:163], v[140:143], a[12:15]\n"
    "v_exp_f32 v100, v100\n"
    "v_mfma_f32_16x16x32_bf16 v[64:67], a[132:135], v[196:199], v[64:67]\n"
    "v_fma_f32 v101, v101, s57, v160\n"
    "v_exp_f32 v101, v101\n"
    "v_mfma_f32_16x16x32_bf16 v[72:75], a[132:135], v[212:215], v[72:75]\n"
    "ds_read_b128 a[128:131], v4 offset:24576\n"
    "v_cvt_pk_bf16_f32 v144, v96, v97\n"
    "v_mfma_f32_16x16x32_bf16 v[80:83], a[132:135], v[228:231], v[80:83]\n"
    "v_fma_f32 v102, v102, s57, v160\n"
    "v_exp_f32 v102, v102\n"
    "v_mfma_f32_16x16x32_bf16 v[88:91], a[132:135], v[244:247], v[88:91]\n"
    "v_fma_f32 v103, v103, s57, v160\n"
    "v_cvt_pk_bf16_f32 v145, v98, v99\n"
    "v_mfma_f32_16x16x32_bf16 a[16:19], a[164:167], v[128:131], a[16:19]\n"
    "v_exp_f32 v103, v103\n"
    "v_cvt_pk_bf16_f32 v146, v100, v101\n"
    "v_mfma_f32_16x16x32_bf16 a[20:23], a[164:167], v[132:135], a[20:23]\n"
    "v_cvt_pk_bf16_f32 v147, v102, v103\n"
    "v_cvt_pk_bf16_f32 v108, 0, v108\n"
    "v_mfma_f32_16x16x32_bf16 a[24:27], a[164:167], v[136:139], a[24:27]\n"
    "ds_read_b64_tr_b16 a[160:161], v12 offset:8192\n"
    "v_cvt_pk_bf16_f32 v109, 0, v109\n"
    "v_cvt_pk_bf16_f32 v110, 0, v110\n"
    "v_mfma_f32_16x16x32_bf16 a[28:31], a[164:167], v[140:143], a[28:31]\n"
    "ds_read_b64_tr_b16 a[162:163], v12 offset:12288\n"
    "v_cvt_pk_bf16_f32 v111, 0, v111\n"
    "v_mfma_f32_16x16x32_bf16 v[64:67], a[136:139], v[200:203], v[64:67]\n"
    "ds_read_b128 a[132:135], v5 offset:24576\n"
    "v_fma_f32 v104, v104, s57, v162\n"
    "v_fma_f32 v105, v105, s57, v162\n"
    "v_mfma_f32_16x16x32_bf16 v[72:75], a[136:139], v[216:219], v[72:75]\n"
    "v_fma_f32 v106, v106, s57, v162\n"
    "v_fma_f32 v107, v107, s57, v162\n"
    "v_mfma_f32_16x16x32_bf16 v[40:43], a[224:227], v[144:147], v[176:179]\n"
    "ds_read_b64_tr_b16 a[164:165], v12 offset:8448\n"
    "v_exp_f32 v104, v104\n"
    "v_mfma_f32_16x16x32_bf16 v[80:83], a[136:139], v[232:235], v[80:83]\n"
    "s_add_u32 m0, s23, 0x8000\n"
    "v_fma_f32 v108, v108, s57, v162\n"
    "v_mfma_f32_16x16x32_bf16 v[88:91], a[136:139], v[248:251], v[88:91]\n"
    "buffer_load_dwordx4 v17, s[12:15], s21 offen offset:0 lds\n"
    "v_exp_f32 v105, v105\n"
    "v_cvt_pk_bf16_f32 v116, 0, v116\n"
    "v_mfma_f32_16x16x32_bf16 a[32:35], a[168:171], v[128:131], a[32:35]\n"
    "v_fma_f32 v109, v109, s57, v162\n"
    "v_cvt_pk_bf16_f32 v148, v104, v105\n"
    "v_mfma_f32_16x16x32_bf16 a[36:39], a[168:171], v[132:135], a[36:39]\n"
    "ds_read_b64_tr_b16 a[166:167], v12 offset:12544\n"
    "v_exp_f32 v106, v106\n"
    "v_mfma_f32_16x16x32_bf16 a[40:43], a[168:171], v[136:139], a[40:43]\n"
    "v_fma_f32 v110, v110, s57, v162\n"
    "v_cvt_pk_bf16_f32 v117, 0, v117\n"
    "v_mfma_f32_16x16x32_bf16 a[44:47], a[168:171], v[140:143], a[44:47]\n"
    "v_exp_f32 v107, v107\n"
    "v_cvt_pk_bf16_f32 v118, 0, v118\n"
    "v_mfma_f32_16x16x32_bf16 v[64:67], a[140:143], v[204:207], v[64:67]\n"
    "v_fma_f32 v111, v111, s57, v162\n"
    "v_cvt_pk_bf16_f32 v149, v106, v107\n"
    "v_mfma_f32_16x16x32_bf16 v[72:75], a[140:143], v[220:223], v[72:75]\n"
    "s_waitcnt lgkmcnt(1)\n"
    "v_exp_f32 v108, v108\n"
    "v_cvt_pk_bf16_f32 v119, 0, v119\n"
    "v_mfma_f32_16x16x32_bf16 v[80:83], a[140:143], v[236:239], v[80:83]\n"
    "v_exp_f32 v109, v109\n"
    "v_mfma_f32_16x16x32_bf16 v[88:91], a[140:143], v[252:255], v[88:91]\n"
    "v_exp_f32 v110, v110\n"
    "v_exp_f32 v111, v111\n"
    "v_mfma_f32_16x16x32_bf16 a[48:51], a[172:175], v[128:131], a[48:51]\n"
    "v_cvt_pk_bf16_f32 v150, v108, v109\n"
    "v_mfma_f32_16x16x32_bf16 a[52:55], a[172:175], v[132:135], a[52:55]\n"
    "buffer_load_dwordx4 v18, s[12:15], s21 offen offset:1024 lds\n"
    "v_cvt_pk_bf16_f32 v151, v110, v111\n"
    "v_fma_f32 v112, v112, s57, v164\n"
    "v_mfma_f32_16x16x32_bf16 a[56:59], a[172:175], v[136:139], a[56:59]\n"
    "ds_read_b128 a[136:139], v6 offset:24576\n"
    "v_fma_f32 v113, v113, s57, v164\n"
    "v_mfma_f32_16x16x32_bf16 v[44:47], a[224:227], v[148:151], v[180:183]\n"
    "ds_read_b64_tr_b16 a[168:169], v12 offset:8704\n"
    "v_fma_f32 v114, v114, s57, v164\n"
    "v_fma_f32 v115, v115, s57, v164\n"
    "v_mfma_f32_16x16x32_bf16 a[60:63], a[172:175], v[140:143], a[60:63]\n"
    "ds_read_b64_tr_b16 a[170:171], v12 offset:12800\n"
    "v_fma_f32 v116, v116, s57, v164\n"
    "v_mfma_f32_16x16x32_bf16 v[68:71], a[144:147], v[192:195], 0\n"
    "ds_read_b128 a[140:143], v7 offset:24576\n"
    "v_exp_f32 v112, v112\n"
    "v_mfma_f32_16x16x32_bf16 v[76:79], a[144:147], v[208:211], 0\n"
    "v_fma_f32 v117, v117, s57, v164\n"
    "v_exp_f32 v113, v113\n"
    "v_mfma_f32_16x16x32_bf16 v[84:87], a[144:147], v[224:227], 0\n"
    "buffer_load_dwordx4 v17, s[12:15], s21 offen offset:2048 lds\n"
    "v_cvt_pk_bf16_f32 v152, v112, v113\n"
    "v_mfma_f32_16x16x32_bf16 v[92:95], a[144:147], v[240:243], 0\n"
    "v_fma_f32 v118, v118, s57, v164\n"
    "v_exp_f32 v114, v114\n"
    "v_mfma_f32_16x16x32_bf16 a[64:67], a[176:179], v[128:131], a[64:67]\n"
    "v_exp_f32 v115, v115\n"
    "v_mfma_f32_16x16x32_bf16 a[68:71], a[176:179], v[132:135], a[68:71]\n"
    "v_fma_f32 v119, v119, s57, v164\n"
    "v_exp_f32 v116, v116\n"
    "v_mfma_f32_16x16x32_bf16 a[72:75], a[176:179], v[136:139], a[72:75]\n"
    "v_exp_f32 v117, v117\n"
    "v_mfma_f32_16x16x32_bf16 a[76:79], a[176:179], v[140:143], a[76:79]\n"
    "v_exp_f32 v118, v118\n"
    "v_cvt_pk_bf16_f32 v153, v114, v115\n"
    "v_mfma_f32_16x16x32_bf16 v[68:71], a[148:151], v[196:199], v[68:71]\n"
    "v_exp_f32 v119, v119\n"
    "v_mfma_f32_16x16x32_bf16 v[76:79], a[148:151], v[212:215], v[76:79]\n"
    "v_cvt_pk_bf16_f32 v154, v116, v117\n"
    "v_cvt_pk_bf16_f32 v155, v118, v119\n"
    "v_cvt_pk_bf16_f32 v124, 0, v124\n"
    "v_mfma_f32_16x16x32_bf16 v[84:87], a[148:151], v[228:231], v[84:87]\n"
    "ds_read_b64_tr_b16 a[172:173], v12 offset:8960\n"
    "v_cvt_pk_bf16_f32 v125, 0, v125\n"
    "v_mfma_f32_16x16x32_bf16 v[92:95], a[148:151], v[244:247], v[92:95]\n"
    "ds_read_b64_tr_b16 a[174:175], v12 offset:13056\n"
    "v_cvt_pk_bf16_f32 v126, 0, v126\n"
    "v_cvt_pk_bf16_f32 v127, 0, v127\n"
    "v_mfma_f32_16x16x32_bf16 v[48:51], a[224:227], v[152:155], v[184:187]\n"
    "buffer_load_dwordx4 v18, s[12:15], s21 offen offset:3072 lds\n"
    "v_fma_f32 v120, v120, s57, v166\n"
    "v_mfma_f32_16x16x32_bf16 a[80:83], a[180:183], v[128:131], a[80:83]\n"
    "ds_read_b128 a[144:147], v4 offset:28672\n"
    "v_fma_f32 v121, v121, s57, v166\n"
    "v_fma_f32 v122, v122, s57, v166\n"
    "v_mfma_f32_16x16x32_bf16 a[84:87], a[180:183], v[132:135], a[84:87]\n"
    "s_add_u32 s21, s21, 0x4000\n"
    "ds_read_b64_tr_b16 a[176:177], v12 offset:9216\n"
    "v_fma_f32 v123, v123, s57, v166\n"
    "v_mfma_f32_16x16x32_bf16 a[88:91], a[180:183], v[136:139], a[88:91]\n"
    "ds_read_b64_tr_b16 a[178:179], v12 offset:13312\n"
    "v_fma_f32 v124, v124, s57, v166\n"
    "v_fma_f32 v125, v125, s57, v166\n"
    "v_mfma_f32_16x16x32_bf16 a[92:95], a[180:183], v[140:143], a[92:95]\n"
    "v_exp_f32 v120, v120\n"
    "v_mfma_f32_16x16x32_bf16 v[68:71], a[152:155], v[200:203], v[68:71]\n"
    "v_fma_f32 v126, v126, s57, v166\n"
    "v_exp_f32 v121, v121\n"
    "v_mfma_f32_16x16x32_bf16 v[76:79], a[152:155], v[216:219], v[76:79]\n"
    "v_exp_f32 v122, v122\n"
    "v_cvt_pk_bf16_f32 v156, v120, v121\n"
    "v_mfma_f32_16x16x32_bf16 v[84:87], a[152:155], v[232:235], v[84:87]\n"
    "v_fma_f32 v127, v127, s57, v166\n"
    "v_cvt_pk_bf16_f32 v64, 0, v64\n"
    "v_mfma_f32_16x16x32_bf16 v[92:95], a[152:155], v[248:251], v[92:95]\n"
    "v_exp_f32 v123, v123\n"
    "v_mfma_f32_16x16x32_bf16 a[96:99], a[184:187], v[128:131], a[96:99]\n"
    "v_exp_f32 v124, v124\n"
    "v_cvt_pk_bf16_f32 v157, v122, v123\n"
    "v_mfma_f32_16x16x32_bf16 a[100:103], a[184:187], v[132:135], a[100:103]\n"
    "v_exp_f32 v125, v125\n"
    "v_exp_f32 v126, v126\n"
    "v_exp_f32 v127, v127\n"
    "v_mfma_f32_16x16x32_bf16 a[104:107], a[184:187], v[136:139], a[104:107]\n"
    "ds_read_b128 a[148:151], v5 offset:28672\n"
    "v_mfma_f32_16x16x32_bf16 a[108:111], a[184:187], v[140:143], a[108:111]\n"
    "s_waitcnt lgkmcnt(1)\n"
    "v_cvt_pk_bf16_f32 v158, v124, v125\n"
    "v_cvt_pk_bf16_f32 v159, v126, v127\n"
    "v_mfma_f32_16x16x32_bf16 v[68:71], a[156:159], v[204:207], v[68:71]\n"
    "ds_read_b64_tr_b16 a[180:181], v12 offset:9472\n"
    "v_cvt_pk_bf16_f32 v65, 0, v65\n"
    "v_mfma_f32_16x16x32_bf16 v[52:55], a[224:227], v[156:159], v[188:191]\n"
    "ds_read_b64_tr_b16 a[182:183], v12 offset:13568\n"
    "v_cvt_pk_bf16_f32 v66, 0, v66\n"
    "v_cvt_pk_bf16_f32 v67, 0, v67\n"
    "v_mfma_f32_16x16x32_bf16 v[76:79], a[156:159], v[220:223], v[76:79]\n"
    "ds_read_b128 a[152:155], v6 offset:28672\n"
    "v_cvt_pk_bf16_f32 v72, 0, v72\n"
    "v_mfma_f32_16x16x32_bf16 v[84:87], a[156:159], v[236:239], v[84:87]\n"
    "ds_read_b64_tr_b16 a[184:185], v12 offset:9728\n"
    "v_cvt_pk_bf16_f32 v73, 0, v73\n"
    "v_cvt_pk_bf16_f32 v74, 0, v74\n"
    "v_mfma_f32_16x16x32_bf16 v[92:95], a[156:159], v[252:255], v[92:95]\n"
    "ds_read_b64_tr_b16 a[186:187], v12 offset:13824\n"
    "v_cvt_pk_bf16_f32 v75, 0, v75\n"
    "v_mfma_f32_16x16x32_bf16 a[112:115], a[188:191], v[128:131], a[112:115]\n"
    "v_max3_f32 v19, v40, v44, v48\n"
    "v_cvt_pk_bf16_f32 v80, 0, v80\n"
    "v_cvt_pk_bf16_f32 v81, 0, v81\n"
    "v_mfma_f32_16x16x32_bf16 a[116:119], a[188:191], v[132:135], a[116:119]\n"
    "v_max_f32 v19, v19, v52\n"
    "v_cvt_pk_bf16_f32 v82, 0, v82\n"
    "v_mfma_f32_16x16x32_bf16 a[120:123], a[188:191], v[136:139], a[120:123]\n"
    "v_cmp_lt_f32 vcc, s25, v19\n"
    "v_cvt_pk_bf16_f32 v83, 0, v83\n"
    "v_mfma_f32_16x16x32_bf16 a[124:127], a[188:191], v[140:143], a[124:127]\n"
    "ds_read_b128 a[156:159], v7 offset:28672\n"
    "ds_read_b64_tr_b16 a[188:189], v12 offset:9984\n"
    "ds_read_b64_tr_b16 a[190:191], v12 offset:14080\n"
    "v_cvt_pk_bf16_f32 v88, 0, v88\n"
    "v_cvt_pk_bf16_f32 v89, 0, v89\n"
    "v_cvt_pk_bf16_f32 v90, 0, v90\n"
    "v_cvt_pk_bf16_f32 v91, 0, v91\n"
    "s_cbranch_vccnz RARE_b9_%=\n"
    "RARE_b9_%=_ret:\n"
    "s_sub_u32 s24, s24, 1\n"
    "s_cmp_lg_u32 s24, 0\n"
    "s_cbranch_scc1 LOOP_%=\n"
    "s_mov_b64 s[60:61], s[90:91]\n"
    "s_sub_u32 s60, s60, 3072\n"
    "s_subb_u32 s61, s61, 0\n"
    "s_and_b32 s61, s61, 0xffff\n"
    "s_mov_b32 s62, 0x480c00\n"
    "s_mov_b64 s[64:65], s[92:93]\n"
    "s_and_b32 s65, s65, 0xffff\n"
    "s_mov_b32 s66, 0x2400000\n"
    "v_bfe_u32 v58, v1, 1, 3\n"
    "v_mul_u32_u24 v58, 0x480, v58\n"
    "v_lshrrev_b32 v59, 4, v1\n"
    "v_lshl_add_u32 v58, v59, 5, v58\n"
    "v_and_b32 v59, 1, v1\n"
    "v_lshl_add_u32 v57, v59, 4, v58\n"
    "v_bfe_u32 v58, v1, 2, 3\n"
    "v_lshlrev_b32 v58, 5, v58\n"
    "v_and_b32 v59, 3, v1\n"
    "v_lshl_add_u32 v58, v59, 3, v58\n"
    "v_lshrrev_b32 v59, 5, v1\n"
    "v_lshl_add_u32 v58, v59, 10, v58\n"
    "v_add_u32 v8, 0x20000, v58\n"
    "v_add_u32 v9, 0x0, v58\n"
    "v_add_u32 v10, 0x10000, v58\n"
    "v_add_u32 v11, 0x8000, v58\n"
    "v_add_u32 v56, 0x18000, v58\n"
    "v_bfe_u32 v58, v1, 3, 1\n"
    "v_mul_u32_u24 v58, 0x2400, v58\n"
    "v_lshrrev_b32 v59, 4, v1\n"
    "v_lshl_add_u32 v58, v59, 7, v58\n"
    "v_and_b32 v59, 7, v1\n"
    "v_lshl_add_u32 v60, v59, 4, v58\n"
    "v_add_u32 v61, 0x4800, v60\n"
    "v_mfma_f32_16x16x32_bf16 v[96:99], a[128:131], v[192:195], 0\n"
    "s_waitcnt vmcnt(8)\n"
    "s_barrier\n"
    "v_cvt_pk_bf16_f32 v68, 0, v68\n"
    "v_cvt_pk_bf16_f32 v69, 0, v69\n"
    "v_mfma_f32_16x16x32_bf16 v[104:107], a[128:131], v[208:211], 0\n"
    "v_cvt_pk_bf16_f32 v70, 0, v70\n"
    "v_cvt_pk_bf16_f32 v71, 0, v71\n"
    "v_mfma_f32_16x16x32_bf16 v[112:115], a[128:131], v[224:227], 0\n"
    "v_fma_f32 v64, v64, s57, v160\n"
    "v_fma_f32 v65, v65, s57, v160\n"
    "v_mfma_f32_16x16x32_bf16 v[120:123], a[128:131], v[240:243], 0\n"
    "v_fma_f32 v66, v66, s57, v160\n"
    "v_exp_f32 v64, v64\n"
    "v_mfma_f32_16x16x32_bf16 a[0:3], a[160:163], v[144:147], a[0:3]\n"
    "v_exp_f32 v65, v65\n"
    "v_mfma_f32_16x16x32_bf16 a[4:7], a[160:163], v[148:151], a[4:7]\n"
    "v_fma_f32 v67, v67, s57, v160\n"
    "v_exp_f32 v66, v66\n"
    "v_mfma_f32_16x16x32_bf16 a[8:11], a[160:163], v[152:155], a[8:11]\n"
    "v_fma_f32 v68, v68, s57, v160\n"
    "v_cvt_pk_bf16_f32 v128, v64, v65\n"
    "v_mfma_f32_16x16x32_bf16 a[12:15], a[160:163], v[156:159], a[12:15]\n"
    "v_exp_f32 v67, v67\n"
    "v_cvt_pk_bf16_f32 v76, 0, v76\n"
    "v_mfma_f32_16x16x32_bf16 v[96:99], a[132:135], v[196:199], v[96:99]\n"
    "v_fma_f32 v69, v69, s57, v160\n"
    "v_cvt_pk_bf16_f32 v129, v66, v67\n"
    "v_mfma_f32_16x16x32_bf16 v[104:107], a[132:135], v[212:215], v[104:107]\n"
    "v_exp_f32 v68, v68\n"
    "v_cvt_pk_bf16_f32 v77, 0, v77\n"
    "v_mfma_f32_16x16x32_bf16 v[112:115], a[132:135], v[228:231], v[112:115]\n"
    "v_fma_f32 v70, v70, s57, v160\n"
    "v_cvt_pk_bf16_f32 v78, 0, v78\n"
    "v_mfma_f32_16x16x32_bf16 v[120:123], a[132:135], v[244:247], v[120:123]\n"
    "v_fma_f32 v71, v71, s57, v160\n"
    "v_cvt_pk_bf16_f32 v79, 0, v79\n"
    "v_mfma_f32_16x16x32_bf16 a[16:19], a[164:167], v[144:147], a[16:19]\n"
    "v_exp_f32 v69, v69\n"
    "v_fma_f32 v72, v72, s57, v162\n"
    "v_mfma_f32_16x16x32_bf16 a[20:23], a[164:167], v[148:151], a[20:23]\n"
    "v_exp_f32 v70, v70\n"
    "v_mfma_f32_16x16x32_bf16 a[24:27], a[164:167], v[152:155], a[24:27]\n"
    "v_exp_f32 v71, v71\n"
    "v_cvt_pk_bf16_f32 v130, v68, v69\n"
    "v_mfma_f32_16x16x32_bf16 a[28:31], a[164:167], v[156:159], a[28:31]\n"
    "ds_read_b128 a[128:131], v4 offset:32768\n"
    "v_cvt_pk_bf16_f32 v131, v70, v71\n"
    "v_mfma_f32_16x16x32_bf16 v[96:99], a[136:139], v[200:203], v[96:99]\n"
    "ds_read_b64_tr_b16 a[160:161], v12 offset:16384\n"
    "v_fma_f32 v73, v73, s57, v162\n"
    "v_fma_f32 v74, v74, s57, v162\n"
    "v_mfma_f32_16x16x32_bf16 v[104:107], a[136:139], v[216:219], v[104:107]\n"
    "ds_read_b64_tr_b16 a[162:163], v12 offset:20480\n"
    "v_fma_f32 v75, v75, s57, v162\n"
    "v_mfma_f32_16x16x32_bf16 v[176:179], a[224:227], v[128:131], v[40:43]\n"
    "ds_read_b128 a[132:135], v5 offset:32768\n"
    "v_exp_f32 v72, v72\n"
    "v_mfma_f32_16x16x32_bf16 v[112:115], a[136:139], v[232:235], v[112:115]\n"
    "v_fma_f32 v76, v76, s57, v162\n"
    "v_mfma_f32_16x16x32_bf16 v[120:123], a[136:139], v[248:251], v[120:123]\n"
    "ds_read_b64_tr_b16 a[164:165], v12 offset:16640\n"
    "v_exp_f32 v73, v73\n"
    "v_cvt_pk_bf16_f32 v84, 0, v84\n"
    "v_mfma_f32_16x16x32_bf16 a[32:35], a[168:171], v[144:147], a[32:35]\n"
    "v_fma_f32 v77, v77, s57, v162\n"
    "v_cvt_pk_bf16_f32 v132, v72, v73\n"
    "v_mfma_f32_16x16x32_bf16 a[36:39], a[168:171], v[148:151], a[36:39]\n"
    "s_add_u32 m0, s73, 0x20000\n"
    "s_add_u32 s68, s72, 0xc00\n"
    "buffer_load_dwordx4 v57, s[60:63], s68 offen offset:0 lds\n"
    "ds_read_b64_tr_b16 a[166:167], v12 offset:20736\n"
    "v_exp_f32 v74, v74\n"
    "v_mfma_f32_16x16x32_bf16 a[40:43], a[168:171], v[152:155], a[40:43]\n"
    "v_fma_f32 v78, v78, s57, v162\n"
    "v_cvt_pk_bf16_f32 v85, 0, v85\n"
    "v_mfma_f32_16x16x32_bf16 a[44:47], a[168:171], v[156:159], a[44:47]\n"
    "v_exp_f32 v75, v75\n"
    "v_mfma_f32_16x16x32_bf16 v[96:99], a[140:143], v[204:207], v[96:99]\n"
    "v_fma_f32 v79, v79, s57, v162\n"
    "v_exp_f32 v76, v76\n"
    "v_mfma_f32_16x16x32_bf16 v[104:107], a[140:143], v[220:223], v[104:107]\n"
    "s_waitcnt lgkmcnt(2)\n"
    "v_exp_f32 v77, v77\n"
    "v_mfma_f32_16x16x32_bf16 v[112:115], a[140:143], v[236:239], v[112:115]\n"
    "v_exp_f32 v78, v78\n"
    "v_cvt_pk_bf16_f32 v133, v74, v75\n"
    "v_mfma_f32_16x16x32_bf16 v[120:123], a[140:143], v[252:255], v[120:123]\n"
    "v_exp_f32 v79, v79\n"
    "v_mfma_f32_16x16x32_bf16 a[48:51], a[172:175], v[144:147], a[48:51]\n"
    "v_cvt_pk_bf16_f32 v134, v76, v77\n"
    "v_cvt_pk_bf16_f32 v135, v78, v79\n"
    "v_cvt_pk_bf16_f32 v86, 0, v86\n"
    "v_mfma_f32_16x16x32_bf16 a[52:55], a[172:175], v[148:151], a[52:55]\n"
    "s_add_u32 s68, s72, 0x2c00\n"
    "buffer_load_dwordx4 v57, s[60:63], s68 offen offset:1024 lds\n"
    "ds_read_b128 a[136:139], v6 offset:32768\n"
    "v_cvt_pk_bf16_f32 v87, 0, v87\n"
    "v_mfma_f32_16x16x32_bf16 a[56:59], a[172:175], v[152:155], a[56:59]\n"
    "ds_read_b64_tr_b16 a[168:169], v12 offset:16896\n"
    "v_fma_f32 v80, v80, s57, v164\n"
    "v_fma_f32 v81, v81, s57, v164\n"
    "v_mfma_f32_16x16x32_bf16 v[180:183], a[224:227], v[132:135], v[44:47]\n"
    "ds_read_b64_tr_b16 a[170:171], v12 offset:20992\n"
    "v_fma_f32 v82, v82, s57, v164\n"
    "v_mfma_f32_16x16x32_bf16 a[60:63], a[172:175], v[156:159], a[60:63]\n"
    "ds_read_b128 a[140:143], v7 offset:32768\n"
    "v_fma_f32 v83, v83, s57, v164\n"
    "v_fma_f32 v84, v84, s57, v164\n"
    "v_mfma_f32_16x16x32_bf16 v[100:103], a[144:147], v[192:195], 0\n"
    "v_exp_f32 v80, v80\n"
    "v_mfma_f32_16x16x32_bf16 v[108:111], a[144:147], v[208:211], 0\n"
    "v_fma_f32 v85, v85, s57, v164\n"
    "v_exp_f32 v81, v81\n"
    "v_mfma_f32_16x16x32_bf16 v[116:119], a[144:147], v[224:227], 0\n"
    "v_exp_f32 v82, v82\n"
    "v_mfma_f32_16x16x32_bf16 v[124:127], a[144:147], v[240:243], 0\n"
    "v_fma_f32 v86, v86, s57, v164\n"
    "v_exp_f32 v83, v83\n"
    "v_mfma_f32_16x16x32_bf16 a[64:67], a[176:179], v[144:147], a[64:67]\n"
    "s_add_u32 s68, s72, 0x4c00\n"
    "buffer_load_dwordx4 v57, s[60:63], s68 offen offset:2048 lds\n"
    "v_exp_f32 v84, v84\n"
    "v_mfma_f32_16x16x32_bf16 a[68:71], a[176:179], v[148:151], a[68:71]\n"
    "v_fma_f32 v87, v87, s57, v164\n"
    "v_exp_f32 v85, v85\n"
    "v_mfma_f32_16x16x32_bf16 a[72:75], a[176:179], v[152:155], a[72:75]\n"
    "v_exp_f32 v86, v86\n"
    "v_mfma_f32_16x16x32_bf16 a[76:79], a[176:179], v[156:159], a[76:79]\n"
    "v_exp_f32 v87, v87\n"
    "v_cvt_pk_bf16_f32 v136, v80, v81\n"
    "v_mfma_f32_16x16x32_bf16 v[100:103], a[148:151], v[196:199], v[100:103]\n"
    "v_cvt_pk_bf16_f32 v137, v82, v83\n"
    "v_cvt_pk_bf16_f32 v138, v84, v85\n"
    "v_mfma_f32_16x16x32_bf16 v[108:111], a[148:151], v[212:215], v[108:111]\n"
    "v_cvt_pk_bf16_f32 v139, v86, v87\n"
    "v_cvt_pk_bf16_f32 v92, 0, v92\n"
    "v_mfma_f32_16x16x32_bf16 v[116:119], a[148:151], v[228:231], v[116:119]\n"
    "ds_read_b64_tr_b16 a[172:173], v12 offset:17152\n"
    "v_cvt_pk_bf16_f32 v93, 0, v93\n"
    "v_cvt_pk_bf16_f32 v94, 0, v94\n"
    "v_mfma_f32_16x16x32_bf16 v[124:127], a[148:151], v[244:247], v[124:127]\n"
    "ds_read_b64_tr_b16 a[174:175], v12 offset:21248\n"
    "v_cvt_pk_bf16_f32 v95, 0, v95\n"
    "v_mfma_f32_16x16x32_bf16 v[184:187], a[224:227], v[136:139], v[48:51]\n"
    "s_add_u32 s68, s72, 0x6c00\n"
    "buffer_load_dwordx4 v57, s[60:63], s68 offen offset:3072 lds\n"
    "ds_read_b128 a[144:147], v4 offset:36864\n"
    "v_fma_f32 v88, v88, s57, v166\n"
    "v_fma_f32 v89, v89, s57, v166\n"
    "v_mfma_f32_16x16x32_bf16 a[80:83], a[180:183], v[144:147], a[80:83]\n"
    "ds_read_b64_tr_b16 a[176:177], v12 offset:17408\n"
    "v_fma_f32 v90, v90, s57, v166\n"
    "v_mfma_f32_16x16x32_bf16 a[84:87], a[180:183], v[148:151], a[84:87]\n"
    "ds_read_b64_tr_b16 a[178:179], v12 offset:21504\n"
    "v_fma_f32 v91, v91, s57, v166\n"
    "v_mfma_f32_16x16x32_bf16 a[88:91], a[180:183], v[152:155], a[88:91]\n"
    "v_fma_f32 v92, v92, s57, v166\n"
    "v_fma_f32 v93, v93, s57, v166\n"
    "v_cvt_pk_bf16_f32 v96, 0, v96\n"
    "v_mfma_f32_16x16x32_bf16 a[92:95], a[180:183], v[156:159], a[92:95]\n"
    "v_exp_f32 v88, v88\n"
    "v_mfma_f32_16x16x32_bf16 v[100:103], a[152:155], v[200:203], v[100:103]\n"
    "v_fma_f32 v94, v94, s57, v166\n"
    "v_exp_f32 v89, v89\n"
    "v_mfma_f32_16x16x32_bf16 v[108:111], a[152:155], v[216:219], v[108:111]\n"
    "v_exp_f32 v90, v90\n"
    "v_mfma_f32_16x16x32_bf16 v[116:119], a[152:155], v[232:235], v[116:119]\n"
    "v_fma_f32 v95, v95, s57, v166\n"
    "v_exp_f32 v91, v91\n"
    "v_mfma_f32_16x16x32_bf16 v[124:127], a[152:155], v[248:251], v[124:127]\n"
    "v_exp_f32 v92, v92\n"
    "v_mfma_f32_16x16x32_bf16 a[96:99], a[184:187], v[144:147], a[96:99]\n"
    "v_exp_f32 v93, v93\n"
    "v_cvt_pk_bf16_f32 v140, v88, v89\n"
    "v_mfma_f32_16x16x32_bf16 a[100:103], a[184:187], v[148:151], a[100:103]\n"
    "v_exp_f32 v94, v94\n"
    "v_exp_f32 v95, v95\n"
    "v_mfma_f32_16x16x32_bf16 a[104:107], a[184:187], v[152:155], a[104:107]\n"
    "ds_read_b128 a[148:151], v5 offset:36864\n"
    "v_mfma_f32_16x16x32_bf16 a[108:111], a[184:187], v[156:159], a[108:111]\n"
    "s_waitcnt lgkmcnt(1)\n"
    "v_cvt_pk_bf16_f32 v141, v90, v91\n"
    "v_cvt_pk_bf16_f32 v142, v92, v93\n"
    "v_cvt_pk_bf16_f32 v143, v94, v95\n"
    "v_mfma_f32_16x16x32_bf16 v[100:103], a[156:159], v[204:207], v[100:103]\n"
    "ds_read_b64_tr_b16 a[180:181], v12 offset:17664\n"
    "v_cvt_pk_bf16_f32 v97, 0, v97\n"
    "v_mfma_f32_16x16x32_bf16 v[188:191], a[224:227], v[140:143], v[52:55]\n"
    "ds_read_b64_tr_b16 a[182:183], v12 offset:21760\n"
    "v_cvt_pk_bf16_f32 v98, 0, v98\n"
    "v_mfma_f32_16x16x32_bf16 v[108:111], a[156:159], v[220:223], v[108:111]\n"
    "ds_read_b128 a[152:155], v6 offset:36864\n"
    "v_cvt_pk_bf16_f32 v99, 0, v99\n"
    "v_cvt_pk_bf16_f32 v104, 0, v104\n"
    "v_mfma_f32_16x16x32_bf16 v[116:119], a[156:159], v[236:239], v[116:119]\n"
    "ds_read_b64_tr_b16 a[184:185], v12 offset:17920\n"
    "v_cvt_pk_bf16_f32 v105, 0, v105\n"
    "v_mfma_f32_16x16x32_bf16 v[124:127], a[156:159], v[252:255], v[124:127]\n"
    "ds_read_b64_tr_b16 a[186:187], v12 offset:22016\n"
    "v_cvt_pk_bf16_f32 v106, 0, v106\n"
    "v_cvt_pk_bf16_f32 v107, 0, v107\n"
    "v_mfma_f32_16x16x32_bf16 a[112:115], a[188:191], v[144:147], a[112:115]\n"
    "v_max3_f32 v19, v176, v180, v184\n"
    "v_cvt_pk_bf16_f32 v112, 0, v112\n"
    "v_mfma_f32_16x16x32_bf16 a[116:119], a[188:191], v[148:151], a[116:119]\n"
    "v_max_f32 v19, v19, v188\n"
    "v_cvt_pk_bf16_f32 v113, 0, v113\n"
    "v_mfma_f32_16x16x32_bf16 a[120:123], a[188:191], v[152:155], a[120:123]\n"
    "v_cmp_lt_f32 vcc, s25, v19\n"
    "v_cvt_pk_bf16_f32 v114, 0, v114\n"
    "v_mfma_f32_16x16x32_bf16 a[124:127], a[188:191], v[156:159], a[124:127]\n"
    "ds_read_b128 a[156:159], v7 offset:36864\n"
    "ds_read_b64_tr_b16 a[188:189], v12 offset:18176\n"
    "ds_read_b64_tr_b16 a[190:191], v12 offset:22272\n"
    "v_cvt_pk_bf16_f32 v115, 0, v115\n"
    "v_cvt_pk_bf16_f32 v120, 0, v120\n"
    "v_cvt_pk_bf16_f32 v121, 0, v121\n"
    "v_cvt_pk_bf16_f32 v122, 0, v122\n"
    "v_cvt_pk_bf16_f32 v123, 0, v123\n"
    "s_cbranch_vccnz RARE_t58_%=\n"
    "RARE_t58_%=_ret:\n"
    "v_mfma_f32_16x16x32_bf16 v[64:67], a[128:131], v[192:195], 0\n"
    "v_cvt_pk_bf16_f32 v100, 0, v100\n"
    "v_cvt_pk_bf16_f32 v101, 0, v101\n"
    "v_mfma_f32_16x16x32_bf16 v[72:75], a[128:131], v[208:211], 0\n"
    "v_cvt_pk_bf16_f32 v102, 0, v102\n"
    "v_cvt_pk_bf16_f32 v103, 0, v103\n"
    "v_mfma_f32_16x16x32_bf16 v[80:83], a[128:131], v[224:227], 0\n"
    "v_fma_f32 v96, v96, s57, v160\n"
    "v_fma_f32 v97, v97, s57, v160\n"
    "v_mfma_f32_16x16x32_bf16 v[88:91], a[128:131], v[240:243], 0\n"
    "v_fma_f32 v98, v98, s57, v160\n"
    "v_exp_f32 v96, v96\n"
    "v_mfma_f32_16x16x32_bf16 a[0:3], a[160:163], v[128:131], a[0:3]\n"
    "v_exp_f32 v97, v97\n"
    "v_mfma_f32_16x16x32_bf16 a[4:7], a[160:163], v[132:135], a[4:7]\n"
    "v_fma_f32 v99, v99, s57, v160\n"
    "v_exp_f32 v98, v98\n"
    "v_mfma_f32_16x16x32_bf16 a[8:11], a[160:163], v[136:139], a[8:11]\n"
    "v_fma_f32 v100, v100, s57, v160\n"
    "v_exp_f32 v99, v99\n"
    "v_mfma_f32_16x16x32_bf16 a[12:15], a[160:163], v[140:143], a[12:15]\n"
    "v_exp_f32 v100, v100\n"
    "v_mfma_f32_16x16x32_bf16 v[64:67], a[132:135], v[196:199], v[64:67]\n"
    "v_fma_f32 v101, v101, s57, v160\n"
    "v_exp_f32 v101, v101\n"
    "v_mfma_f32_16x16x32_bf16 v[72:75], a[132:135], v[212:215], v[72:75]\n"
    "ds_read_b128 a[128:131], v4 offset:40960\n"
    "v_cvt_pk_bf16_f32 v144, v96, v97\n"
    "v_mfma_f32_16x16x32_bf16 v[80:83], a[132:135], v[228:231], v[80:83]\n"
    "v_fma_f32 v102, v102, s57, v160\n"
    "v_exp_f32 v102, v102\n"
    "v_mfma_f32_16x16x32_bf16 v[88:91], a[132:135], v[244:247], v[88:91]\n"
    "v_fma_f32 v103, v103, s57, v160\n"
    "v_cvt_pk_bf16_f32 v145, v98, v99\n"
    "v_mfma_f32_16x16x32_bf16 a[16:19], a[164:167], v[128:131], a[16:19]\n"
    "v_exp_f32 v103, v103\n"
    "v_cvt_pk_bf16_f32 v146, v100, v101\n"
    "v_mfma_f32_16x16x32_bf16 a[20:23], a[164:167], v[132:135], a[20:23]\n"
    "v_cvt_pk_bf16_f32 v147, v102, v103\n"
    "v_cvt_pk_bf16_f32 v108, 0, v108\n"
    "v_mfma_f32_16x16x32_bf16 a[24:27], a[164:167], v[136:139], a[24:27]\n"
    "ds_read_b64_tr_b16 a[160:161], v12 offset:24576\n"
    "v_cvt_pk_bf16_f32 v109, 0, v109\n"
    "v_cvt_pk_bf16_f32 v110, 0, v110\n"
    "v_mfma_f32_16x16x32_bf16 a[28:31], a[164:167], v[140:143], a[28:31]\n"
    "ds_read_b64_tr_b16 a[162:163], v12 offset:28672\n"
    "v_cvt_pk_bf16_f32 v111, 0, v111\n"
    "v_mfma_f32_16x16x32_bf16 v[64:67], a[136:139], v[200:203], v[64:67]\n"
    "ds_read_b128 a[132:135], v5 offset:40960\n"
    "v_fma_f32 v104, v104, s57, v162\n"
    "v_fma_f32 v105, v105, s57, v162\n"
    "v_mfma_f32_16x16x32_bf16 v[72:75], a[136:139], v[216:219], v[72:75]\n"
    "v_fma_f32 v106, v106, s57, v162\n"
    "v_fma_f32 v107, v107, s57, v162\n"
    "v_mfma_f32_16x16x32_bf16 v[40:43], a[224:227], v[144:147], v[176:179]\n"
    "ds_read_b64_tr_b16 a[164:165], v12 offset:24832\n"
    "v_exp_f32 v104, v104\n"
    "v_mfma_f32_16x16x32_bf16 v[80:83], a[136:139], v[232:235], v[80:83]\n"
    "s_add_u32 m0, s23, 0xc000\n"
    "v_fma_f32 v108, v108, s57, v162\n"
    "v_mfma_f32_16x16x32_bf16 v[88:91], a[136:139], v[248:251], v[88:91]\n"
    "buffer_load_dwordx4 v17, s[12:15], s21 offen offset:0 lds\n"
    "v_exp_f32 v105, v105\n"
    "v_cvt_pk_bf16_f32 v116, 0, v116\n"
    "v_mfma_f32_16x16x32_bf16 a[32:35], a[168:171], v[128:131], a[32:35]\n"
    "v_fma_f32 v109, v109, s57, v162\n"
    "v_cvt_pk_bf16_f32 v148, v104, v105\n"
    "v_mfma_f32_16x16x32_bf16 a[36:39], a[168:171], v[132:135], a[36:39]\n"
    "ds_read_b64_tr_b16 a[166:167], v12 offset:28928\n"
    "v_exp_f32 v106, v106\n"
    "v_mfma_f32_16x16x32_bf16 a[40:43], a[168:171], v[136:139], a[40:43]\n"
    "v_fma_f32 v110, v110, s57, v162\n"
    "v_cvt_pk_bf16_f32 v117, 0, v117\n"
    "v_mfma_f32_16x16x32_bf16 a[44:47], a[168:171], v[140:143], a[44:47]\n"
    "v_exp_f32 v107, v107\n"
    "v_cvt_pk_bf16_f32 v118, 0, v118\n"
    "v_mfma_f32_16x16x32_bf16 v[64:67], a[140:143], v[204:207], v[64:67]\n"
    "v_fma_f32 v111, v111, s57, v162\n"
    "v_cvt_pk_bf16_f32 v149, v106, v107\n"
    "v_mfma_f32_16x16x32_bf16 v[72:75], a[140:143], v[220:223], v[72:75]\n"
    "s_waitcnt lgkmcnt(1)\n"
    "v_exp_f32 v108, v108\n"
    "v_cvt_pk_bf16_f32 v119, 0, v119\n"
    "v_mfma_f32_16x16x32_bf16 v[80:83], a[140:143], v[236:239], v[80:83]\n"
    "v_exp_f32 v109, v109\n"
    "v_mfma_f32_16x16x32_bf16 v[88:91], a[140:143], v[252:255], v[88:91]\n"
    "v_exp_f32 v110, v110\n"
    "v_exp_f32 v111, v111\n"
    "v_mfma_f32_16x16x32_bf16 a[48:51], a[172:175], v[128:131], a[48:51]\n"
    "v_cvt_pk_bf16_f32 v150, v108, v109\n"
    "v_mfma_f32_16x16x32_bf16 a[52:55], a[172:175], v[132:135], a[52:55]\n"
    "buffer_load_dwordx4 v18, s[12:15], s21 offen offset:1024 lds\n"
    "v_cvt_pk_bf16_f32 v151, v110, v111\n"
    "v_fma_f32 v112, v112, s57, v164\n"
    "v_mfma_f32_16x16x32_bf16 a[56:59], a[172:175], v[136:139], a[56:59]\n"
    "ds_read_b128 a[136:139], v6 offset:40960\n"
    "v_fma_f32 v113, v113, s57, v164\n"
    "v_mfma_f32_16x16x32_bf16 v[44:47], a[224:227], v[148:151], v[180:183]\n"
    "ds_read_b64_tr_b16 a[168:169], v12 offset:25088\n"
    "v_fma_f32 v114, v114, s57, v164\n"
    "v_fma_f32 v115, v115, s57, v164\n"
    "v_mfma_f32_16x16x32_bf16 a[60:63], a[172:175], v[140:143], a[60:63]\n"
    "ds_read_b64_tr_b16 a[170:171], v12 offset:29184\n"
    "v_fma_f32 v116, v116, s57, v164\n"
    "v_mfma_f32_16x16x32_bf16 v[68:71], a[144:147], v[192:195], 0\n"
    "ds_read_b128 a[140:143], v7 offset:40960\n"
    "v_exp_f32 v112, v112\n"
    "v_mfma_f32_16x16x32_bf16 v[76:79], a[144:147], v[208:211], 0\n"
    "v_fma_f32 v117, v117, s57, v164\n"
    "v_exp_f32 v113, v113\n"
    "v_mfma_f32_16x16x32_bf16 v[84:87], a[144:147], v[224:227], 0\n"
    "buffer_load_dwordx4 v17, s[12:15], s21 offen offset:2048 lds\n"
    "v_cvt_pk_bf16_f32 v152, v112, v113\n"
    "v_mfma_f32_16x16x32_bf16 v[92:95], a[144:147], v[240:243], 0\n"
    "v_fma_f32 v118, v118, s57, v164\n"
    "v_exp_f32 v114, v114\n"
    "v_mfma_f32_16x16x32_bf16 a[64:67], a[176:179], v[128:131], a[64:67]\n"
    "v_exp_f32 v115, v115\n"
    "v_mfma_f32_16x16x32_bf16 a[68:71], a[176:179], v[132:135], a[68:71]\n"
    "v_fma_f32 v119, v119, s57, v164\n"
    "v_exp_f32 v116, v116\n"
    "v_mfma_f32_16x16x32_bf16 a[72:75], a[176:179], v[136:139], a[72:75]\n"
    "v_exp_f32 v117, v117\n"
    "v_mfma_f32_16x16x32_bf16 a[76:79], a[176:179], v[140:143], a[76:79]\n"
    "v_exp_f32 v118, v118\n"
    "v_cvt_pk_bf16_f32 v153, v114, v115\n"
    "v_mfma_f32_16x16x32_bf16 v[68:71], a[148:151], v[196:199], v[68:71]\n"
    "v_exp_f32 v119, v119\n"
    "v_mfma_f32_16x16x32_bf16 v[76:79], a[148:151], v[212:215], v[76:79]\n"
    "v_cvt_pk_bf16_f32 v154, v116, v117\n"
    "v_cvt_pk_bf16_f32 v155, v118, v119\n"
    "v_cvt_pk_bf16_f32 v124, 0, v124\n"
    "v_mfma_f32_16x16x32_bf16 v[84:87], a[148:151], v[228:231], v[84:87]\n"
    "ds_read_b64_tr_b16 a[172:173], v12 offset:25344\n"
    "v_cvt_pk_bf16_f32 v125, 0, v125\n"
    "v_mfma_f32_16x16x32_bf16 v[92:95], a[148:151], v[244:247], v[92:95]\n"
    "ds_read_b64_tr_b16 a[174:175], v12 offset:29440\n"
    "v_cvt_pk_bf16_f32 v126, 0, v126\n"
    "v_cvt_pk_bf16_f32 v127, 0, v127\n"
    "v_mfma_f32_16x16x32_bf16 v[48:51], a[224:227], v[152:155], v[184:187]\n"
    "buffer_load_dwordx4 v18, s[12:15], s21 offen offset:3072 lds\n"
    "v_fma_f32 v120, v120, s57, v166\n"
    "v_mfma_f32_16x16x32_bf16 a[80:83], a[180:183], v[128:131], a[80:83]\n"
    "ds_read_b128 a[144:147], v4 offset:45056\n"
    "v_fma_f32 v121, v121, s57, v166\n"
    "v_fma_f32 v122, v122, s57, v166\n"
    "v_mfma_f32_16x16x32_bf16 a[84:87], a[180:183], v[132:135], a[84:87]\n"
    "s_add_u32 s21, s21, 0x4000\n"
    "ds_read_b64_tr_b16 a[176:177], v12 offset:25600\n"
    "v_fma_f32 v123, v123, s57, v166\n"
    "v_mfma_f32_16x16x32_bf16 a[88:91], a[180:183], v[136:139], a[88:91]\n"
    "ds_read_b64_tr_b16 a[178:179], v12 offset:29696\n"
    "v_fma_f32 v124, v124, s57, v166\n"
    "v_fma_f32 v125, v125, s57, v166\n"
    "v_mfma_f32_16x16x32_bf16 a[92:95], a[180:183], v[140:143], a[92:95]\n"
    "v_exp_f32 v120, v120\n"
    "v_mfma_f32_16x16x32_bf16 v[68:71], a[152:155], v[200:203], v[68:71]\n"
    "v_fma_f32 v126, v126, s57, v166\n"
    "v_exp_f32 v121, v121\n"
    "v_mfma_f32_16x16x32_bf16 v[76:79], a[152:155], v[216:219], v[76:79]\n"
    "v_exp_f32 v122, v122\n"
    "v_cvt_pk_bf16_f32 v156, v120, v121\n"
    "v_mfma_f32_16x16x32_bf16 v[84:87], a[152:155], v[232:235], v[84:87]\n"
    "v_fma_f32 v127, v127, s57, v166\n"
    "v_cvt_pk_bf16_f32 v64, 0, v64\n"
    "v_mfma_f32_16x16x32_bf16 v[92:95], a[152:155], v[248:251], v[92:95]\n"
    "v_exp_f32 v123, v123\n"
    "v_mfma_f32_16x16x32_bf16 a[96:99], a[184:187], v[128:131], a[96:99]\n"
    "v_exp_f32 v124, v124\n"
    "v_cvt_pk_bf16_f32 v157, v122, v123\n"
    "v_mfma_f32_16x16x32_bf16 a[100:103], a[184:187], v[132:135], a[100:103]\n"
    "v_exp_f32 v125, v125\n"
    "v_exp_f32 v126, v126\n"
    "v_exp_f32 v127, v127\n"
    "v_mfma_f32_16x16x32_bf16 a[104:107], a[184:187], v[136:139], a[104:107]\n"
    "ds_read_b128 a[148:151], v5 offset:45056\n"
    "v_mfma_f32_16x16x32_bf16 a[108:111], a[184:187], v[140:143], a[108:111]\n"
    "s_waitcnt lgkmcnt(1)\n"
    "v_cvt_pk_bf16_f32 v158, v124, v125\n"
    "v_cvt_pk_bf16_f32 v159, v126, v127\n"
    "v_mfma_f32_16x16x32_bf16 v[68:71], a[156:159], v[204:207], v[68:71]\n"
    "ds_read_b64_tr_b16 a[180:181], v12 offset:25856\n"
    "v_cvt_pk_bf16_f32 v65, 0, v65\n"
    "v_mfma_f32_16x16x32_bf16 v[52:55], a[224:227], v[156:159], v[188:191]\n"
    "ds_read_b64_tr_b16 a[182:183], v12 offset:29952\n"
    "v_cvt_pk_bf16_f32 v66, 0, v66\n"
    "v_cvt_pk_bf16_f32 v67, 0, v67\n"
    "v_mfma_f32_16x16x32_bf16 v[76:79], a[156:159], v[220:223], v[76:79]\n"
    "ds_read_b128 a[152:155], v6 offset:45056\n"
    "v_cvt_pk_bf16_f32 v72, 0, v72\n"
    "v_mfma_f32_16x16x32_bf16 v[84:87], a[156:159], v[236:239], v[84:87]\n"
    "ds_read_b64_tr_b16 a[184:185], v12 offset:26112\n"
    "v_cvt_pk_bf16_f32 v73, 0, v73\n"
    "v_cvt_pk_bf16_f32 v74, 0, v74\n"
    "v_mfma_f32_16x16x32_bf16 v[92:95], a[156:159], v[252:255], v[92:95]\n"
    "ds_read_b64_tr_b16 a[186:187], v12 offset:30208\n"
    "v_cvt_pk_bf16_f32 v75, 0, v75\n"
    "v_mfma_f32_16x16x32_bf16 a[112:115], a[188:191], v[128:131], a[112:115]\n"
    "v_max3_f32 v19, v40, v44, v48\n"
    "v_cvt_pk_bf16_f32 v80, 0, v80\n"
    "v_cvt_pk_bf16_f32 v81, 0, v81\n"
    "v_mfma_f32_16x16x32_bf16 a[116:119], a[188:191], v[132:135], a[116:119]\n"
    "v_max_f32 v19, v19, v52\n"
    "v_cvt_pk_bf16_f32 v82, 0, v82\n"
    "v_mfma_f32_16x16x32_bf16 a[120:123], a[188:191], v[136:139], a[120:123]\n"
    "v_cmp_lt_f32 vcc, s25, v19\n"
    "v_cvt_pk_bf16_f32 v83, 0, v83\n"
    "v_mfma_f32_16x16x32_bf16 a[124:127], a[188:191], v[140:143], a[124:127]\n"
    "ds_read_b128 a[156:159], v7 offset:45056\n"
    "ds_read_b64_tr_b16 a[188:189], v12 offset:26368\n"
    "ds_read_b64_tr_b16 a[190:191], v12 offset:30464\n"
    "v_cvt_pk_bf16_f32 v88, 0, v88\n"
    "v_cvt_pk_bf16_f32 v89, 0, v89\n"
    "v_cvt_pk_bf16_f32 v90, 0, v90\n"
    "v_cvt_pk_bf16_f32 v91, 0, v91\n"
    "s_cbranch_vccnz RARE_t59_%=\n"
    "RARE_t59_%=_ret:\n"
    "v_mfma_f32_16x16x32_bf16 v[96:99], a[128:131], v[192:195], 0\n"
    "s_waitcnt vmcnt(8)\n"
    "s_barrier\n"
    "v_cvt_pk_bf16_f32 v68, 0, v68\n"
    "v_cvt_pk_bf16_f32 v69, 0, v69\n"
    "v_mfma_f32_16x16x32_bf16 v[104:107], a[128:131], v[208:211], 0\n"
    "v_cvt_pk_bf16_f32 v70, 0, v70\n"
    "v_cvt_pk_bf16_f32 v71, 0, v71\n"
    "v_mfma_f32_16x16x32_bf16 v[112:115], a[128:131], v[224:227], 0\n"
    "v_fma_f32 v64, v64, s57, v160\n"
    "v_fma_f32 v65, v65, s57, v160\n"
    "v_mfma_f32_16x16x32_bf16 v[120:123], a[128:131], v[240:243], 0\n"
    "v_fma_f32 v66, v66, s57, v160\n"
    "v_exp_f32 v64, v64\n"
    "v_mfma_f32_16x16x32_bf16 a[0:3], a[160:163], v[144:147], a[0:3]\n"
    "v_exp_f32 v65, v65\n"
    "v_mfma_f32_16x16x32_bf16 a[4:7], a[160:163], v[148:151], a[4:7]\n"
    "v_fma_f32 v67, v67, s57, v160\n"
    "v_exp_f32 v66, v66\n"
    "v_mfma_f32_16x16x32_bf16 a[8:11], a[160:163], v[152:155], a[8:11]\n"
    "v_fma_f32 v68, v68, s57, v160\n"
    "v_cvt_pk_bf16_f32 v128, v64, v65\n"
    "v_mfma_f32_16x16x32_bf16 a[12:15], a[160:163], v[156:159], a[12:15]\n"
    "v_exp_f32 v67, v67\n"
    "v_cvt_pk_bf16_f32 v76, 0, v76\n"
    "v_mfma_f32_16x16x32_bf16 v[96:99], a[132:135], v[196:199], v[96:99]\n"
    "v_fma_f32 v69, v69, s57, v160\n"
    "v_cvt_pk_bf16_f32 v129, v66, v67\n"
    "v_mfma_f32_16x16x32_bf16 v[104:107], a[132:135], v[212:215], v[104:107]\n"
    "v_exp_f32 v68, v68\n"
    "v_cvt_pk_bf16_f32 v77, 0, v77\n"
    "v_mfma_f32_16x16x32_bf16 v[112:115], a[132:135], v[228:231], v[112:115]\n"
    "v_fma_f32 v70, v70, s57, v160\n"
    "v_cvt_pk_bf16_f32 v78, 0, v78\n"
    "v_mfma_f32_16x16x32_bf16 v[120:123], a[132:135], v[244:247], v[120:123]\n"
    "v_fma_f32 v71, v71, s57, v160\n"
    "v_cvt_pk_bf16_f32 v79, 0, v79\n"
    "v_mfma_f32_16x16x32_bf16 a[16:19], a[164:167], v[144:147], a[16:19]\n"
    "v_exp_f32 v69, v69\n"
    "v_fma_f32 v72, v72, s57, v162\n"
    "v_mfma_f32_16x16x32_bf16 a[20:23], a[164:167], v[148:151], a[20:23]\n"
    "v_exp_f32 v70, v70\n"
    "v_mfma_f32_16x16x32_bf16 a[24:27], a[164:167], v[152:155], a[24:27]\n"
    "v_exp_f32 v71, v71\n"
    "v_cvt_pk_bf16_f32 v130, v68, v69\n"
    "v_mfma_f32_16x16x32_bf16 a[28:31], a[164:167], v[156:159], a[28:31]\n"
    "ds_read_b128 a[128:131], v4 offset:49152\n"
    "v_cvt_pk_bf16_f32 v131, v70, v71\n"
    "v_mfma_f32_16x16x32_bf16 v[96:99], a[136:139], v[200:203], v[96:99]\n"
    "ds_read_b64_tr_b16 a[160:161], v12 offset:32768\n"
    "v_fma_f32 v73, v73, s57, v162\n"
    "v_fma_f32 v74, v74, s57, v162\n"
    "v_mfma_f32_16x16x32_bf16 v[104:107], a[136:139], v[216:219], v[104:107]\n"
    "ds_read_b64_tr_b16 a[162:163], v12 offset:36864\n"
    "v_fma_f32 v75, v75, s57, v162\n"
    "v_mfma_f32_16x16x32_bf16 v[176:179], a[224:227], v[128:131], v[40:43]\n"
    "ds_read_b128 a[132:135], v5 offset:49152\n"
    "v_exp_f32 v72, v72\n"
    "v_mfma_f32_16x16x32_bf16 v[112:115], a[136:139], v[232:235], v[112:115]\n"
    "v_fma_f32 v76, v76, s57, v162\n"
    "v_mfma_f32_16x16x32_bf16 v[120:123], a[136:139], v[248:251], v[120:123]\n"
    "ds_read_b64_tr_b16 a[164:165], v12 offset:33024\n"
    "v_exp_f32 v73, v73\n"
    "v_cvt_pk_bf16_f32 v84, 0, v84\n"
    "v_mfma_f32_16x16x32_bf16 a[32:35], a[168:171], v[144:147], a[32:35]\n"
    "v_fma_f32 v77, v77, s57, v162\n"
    "v_cvt_pk_bf16_f32 v132, v72, v73\n"
    "v_mfma_f32_16x16x32_bf16 a[36:39], a[168:171], v[148:151], a[36:39]\n"
    "s_add_u32 m0, s73, 0x21000\n"
    "s_add_u32 s68, s72, 0x9c00\n"
    "buffer_load_dwordx4 v57, s[60:63], s68 offen offset:0 lds\n"
    "ds_read_b64_tr_b16 a[166:167], v12 offset:37120\n"
    "v_exp_f32 v74, v74\n"
    "v_mfma_f32_16x16x32_bf16 a[40:43], a[168:171], v[152:155], a[40:43]\n"
    "v_fma_f32 v78, v78, s57, v162\n"
    "v_cvt_pk_bf16_f32 v85, 0, v85\n"
    "v_mfma_f32_16x16x32_bf16 a[44:47], a[168:171], v[156:159], a[44:47]\n"
    "v_exp_f32 v75, v75\n"
    "v_mfma_f32_16x16x32_bf16 v[96:99], a[140:143], v[204:207], v[96:99]\n"
    "v_fma_f32 v79, v79, s57, v162\n"
    "v_exp_f32 v76, v76\n"
    "v_mfma_f32_16x16x32_bf16 v[104:107], a[140:143], v[220:223], v[104:107]\n"
    "s_waitcnt lgkmcnt(2)\n"
    "v_exp_f32 v77, v77\n"
    "v_mfma_f32_16x16x32_bf16 v[112:115], a[140:143], v[236:239], v[112:115]\n"
    "v_exp_f32 v78, v78\n"
    "v_cvt_pk_bf16_f32 v133, v74, v75\n"
    "v_mfma_f32_16x16x32_bf16 v[120:123], a[140:143], v[252:255], v[120:123]\n"
    "v_exp_f32 v79, v79\n"
    "v_mfma_f32_16x16x32_bf16 a[48:51], a[172:175], v[144:147], a[48:51]\n"
    "v_cvt_pk_bf16_f32 v134, v76, v77\n"
    "v_cvt_pk_bf16_f32 v135, v78, v79\n"
    "v_cvt_pk_bf16_f32 v86, 0, v86\n"
    "v_mfma_f32_16x16x32_bf16 a[52:55], a[172:175], v[148:151], a[52:55]\n"
    "s_add_u32 s68, s72, 0xbc00\n"
    "buffer_load_dwordx4 v57, s[60:63], s68 offen offset:1024 lds\n"
    "ds_read_b128 a[136:139], v6 offset:49152\n"
    "v_cvt_pk_bf16_f32 v87, 0, v87\n"
    "v_mfma_f32_16x16x32_bf16 a[56:59], a[172:175], v[152:155], a[56:59]\n"
    "ds_read_b64_tr_b16 a[168:169], v12 offset:33280\n"
    "v_fma_f32 v80, v80, s57, v164\n"
    "v_fma_f32 v81, v81, s57, v164\n"
    "v_mfma_f32_16x16x32_bf16 v[180:183], a[224:227], v[132:135], v[44:47]\n"
    "ds_read_b64_tr_b16 a[170:171], v12 offset:37376\n"
    "v_fma_f32 v82, v82, s57, v164\n"
    "v_mfma_f32_16x16x32_bf16 a[60:63], a[172:175], v[156:159], a[60:63]\n"
    "ds_read_b128 a[140:143], v7 offset:49152\n"
    "v_fma_f32 v83, v83, s57, v164\n"
    "v_fma_f32 v84, v84, s57, v164\n"
    "v_mfma_f32_16x16x32_bf16 v[100:103], a[144:147], v[192:195], 0\n"
    "v_exp_f32 v80, v80\n"
    "v_mfma_f32_16x16x32_bf16 v[108:111], a[144:147], v[208:211], 0\n"
    "v_fma_f32 v85, v85, s57, v164\n"
    "v_exp_f32 v81, v81\n"
    "v_mfma_f32_16x16x32_bf16 v[116:119], a[144:147], v[224:227], 0\n"
    "v_exp_f32 v82, v82\n"
    "v_mfma_f32_16x16x32_bf16 v[124:127], a[144:147], v[240:243], 0\n"
    "v_fma_f32 v86, v86, s57, v164\n"
    "v_exp_f32 v83, v83\n"
    "v_mfma_f32_16x16x32_bf16 a[64:67], a[176:179], v[144:147], a[64:67]\n"
    "s_add_u32 s68, s72, 0xdc00\n"
    "buffer_load_dwordx4 v57, s[60:63], s68 offen offset:2048 lds\n"
    "v_exp_f32 v84, v84\n"
    "v_mfma_f32_16x16x32_bf16 a[68:71], a[176:179], v[148:151], a[68:71]\n"
    "v_fma_f32 v87, v87, s57, v164\n"
    "v_exp_f32 v85, v85\n"
    "v_mfma_f32_16x16x32_bf16 a[72:75], a[176:179], v[152:155], a[72:75]\n"
    "v_exp_f32 v86, v86\n"
    "v_mfma_f32_16x16x32_bf16 a[76:79], a[176:179], v[156:159], a[76:79]\n"
    "v_exp_f32 v87, v87\n"
    "v_cvt_pk_bf16_f32 v136, v80, v81\n"
    "v_mfma_f32_16x16x32_bf16 v[100:103], a[148:151], v[196:199], v[100:103]\n"
    "v_cvt_pk_bf16_f32 v137, v82, v83\n"
    "v_cvt_pk_bf16_f32 v138, v84, v85\n"
    "v_mfma_f32_16x16x32_bf16 v[108:111], a[148:151], v[212:215], v[108:111]\n"
    "v_cvt_pk_bf16_f32 v139, v86, v87\n"
    "v_cvt_pk_bf16_f32 v92, 0, v92\n"
    "v_mfma_f32_16x16x32_bf16 v[116:119], a[148:151], v[228:231], v[116:119]\n"
    "ds_read_b64_tr_b16 a[172:173], v12 offset:33536\n"
    "v_cvt_pk_bf16_f32 v93, 0, v93\n"
    "v_cvt_pk_bf16_f32 v94, 0, v94\n"
    "v_mfma_f32_16x16x32_bf16 v[124:127], a[148:151], v[244:247], v[124:127]\n"
    "ds_read_b64_tr_b16 a[174:175], v12 offset:37632\n"
    "v_cvt_pk_bf16_f32 v95, 0, v95\n"
    "v_mfma_f32_16x16x32_bf16 v[184:187], a[224:227], v[136:139], v[48:51]\n"
    "s_add_u32 s68, s72, 0xfc00\n"
    "buffer_load_dwordx4 v57, s[60:63], s68 offen offset:3072 lds\n"
    "ds_read_b128 a[144:147], v4 offset:53248\n"
    "v_fma_f32 v88, v88, s57, v166\n"
    "v_fma_f32 v89, v89, s57, v166\n"
    "v_mfma_f32_16x16x32_bf16 a[80:83], a[180:183], v[144:147], a[80:83]\n"
    "ds_read_b64_tr_b16 a[176:177], v12 offset:33792\n"
    "v_fma_f32 v90, v90, s57, v166\n"
    "v_mfma_f32_16x16x32_bf16 a[84:87], a[180:183], v[148:151], a[84:87]\n"
    "ds_read_b64_tr_b16 a[178:179], v12 offset:37888\n"
    "v_fma_f32 v91, v91, s57, v166\n"
    "v_mfma_f32_16x16x32_bf16 a[88:91], a[180:183], v[152:155], a[88:91]\n"
    "v_fma_f32 v92, v92, s57, v166\n"
    "v_fma_f32 v93, v93, s57, v166\n"
    "v_cvt_pk_bf16_f32 v96, 0, v96\n"
    "v_mfma_f32_16x16x32_bf16 a[92:95], a[180:183], v[156:159], a[92:95]\n"
    "v_exp_f32 v88, v88\n"
    "v_mfma_f32_16x16x32_bf16 v[100:103], a[152:155], v[200:203], v[100:103]\n"
    "v_fma_f32 v94, v94, s57, v166\n"
    "v_exp_f32 v89, v89\n"
    "v_mfma_f32_16x16x32_bf16 v[108:111], a[152:155], v[216:219], v[108:111]\n"
    "v_exp_f32 v90, v90\n"
    "v_mfma_f32_16x16x32_bf16 v[116:119], a[152:155], v[232:235], v[116:119]\n"
    "v_fma_f32 v95, v95, s57, v166\n"
    "v_exp_f32 v91, v91\n"
    "v_mfma_f32_16x16x32_bf16 v[124:127], a[152:155], v[248:251], v[124:127]\n"
    "v_exp_f32 v92, v92\n"
    "v_mfma_f32_16x16x32_bf16 a[96:99], a[184:187], v[144:147], a[96:99]\n"
    "v_exp_f32 v93, v93\n"
    "v_cvt_pk_bf16_f32 v140, v88, v89\n"
    "v_mfma_f32_16x16x32_bf16 a[100:103], a[184:187], v[148:151], a[100:103]\n"
    "v_exp_f32 v94, v94\n"
    "v_exp_f32 v95, v95\n"
    "v_mfma_f32_16x16x32_bf16 a[104:107], a[184:187], v[152:155], a[104:107]\n"
    "ds_read_b128 a[148:151], v5 offset:53248\n"
    "v_mfma_f32_16x16x32_bf16 a[108:111], a[184:187], v[156:159], a[108:111]\n"
    "s_waitcnt lgkmcnt(1)\n"
    "v_cvt_pk_bf16_f32 v141, v90, v91\n"
    "v_cvt_pk_bf16_f32 v142, v92, v93\n"
    "v_cvt_pk_bf16_f32 v143, v94, v95\n"
    "v_mfma_f32_16x16x32_bf16 v[100:103], a[156:159], v[204:207], v[100:103]\n"
    "ds_read_b64_tr_b16 a[180:181], v12 offset:34048\n"
    "v_cvt_pk_bf16_f32 v97, 0, v97\n"
    "v_mfma_f32_16x16x32_bf16 v[188:191], a[224:227], v[140:143], v[52:55]\n"
    "ds_read_b64_tr_b16 a[182:183], v12 offset:38144\n"
    "v_cvt_pk_bf16_f32 v98, 0, v98\n"
    "v_mfma_f32_16x16x32_bf16 v[108:111], a[156:159], v[220:223], v[108:111]\n"
    "ds_read_b128 a[152:155], v6 offset:53248\n"
    "v_cvt_pk_bf16_f32 v99, 0, v99\n"
    "v_cvt_pk_bf16_f32 v104, 0, v104\n"
    "v_mfma_f32_16x16x32_bf16 v[116:119], a[156:159], v[236:239], v[116:119]\n"
    "ds_read_b64_tr_b16 a[184:185], v12 offset:34304\n"
    "v_cvt_pk_bf16_f32 v105, 0, v105\n"
    "v_mfma_f32_16x16x32_bf16 v[124:127], a[156:159], v[252:255], v[124:127]\n"
    "ds_read_b64_tr_b16 a[186:187], v12 offset:38400\n"
    "v_cvt_pk_bf16_f32 v106, 0, v106\n"
    "v_cvt_pk_bf16_f32 v107, 0, v107\n"
    "v_mfma_f32_16x16x32_bf16 a[112:115], a[188:191], v[144:147], a[112:115]\n"
    "v_max3_f32 v19, v176, v180, v184\n"
    "v_cvt_pk_bf16_f32 v112, 0, v112\n"
    "v_mfma_f32_16x16x32_bf16 a[116:119], a[188:191], v[148:151], a[116:119]\n"
    "v_max_f32 v19, v19, v188\n"
    "v_cvt_pk_bf16_f32 v113, 0, v113\n"
    "v_mfma_f32_16x16x32_bf16 a[120:123], a[188:191], v[152:155], a[120:123]\n"
    "v_cmp_lt_f32 vcc, s25, v19\n"
    "v_cvt_pk_bf16_f32 v114, 0, v114\n"
    "v_mfma_f32_16x16x32_bf16 a[124:127], a[188:191], v[156:159], a[124:127]\n"
    "ds_read_b128 a[156:159], v7 offset:53248\n"
    "ds_read_b64_tr_b16 a[188:189], v12 offset:34560\n"
    "ds_read_b64_tr_b16 a[190:191], v12 offset:38656\n"
    "v_cvt_pk_bf16_f32 v115, 0, v115\n"
    "v_cvt_pk_bf16_f32 v120, 0, v120\n"
    "v_cvt_pk_bf16_f32 v121, 0, v121\n"
    "v_cvt_pk_bf16_f32 v122, 0, v122\n"
    "v_cvt_pk_bf16_f32 v123, 0, v123\n"
    "s_cbranch_vccnz RARE_t60_%=\n"
    "RARE_t60_%=_ret:\n"
    "v_mfma_f32_16x16x32_bf16 v[64:67], a[128:131], v[192:195], 0\n"
    "v_cvt_pk_bf16_f32 v100, 0, v100\n"
    "v_cvt_pk_bf16_f32 v101, 0, v101\n"
    "v_mfma_f32_16x16x32_bf16 v[72:75], a[128:131], v[208:211], 0\n"
    "v_cvt_pk_bf16_f32 v102, 0, v102\n"
    "v_cvt_pk_bf16_f32 v103, 0, v103\n"
    "v_mfma_f32_16x16x32_bf16 v[80:83], a[128:131], v[224:227], 0\n"
    "v_fma_f32 v96, v96, s57, v160\n"
    "v_fma_f32 v97, v97, s57, v160\n"
    "v_mfma_f32_16x16x32_bf16 v[88:91], a[128:131], v[240:243], 0\n"
    "v_fma_f32 v98, v98, s57, v160\n"
    "v_exp_f32 v96, v96\n"
    "v_mfma_f32_16x16x32_bf16 a[0:3], a[160:163], v[128:131], a[0:3]\n"
    "v_exp_f32 v97, v97\n"
    "v_mfma_f32_16x16x32_bf16 a[4:7], a[160:163], v[132:135], a[4:7]\n"
    "v_fma_f32 v99, v99, s57, v160\n"
    "v_exp_f32 v98, v98\n"
    "v_mfma_f32_16x16x32_bf16 a[8:11], a[160:163], v[136:139], a[8:11]\n"
    "v_fma_f32 v100, v100, s57, v160\n"
    "v_cvt_pk_bf16_f32 v144, v96, v97\n"
    "v_mfma_f32_16x16x32_bf16 a[12:15], a[160:163], v[140:143], a[12:15]\n"
    "v_exp_f32 v99, v99\n"
    "v_cvt_pk_bf16_f32 v108, 0, v108\n"
    "v_mfma_f32_16x16x32_bf16 v[64:67], a[132:135], v[196:199], v[64:67]\n"
    "v_fma_f32 v101, v101, s57, v160\n"
    "v_cvt_pk_bf16_f32 v145, v98, v99\n"
    "v_mfma_f32_16x16x32_bf16 v[72:75], a[132:135], v[212:215], v[72:75]\n"
    "v_exp_f32 v100, v100\n"
    "v_cvt_pk_bf16_f32 v109, 0, v109\n"
    "v_mfma_f32_16x16x32_bf16 v[80:83], a[132:135], v[228:231], v[80:83]\n"
    "v_fma_f32 v102, v102, s57, v160\n"
    "v_cvt_pk_bf16_f32 v110, 0, v110\n"
    "v_mfma_f32_16x16x32_bf16 v[88:91], a[132:135], v[244:247], v[88:91]\n"
    "v_fma_f32 v103, v103, s57, v160\n"
    "v_cvt_pk_bf16_f32 v111, 0, v111\n"
    "v_mfma_f32_16x16x32_bf16 a[16:19], a[164:167], v[128:131], a[16:19]\n"
    "v_exp_f32 v101, v101\n"
    "v_fma_f32 v104, v104, s57, v162\n"
    "v_mfma_f32_16x16x32_bf16 a[20:23], a[164:167], v[132:135], a[20:23]\n"
    "v_exp_f32 v102, v102\n"
    "v_mfma_f32_16x16x32_bf16 a[24:27], a[164:167], v[136:139], a[24:27]\n"
    "v_exp_f32 v103, v103\n"
    "v_cvt_pk_bf16_f32 v146, v100, v101\n"
    "v_mfma_f32_16x16x32_bf16 a[28:31], a[164:167], v[140:143], a[28:31]\n"
    "ds_read_b128 a[128:131], v4 offset:57344\n"
    "v_cvt_pk_bf16_f32 v147, v102, v103\n"
    "v_mfma_f32_16x16x32_bf16 v[64:67], a[136:139], v[200:203], v[64:67]\n"
    "ds_read_b64_tr_b16 a[160:161], v12 offset:40960\n"
    "v_fma_f32 v105, v105, s57, v162\n"
    "v_fma_f32 v106, v106, s57, v162\n"
    "v_mfma_f32_16x16x32_bf16 v[72:75], a[136:139], v[216:219], v[72:75]\n"
    "ds_read_b64_tr_b16 a[162:163], v12 offset:45056\n"
    "v_fma_f32 v107, v107, s57, v162\n"
    "v_mfma_f32_16x16x32_bf16 v[40:43], a[224:227], v[144:147], v[176:179]\n"
    "ds_read_b128 a[132:135], v5 offset:57344\n"
    "v_exp_f32 v104, v104\n"
    "v_mfma_f32_16x16x32_bf16 v[80:83], a[136:139], v[232:235], v[80:83]\n"
    "v_fma_f32 v108, v108, s57, v162\n"
    "v_mfma_f32_16x16x32_bf16 v[88:91], a[136:139], v[248:251], v[88:91]\n"
    "ds_read_b64_tr_b16 a[164:165], v12 offset:41216\n"
    "v_exp_f32 v105, v105\n"
    "v_cvt_pk_bf16_f32 v116, 0, v116\n"
    "v_mfma_f32_16x16x32_bf16 a[32:35], a[168:171], v[128:131], a[32:35]\n"
    "v_fma_f32 v109, v109, s57, v162\n"
    "v_cvt_pk_bf16_f32 v148, v104, v105\n"
    "v_mfma_f32_16x16x32_bf16 a[36:39], a[168:171], v[132:135], a[36:39]\n"
    "s_add_u32 m0, s73, 0x0\n"
    "s_add_u32 s68, s72, 0xc80\n"
    "buffer_load_dwordx4 v57, s[60:63], s68 offen offset:0 lds\n"
    "ds_read_b64_tr_b16 a[166:167], v12 offset:45312\n"
    "v_exp_f32 v106, v106\n"
    "v_mfma_f32_16x16x32_bf16 a[40:43], a[168:171], v[136:139], a[40:43]\n"
    "v_fma_f32 v110, v110, s57, v162\n"
    "v_cvt_pk_bf16_f32 v117, 0, v117\n"
    "v_mfma_f32_16x16x32_bf16 a[44:47], a[168:171], v[140:143], a[44:47]\n"
    "v_exp_f32 v107, v107\n"
    "v_mfma_f32_16x16x32_bf16 v[64:67], a[140:143], v[204:207], v[64:67]\n"
    "v_fma_f32 v111, v111, s57, v162\n"
    "v_exp_f32 v108, v108\n"
    "v_mfma_f32_16x16x32_bf16 v[72:75], a[140:143], v[220:223], v[72:75]\n"
    "s_waitcnt lgkmcnt(2)\n"
    "v_exp_f32 v109, v109\n"
    "v_mfma_f32_16x16x32_bf16 v[80:83], a[140:143], v[236:239], v[80:83]\n"
    "v_exp_f32 v110, v110\n"
    "v_cvt_pk_bf16_f32 v149, v106, v107\n"
    "v_mfma_f32_16x16x32_bf16 v[88:91], a[140:143], v[252:255], v[88:91]\n"
    "v_exp_f32 v111, v111\n"
    "v_mfma_f32_16x16x32_bf16 a[48:51], a[172:175], v[128:131], a[48:51]\n"
    "v_cvt_pk_bf16_f32 v150, v108, v109\n"
    "v_cvt_pk_bf16_f32 v151, v110, v111\n"
    "v_cvt_pk_bf16_f32 v118, 0, v118\n"
    "v_mfma_f32_16x16x32_bf16 a[52:55], a[172:175], v[132:135], a[52:55]\n"
    "s_add_u32 s68, s72, 0x2c80\n"
    "buffer_load_dwordx4 v57, s[60:63], s68 offen offset:1024 lds\n"
    "ds_read_b128 a[136:139], v6 offset:57344\n"
    "v_cvt_pk_bf16_f32 v119, 0, v119\n"
    "v_mfma_f32_16x16x32_bf16 a[56:59], a[172:175], v[136:139], a[56:59]\n"
    "ds_read_b64_tr_b16 a[168:169], v12 offset:41472\n"
    "v_fma_f32 v112, v112, s57, v164\n"
    "v_fma_f32 v113, v113, s57, v164\n"
    "v_mfma_f32_16x16x32_bf16 v[44:47], a[224:227], v[148:151], v[180:183]\n"
    "ds_read_b64_tr_b16 a[170:171], v12 offset:45568\n"
    "v_fma_f32 v114, v114, s57, v164\n"
    "v_mfma_f32_16x16x32_bf16 a[60:63], a[172:175], v[140:143], a[60:63]\n"
    "ds_read_b128 a[140:143], v7 offset:57344\n"
    "v_fma_f32 v115, v115, s57, v164\n"
    "v_fma_f32 v116, v116, s57, v164\n"
    "v_mfma_f32_16x16x32_bf16 v[68:71], a[144:147], v[192:195], 0\n"
    "v_exp_f32 v112, v112\n"
    "v_mfma_f32_16x16x32_bf16 v[76:79], a[144:147], v[208:211], 0\n"
    "v_fma_f32 v117, v117, s57, v164\n"
    "v_exp_f32 v113, v113\n"
    "v_mfma_f32_16x16x32_bf16 v[84:87], a[144:147], v[224:227], 0\n"
    "v_exp_f32 v114, v114\n"
    "v_mfma_f32_16x16x32_bf16 v[92:95], a[144:147], v[240:243], 0\n"
    "v_fma_f32 v118, v118, s57, v164\n"
    "v_exp_f32 v115, v115\n"
    "v_mfma_f32_16x16x32_bf16 a[64:67], a[176:179], v[128:131], a[64:67]\n"
    "s_add_u32 s68, s72, 0x4c80\n"
    "buffer_load_dwordx4 v57, s[60:63], s68 offen offset:2048 lds\n"
    "v_exp_f32 v116, v116\n"
    "v_mfma_f32_16x16x32_bf16 a[68:71], a[176:179], v[132:135], a[68:71]\n"
    "v_fma_f32 v119, v119, s57, v164\n"
    "v_exp_f32 v117, v117\n"
    "v_mfma_f32_16x16x32_bf16 a[72:75], a[176:179], v[136:139], a[72:75]\n"
    "v_exp_f32 v118, v118\n"
    "v_mfma_f32_16x16x32_bf16 a[76:79], a[176:179], v[140:143], a[76:79]\n"
    "v_exp_f32 v119, v119\n"
    "v_cvt_pk_bf16_f32 v152, v112, v113\n"
    "v_mfma_f32_16x16x32_bf16 v[68:71], a[148:151], v[196:199], v[68:71]\n"
    "v_cvt_pk_bf16_f32 v153, v114, v115\n"
    "v_cvt_pk_bf16_f32 v154, v116, v117\n"
    "v_mfma_f32_16x16x32_bf16 v[76:79], a[148:151], v[212:215], v[76:79]\n"
    "v_cvt_pk_bf16_f32 v155, v118, v119\n"
    "v_cvt_pk_bf16_f32 v124, 0, v124\n"
    "v_mfma_f32_16x16x32_bf16 v[84:87], a[148:151], v[228:231], v[84:87]\n"
    "ds_read_b64_tr_b16 a[172:173], v12 offset:41728\n"
    "v_cvt_pk_bf16_f32 v125, 0, v125\n"
    "v_cvt_pk_bf16_f32 v126, 0, v126\n"
    "v_mfma_f32_16x16x32_bf16 v[92:95], a[148:151], v[244:247], v[92:95]\n"
    "ds_read_b64_tr_b16 a[174:175], v12 offset:45824\n"
    "v_cvt_pk_bf16_f32 v127, 0, v127\n"
    "v_mfma_f32_16x16x32_bf16 v[48:51], a[224:227], v[152:155], v[184:187]\n"
    "s_add_u32 s68, s72, 0x6c80\n"
    "buffer_load_dwordx4 v57, s[60:63], s68 offen offset:3072 lds\n"
    "ds_read_b128 a[144:147], v4 offset:61440\n"
    "v_fma_f32 v120, v120, s57, v166\n"
    "v_fma_f32 v121, v121, s57, v166\n"
    "v_mfma_f32_16x16x32_bf16 a[80:83], a[180:183], v[128:131], a[80:83]\n"
    "ds_read_b64_tr_b16 a[176:177], v12 offset:41984\n"
    "v_fma_f32 v122, v122, s57, v166\n"
    "v_mfma_f32_16x16x32_bf16 a[84:87], a[180:183], v[132:135], a[84:87]\n"
    "ds_read_b64_tr_b16 a[178:179], v12 offset:46080\n"
    "v_fma_f32 v123, v123, s57, v166\n"
    "v_mfma_f32_16x16x32_bf16 a[88:91], a[180:183], v[136:139], a[88:91]\n"
    "v_fma_f32 v124, v124, s57, v166\n"
    "v_fma_f32 v125, v125, s57, v166\n"
    "v_cvt_pk_bf16_f32 v64, 0, v64\n"
    "v_mfma_f32_16x16x32_bf16 a[92:95], a[180:183], v[140:143], a[92:95]\n"
    "v_exp_f32 v120, v120\n"
    "v_mfma_f32_16x16x32_bf16 v[68:71], a[152:155], v[200:203], v[68:71]\n"
    "v_fma_f32 v126, v126, s57, v166\n"
    "v_exp_f32 v121, v121\n"
    "v_mfma_f32_16x16x32_bf16 v[76:79], a[152:155], v[216:219], v[76:79]\n"
    "v_exp_f32 v122, v122\n"
    "v_mfma_f32_16x16x32_bf16 v[84:87], a[152:155], v[232:235], v[84:87]\n"
    "v_fma_f32 v127, v127, s57, v166\n"
    "v_exp_f32 v123, v123\n"
    "v_mfma_f32_16x16x32_bf16 v[92:95], a[152:155], v[248:251], v[92:95]\n"
    "v_exp_f32 v124, v124\n"
    "v_mfma_f32_16x16x32_bf16 a[96:99], a[184:187], v[128:131], a[96:99]\n"
    "v_exp_f32 v125, v125\n"
    "v_cvt_pk_bf16_f32 v156, v120, v121\n"
    "v_mfma_f32_16x16x32_bf16 a[100:103], a[184:187], v[132:135], a[100:103]\n"
    "v_exp_f32 v126, v126\n"
    "v_exp_f32 v127, v127\n"
    "v_mfma_f32_16x16x32_bf16 a[104:107], a[184:187], v[136:139], a[104:107]\n"
    "ds_read_b128 a[148:151], v5 offset:61440\n"
    "v_mfma_f32_16x16x32_bf16 a[108:111], a[184:187], v[140:143], a[108:111]\n"
    "s_waitcnt lgkmcnt(1)\n"
    "v_cvt_pk_bf16_f32 v157, v122, v123\n"
    "v_cvt_pk_bf16_f32 v158, v124, v125\n"
    "v_cvt_pk_bf16_f32 v159, v126, v127\n"
    "v_mfma_f32_16x16x32_bf16 v[68:71], a[156:159], v[204:207], v[68:71]\n"
    "ds_read_b64_tr_b16 a[180:181], v12 offset:42240\n"
    "v_cvt_pk_bf16_f32 v65, 0, v65\n"
    "v_mfma_f32_16x16x32_bf16 v[52:55], a[224:227], v[156:159], v[188:191]\n"
    "ds_read_b64_tr_b16 a[182:183], v12 offset:46336\n"
    "v_cvt_pk_bf16_f32 v66, 0, v66\n"
    "v_mfma_f32_16x16x32_bf16 v[76:79], a[156:159], v[220:223], v[76:79]\n"
    "ds_read_b128 a[152:155], v6 offset:61440\n"
    "v_cvt_pk_bf16_f32 v67, 0, v67\n"
    "v_cvt_pk_bf16_f32 v72, 0, v72\n"
    "v_mfma_f32_16x16x32_bf16 v[84:87], a[156:159], v[236:239], v[84:87]\n"
    "ds_read_b64_tr_b16 a[184:185], v12 offset:42496\n"
    "v_cvt_pk_bf16_f32 v73, 0, v73\n"
    "v_mfma_f32_16x16x32_bf16 v[92:95], a[156:159], v[252:255], v[92:95]\n"
    "ds_read_b64_tr_b16 a[186:187], v12 offset:46592\n"
    "v_cvt_pk_bf16_f32 v74, 0, v74\n"
    "v_cvt_pk_bf16_f32 v75, 0, v75\n"
    "v_mfma_f32_16x16x32_bf16 a[112:115], a[188:191], v[128:131], a[112:115]\n"
    "v_max3_f32 v19, v40, v44, v48\n"
    "v_cvt_pk_bf16_f32 v80, 0, v80\n"
    "v_mfma_f32_16x16x32_bf16 a[116:119], a[188:191], v[132:135], a[116:119]\n"
    "v_max_f32 v19, v19, v52\n"
    "v_cvt_pk_bf16_f32 v81, 0, v81\n"
    "v_mfma_f32_16x16x32_bf16 a[120:123], a[188:191], v[136:139], a[120:123]\n"
    "v_cmp_lt_f32 vcc, s25, v19\n"
    "v_cvt_pk_bf16_f32 v82, 0, v82\n"
    "v_mfma_f32_16x16x32_bf16 a[124:127], a[188:191], v[140:143], a[124:127]\n"
    "ds_read_b128 a[156:159], v7 offset:61440\n"
    "ds_read_b64_tr_b16 a[188:189], v12 offset:42752\n"
    "ds_read_b64_tr_b16 a[190:191], v12 offset:46848\n"
    "v_cvt_pk_bf16_f32 v83, 0, v83\n"
    "v_cvt_pk_bf16_f32 v88, 0, v88\n"
    "v_cvt_pk_bf16_f32 v89, 0, v89\n"
    "v_cvt_pk_bf16_f32 v90, 0, v90\n"
    "v_cvt_pk_bf16_f32 v91, 0, v91\n"
    "s_cbranch_vccnz RARE_t61_%=\n"
    "RARE_t61_%=_ret:\n"
    "v_mfma_f32_16x16x32_bf16 v[96:99], a[128:131], v[192:195], 0\n"
    "s_waitcnt vmcnt(8)\n"
    "s_barrier\n"
    "v_cvt_pk_bf16_f32 v68, 0, v68\n"
    "v_cvt_pk_bf16_f32 v69, 0, v69\n"
    "v_mfma_f32_16x16x32_bf16 v[104:107], a[128:131], v[208:211], 0\n"
    "v_cvt_pk_bf16_f32 v70, 0, v70\n"
    "v_cvt_pk_bf16_f32 v71, 0, v71\n"
    "v_mfma_f32_16x16x32_bf16 v[112:115], a[128:131], v[224:227], 0\n"
    "v_fma_f32 v64, v64, s57, v160\n"
    "v_fma_f32 v65, v65, s57, v160\n"
    "v_mfma_f32_16x16x32_bf16 v[120:123], a[128:131], v[240:243], 0\n"
    "v_fma_f32 v66, v66, s57, v160\n"
    "v_exp_f32 v64, v64\n"
    "v_mfma_f32_16x16x32_bf16 a[0:3], a[160:163], v[144:147], a[0:3]\n"
    "v_exp_f32 v65, v65\n"
    "v_mfma_f32_16x16x32_bf16 a[4:7], a[160:163], v[148:151], a[4:7]\n"
    "v_fma_f32 v67, v67, s57, v160\n"
    "v_cvt_pk_bf16_f32 v128, v64, v65\n"
    "v_mfma_f32_16x16x32_bf16 a[8:11], a[160:163], v[152:155], a[8:11]\n"
    "v_fma_f32 v68, v68, s57, v160\n"
    "v_exp_f32 v66, v66\n"
    "v_mfma_f32_16x16x32_bf16 a[12:15], a[160:163], v[156:159], a[12:15]\n"
    "v_exp_f32 v67, v67\n"
    "v_mfma_f32_16x16x32_bf16 v[96:99], a[132:135], v[196:199], v[96:99]\n"
    "v_fma_f32 v69, v69, s57, v160\n"
    "v_cvt_pk_bf16_f32 v129, v66, v67\n"
    "v_mfma_f32_16x16x32_bf16 v[104:107], a[132:135], v[212:215], v[104:107]\n"
    "v_exp_f32 v68, v68\n"
    "v_cvt_pk_bf16_f32 v76, 0, v76\n"
    "v_mfma_f32_16x16x32_bf16 v[112:115], a[132:135], v[228:231], v[112:115]\n"
    "v_fma_f32 v70, v70, s57, v160\n"
    "v_cvt_pk_bf16_f32 v77, 0, v77\n"
    "v_mfma_f32_16x16x32_bf16 v[120:123], a[132:135], v[244:247], v[120:123]\n"
    "v_fma_f32 v71, v71, s57, v160\n"
    "v_cvt_pk_bf16_f32 v78, 0, v78\n"
    "v_mfma_f32_16x16x32_bf16 a[16:19], a[164:167], v[144:147], a[16:19]\n"
    "v_exp_f32 v69, v69\n"
    "v_mfma_f32_16x16x32_bf16 a[20:23], a[164:167], v[148:151], a[20:23]\n"
    "v_exp_f32 v70, v70\n"
    "v_cvt_pk_bf16_f32 v130, v68, v69\n"
    "v_mfma_f32_16x16x32_bf16 a[24:27], a[164:167], v[152:155], a[24:27]\n"
    "v_exp_f32 v71, v71\n"
    "v_mfma_f32_16x16x32_bf16 a[28:31], a[164:167], v[156:159], a[28:31]\n"
    "v_cvt_pk_bf16_f32 v131, v70, v71\n"
    "v_cvt_pk_bf16_f32 v79, 0, v79\n"
    "v_mfma_f32_16x16x32_bf16 v[96:99], a[136:139], v[200:203], v[96:99]\n"
    "ds_read_b64_tr_b16 a[160:161], v12 offset:49152\n"
    "v_fma_f32 v72, v72, s57, v162\n"
    "v_fma_f32 v73, v73, s57, v162\n"
    "v_mfma_f32_16x16x32_bf16 v[104:107], a[136:139], v[216:219], v[104:107]\n"
    "ds_read_b64_tr_b16 a[162:163], v12 offset:53248\n"
    "v_fma_f32 v74, v74, s57, v162\n"
    "v_mfma_f32_16x16x32_bf16 v[176:179], a[224:227], v[128:131], v[40:43]\n"
    "v_fma_f32 v75, v75, s57, v162\n"
    "v_mfma_f32_16x16x32_bf16 v[112:115], a[136:139], v[232:235], v[112:115]\n"
    "v_fma_f32 v76, v76, s57, v162\n"
    "v_exp_f32 v72, v72\n"
    "v_mfma_f32_16x16x32_bf16 v[120:123], a[136:139], v[248:251], v[120:123]\n"
    "ds_read_b64_tr_b16 a[164:165], v12 offset:49408\n"
    "v_exp_f32 v73, v73\n"
    "v_mfma_f32_16x16x32_bf16 a[32:35], a[168:171], v[144:147], a[32:35]\n"
    "v_fma_f32 v77, v77, s57, v162\n"
    "v_exp_f32 v74, v74\n"
    "v_mfma_f32_16x16x32_bf16 a[36:39], a[168:171], v[148:151], a[36:39]\n"
    "s_add_u32 m0, s73, 0x1000\n"
    "s_add_u32 s68, s72, 0x9c80\n"
    "buffer_load_dwordx4 v57, s[60:63], s68 offen offset:0 lds\n"
    "ds_read_b64_tr_b16 a[166:167], v12 offset:53504\n"
    "v_cvt_pk_bf16_f32 v132, v72, v73\n"
    "v_mfma_f32_16x16x32_bf16 a[40:43], a[168:171], v[152:155], a[40:43]\n"
    "v_fma_f32 v78, v78, s57, v162\n"
    "v_cvt_pk_bf16_f32 v84, 0, v84\n"
    "v_mfma_f32_16x16x32_bf16 a[44:47], a[168:171], v[156:159], a[44:47]\n"
    "v_exp_f32 v75, v75\n"
    "v_cvt_pk_bf16_f32 v85, 0, v85\n"
    "v_mfma_f32_16x16x32_bf16 v[96:99], a[140:143], v[204:207], v[96:99]\n"
    "v_fma_f32 v79, v79, s57, v162\n"
    "v_cvt_pk_bf16_f32 v133, v74, v75\n"
    "v_mfma_f32_16x16x32_bf16 v[104:107], a[140:143], v[220:223], v[104:107]\n"
    "s_waitcnt lgkmcnt(2)\n"
    "v_exp_f32 v76, v76\n"
    "v_mfma_f32_16x16x32_bf16 v[112:115], a[140:143], v[236:239], v[112:115]\n"
    "v_exp_f32 v77, v77\n"
    "v_cvt_pk_bf16_f32 v86, 0, v86\n"
    "v_mfma_f32_16x16x32_bf16 v[120:123], a[140:143], v[252:255], v[120:123]\n"
    "v_exp_f32 v78, v78\n"
    "v_exp_f32 v79, v79\n"
    "v_mfma_f32_16x16x32_bf16 a[48:51], a[172:175], v[144:147], a[48:51]\n"
    "ds_read_b64_tr_b16 a[168:169], v12 offset:49664\n"
    "v_mfma_f32_16x16x32_bf16 a[52:55], a[172:175], v[148:151], a[52:55]\n"
    "s_add_u32 s68, s72, 0xbc80\n"
    "buffer_load_dwordx4 v57, s[60:63], s68 offen offset:1024 lds\n"
    "v_cvt_pk_bf16_f32 v134, v76, v77\n"
    "v_cvt_pk_bf16_f32 v135, v78, v79\n"
    "v_mfma_f32_16x16x32_bf16 a[56:59], a[172:175], v[152:155], a[56:59]\n"
    "ds_read_b64_tr_b16 a[170:171], v12 offset:53760\n"
    "v_cvt_pk_bf16_f32 v87, 0, v87\n"
    "v_mfma_f32_16x16x32_bf16 v[180:183], a[224:227], v[132:135], v[44:47]\n"
    "v_fma_f32 v80, v80, s57, v164\n"
    "v_fma_f32 v81, v81, s57, v164\n"
    "v_mfma_f32_16x16x32_bf16 a[60:63], a[172:175], v[156:159], a[60:63]\n"
    "v_fma_f32 v82, v82, s57, v164\n"
    "v_fma_f32 v83, v83, s57, v164\n"
    "v_mfma_f32_16x16x32_bf16 v[100:103], a[144:147], v[192:195], 0\n"
    "v_fma_f32 v84, v84, s57, v164\n"
    "v_exp_f32 v80, v80\n"
    "v_mfma_f32_16x16x32_bf16 v[108:111], a[144:147], v[208:211], 0\n"
    "v_fma_f32 v85, v85, s57, v164\n"
    "v_cvt_pk_bf16_f32 v92, 0, v92\n"
    "v_mfma_f32_16x16x32_bf16 v[116:119], a[144:147], v[224:227], 0\n"
    "v_exp_f32 v81, v81\n"
    "v_mfma_f32_16x16x32_bf16 v[124:127], a[144:147], v[240:243], 0\n"
    "v_fma_f32 v86, v86, s57, v164\n"
    "v_exp_f32 v82, v82\n"
    "v_mfma_f32_16x16x32_bf16 a[64:67], a[176:179], v[144:147], a[64:67]\n"
    "s_add_u32 s68, s72, 0xdc80\n"
    "buffer_load_dwordx4 v57, s[60:63], s68 offen offset:2048 lds\n"
    "v_exp_f32 v83, v83\n"
    "v_mfma_f32_16x16x32_bf16 a[68:71], a[176:179], v[148:151], a[68:71]\n"
    "v_fma_f32 v87, v87, s57, v164\n"
    "v_exp_f32 v84, v84\n"
    "v_mfma_f32_16x16x32_bf16 a[72:75], a[176:179], v[152:155], a[72:75]\n"
    "v_exp_f32 v85, v85\n"
    "v_mfma_f32_16x16x32_bf16 a[76:79], a[176:179], v[156:159], a[76:79]\n"
    "v_exp_f32 v86, v86\n"
    "v_cvt_pk_bf16_f32 v136, v80, v81\n"
    "v_mfma_f32_16x16x32_bf16 v[100:103], a[148:151], v[196:199], v[100:103]\n"
    "v_exp_f32 v87, v87\n"
    "v_mfma_f32_16x16x32_bf16 v[108:111], a[148:151], v[212:215], v[108:111]\n"
    "v_cvt_pk_bf16_f32 v137, v82, v83\n"
    "v_cvt_pk_bf16_f32 v138, v84, v85\n"
    "v_mfma_f32_16x16x32_bf16 v[116:119], a[148:151], v[228:231], v[116:119]\n"
    "v_cvt_pk_bf16_f32 v139, v86, v87\n"
    "v_cvt_pk_bf16_f32 v93, 0, v93\n"
    "v_mfma_f32_16x16x32_bf16 v[124:127], a[148:151], v[244:247], v[124:127]\n"
    "ds_read_b64_tr_b16 a[172:173], v12 offset:49920\n"
    "ds_read_b64_tr_b16 a[174:175], v12 offset:54016\n"
    "v_cvt_pk_bf16_f32 v94, 0, v94\n"
    "v_mfma_f32_16x16x32_bf16 v[184:187], a[224:227], v[136:139], v[48:51]\n"
    "s_add_u32 s68, s72, 0xfc80\n"
    "buffer_load_dwordx4 v57, s[60:63], s68 offen offset:3072 lds\n"
    "ds_read_b64_tr_b16 a[176:177], v12 offset:50176\n"
    "v_cvt_pk_bf16_f32 v95, 0, v95\n"
    "v_mfma_f32_16x16x32_bf16 a[80:83], a[180:183], v[144:147], a[80:83]\n"
    "ds_read_b64_tr_b16 a[178:179], v12 offset:54272\n"
    "v_fma_f32 v88, v88, s57, v166\n"
    "v_fma_f32 v89, v89, s57, v166\n"
    "v_mfma_f32_16x16x32_bf16 a[84:87], a[180:183], v[148:151], a[84:87]\n"
    "v_fma_f32 v90, v90, s57, v166\n"
    "v_fma_f32 v91, v91, s57, v166\n"
    "v_mfma_f32_16x16x32_bf16 a[88:91], a[180:183], v[152:155], a[88:91]\n"
    "v_fma_f32 v92, v92, s57, v166\n"
    "v_fma_f32 v93, v93, s57, v166\n"
    "v_mfma_f32_16x16x32_bf16 a[92:95], a[180:183], v[156:159], a[92:95]\n"
    "v_exp_f32 v88, v88\n"
    "v_mfma_f32_16x16x32_bf16 v[100:103], a[152:155], v[200:203], v[100:103]\n"
    "v_fma_f32 v94, v94, s57, v166\n"
    "v_exp_f32 v89, v89\n"
    "v_mfma_f32_16x16x32_bf16 v[108:111], a[152:155], v[216:219], v[108:111]\n"
    "v_exp_f32 v90, v90\n"
    "v_mfma_f32_16x16x32_bf16 v[116:119], a[152:155], v[232:235], v[116:119]\n"
    "v_fma_f32 v95, v95, s57, v166\n"
    "v_exp_f32 v91, v91\n"
    "v_mfma_f32_16x16x32_bf16 v[124:127], a[152:155], v[248:251], v[124:127]\n"
    "v_exp_f32 v92, v92\n"
    "v_mfma_f32_16x16x32_bf16 a[96:99], a[184:187], v[144:147], a[96:99]\n"
    "v_exp_f32 v93, v93\n"
    "v_mfma_f32_16x16x32_bf16 a[100:103], a[184:187], v[148:151], a[100:103]\n"
    "v_exp_f32 v94, v94\n"
    "v_exp_f32 v95, v95\n"
    "v_mfma_f32_16x16x32_bf16 a[104:107], a[184:187], v[152:155], a[104:107]\n"
    "v_cvt_pk_bf16_f32 v140, v88, v89\n"
    "v_mfma_f32_16x16x32_bf16 a[108:111], a[184:187], v[156:159], a[108:111]\n"
    "s_waitcnt lgkmcnt(0)\n"
    "v_cvt_pk_bf16_f32 v141, v90, v91\n"
    "v_cvt_pk_bf16_f32 v142, v92, v93\n"
    "v_cvt_pk_bf16_f32 v143, v94, v95\n"
    "v_mfma_f32_16x16x32_bf16 v[100:103], a[156:159], v[204:207], v[100:103]\n"
    "ds_read_b64_tr_b16 a[180:181], v12 offset:50432\n"
    "v_cvt_pk_bf16_f32 v96, 0, v96\n"
    "v_mfma_f32_16x16x32_bf16 v[188:191], a[224:227], v[140:143], v[52:55]\n"
    "ds_read_b64_tr_b16 a[182:183], v12 offset:54528\n"
    "v_cvt_pk_bf16_f32 v97, 0, v97\n"
    "v_mfma_f32_16x16x32_bf16 v[108:111], a[156:159], v[220:223], v[108:111]\n"
    "ds_read_b64_tr_b16 a[184:185], v12 offset:50688\n"
    "v_cvt_pk_bf16_f32 v98, 0, v98\n"
    "v_mfma_f32_16x16x32_bf16 v[116:119], a[156:159], v[236:239], v[116:119]\n"
    "ds_read_b64_tr_b16 a[186:187], v12 offset:54784\n"
    "v_cvt_pk_bf16_f32 v99, 0, v99\n"
    "v_cvt_pk_bf16_f32 v104, 0, v104\n"
    "v_mfma_f32_16x16x32_bf16 v[124:127], a[156:159], v[252:255], v[124:127]\n"
    "v_cvt_pk_bf16_f32 v105, 0, v105\n"
    "v_cvt_pk_bf16_f32 v106, 0, v106\n"
    "v_mfma_f32_16x16x32_bf16 a[112:115], a[188:191], v[144:147], a[112:115]\n"
    "v_max3_f32 v19, v176, v180, v184\n"
    "v_cvt_pk_bf16_f32 v107, 0, v107\n"
    "v_mfma_f32_16x16x32_bf16 a[116:119], a[188:191], v[148:151], a[116:119]\n"
    "v_max_f32 v19, v19, v188\n"
    "v_cvt_pk_bf16_f32 v112, 0, v112\n"
    "v_mfma_f32_16x16x32_bf16 a[120:123], a[188:191], v[152:155], a[120:123]\n"
    "v_cmp_lt_f32 vcc, s25, v19\n"
    "v_cvt_pk_bf16_f32 v113, 0, v113\n"
    "v_cvt_pk_bf16_f32 v114, 0, v114\n"
    "v_mfma_f32_16x16x32_bf16 a[124:127], a[188:191], v[156:159], a[124:127]\n"
    "ds_read_b64_tr_b16 a[188:189], v12 offset:50944\n"
    "ds_read_b64_tr_b16 a[190:191], v12 offset:55040\n"
    "v_cvt_pk_bf16_f32 v115, 0, v115\n"
    "v_cvt_pk_bf16_f32 v120, 0, v120\n"
    "v_cvt_pk_bf16_f32 v121, 0, v121\n"
    "v_cvt_pk_bf16_f32 v122, 0, v122\n"
    "v_cvt_pk_bf16_f32 v123, 0, v123\n"
    "s_cbranch_vccnz RARE_t62_%=\n"
    "RARE_t62_%=_ret:\n"
    "v_cvt_pk_bf16_f32 v100, 0, v100\n"
    "v_cvt_pk_bf16_f32 v101, 0, v101\n"
    "v_cvt_pk_bf16_f32 v102, 0, v102\n"
    "v_cvt_pk_bf16_f32 v103, 0, v103\n"
    "v_fma_f32 v96, v96, s57, v160\n"
    "v_fma_f32 v97, v97, s57, v160\n"
    "v_fma_f32 v98, v98, s57, v160\n"
    "v_mfma_f32_16x16x32_bf16 a[0:3], a[160:163], v[128:131], a[0:3]\n"
    "v_exp_f32 v96, v96\n"
    "v_mfma_f32_16x16x32_bf16 a[4:7], a[160:163], v[132:135], a[4:7]\n"
    "v_fma_f32 v99, v99, s57, v160\n"
    "v_exp_f32 v97, v97\n"
    "v_mfma_f32_16x16x32_bf16 a[8:11], a[160:163], v[136:139], a[8:11]\n"
    "v_fma_f32 v100, v100, s57, v160\n"
    "v_cvt_pk_bf16_f32 v144, v96, v97\n"
    "v_mfma_f32_16x16x32_bf16 a[12:15], a[160:163], v[140:143], a[12:15]\n"
    "v_exp_f32 v98, v98\n"
    "v_fma_f32 v101, v101, s57, v160\n"
    "v_cvt_pk_bf16_f32 v108, 0, v108\n"
    "v_exp_f32 v99, v99\n"
    "v_fma_f32 v102, v102, s57, v160\n"
    "v_cvt_pk_bf16_f32 v145, v98, v99\n"
    "v_fma_f32 v103, v103, s57, v160\n"
    "v_cvt_pk_bf16_f32 v109, 0, v109\n"
    "v_mfma_f32_16x16x32_bf16 a[16:19], a[164:167], v[128:131], a[16:19]\n"
    "v_exp_f32 v100, v100\n"
    "v_mfma_f32_16x16x32_bf16 a[20:23], a[164:167], v[132:135], a[20:23]\n"
    "v_exp_f32 v101, v101\n"
    "v_mfma_f32_16x16x32_bf16 a[24:27], a[164:167], v[136:139], a[24:27]\n"
    "v_exp_f32 v102, v102\n"
    "v_exp_f32 v103, v103\n"
    "v_mfma_f32_16x16x32_bf16 a[28:31], a[164:167], v[140:143], a[28:31]\n"
    "v_cvt_pk_bf16_f32 v146, v100, v101\n"
    "v_cvt_pk_bf16_f32 v147, v102, v103\n"
    "v_cvt_pk_bf16_f32 v110, 0, v110\n"
    "ds_read_b64_tr_b16 a[160:161], v12 offset:57344\n"
    "v_cvt_pk_bf16_f32 v111, 0, v111\n"
    "v_mfma_f32_16x16x32_bf16 v[40:43], a[224:227], v[144:147], v[176:179]\n"
    "ds_read_b64_tr_b16 a[162:163], v12 offset:61440\n"
    "v_fma_f32 v104, v104, s57, v162\n"
    "v_fma_f32 v105, v105, s57, v162\n"
    "v_fma_f32 v106, v106, s57, v162\n"
    "v_fma_f32 v107, v107, s57, v162\n"
    "v_fma_f32 v108, v108, s57, v162\n"
    "v_mfma_f32_16x16x32_bf16 a[32:35], a[168:171], v[128:131], a[32:35]\n"
    "v_fma_f32 v109, v109, s57, v162\n"
    "v_cvt_pk_bf16_f32 v116, 0, v116\n"
    "v_mfma_f32_16x16x32_bf16 a[36:39], a[168:171], v[132:135], a[36:39]\n"
    "ds_read_b64_tr_b16 a[164:165], v12 offset:57600\n"
    "v_cvt_pk_bf16_f32 v117, 0, v117\n"
    "v_mfma_f32_16x16x32_bf16 a[40:43], a[168:171], v[136:139], a[40:43]\n"
    "v_fma_f32 v110, v110, s57, v162\n"
    "v_cvt_pk_bf16_f32 v118, 0, v118\n"
    "v_mfma_f32_16x16x32_bf16 a[44:47], a[168:171], v[140:143], a[44:47]\n"
    "s_add_u32 m0, s73, 0x10000\n"
    "s_add_u32 s68, s72, 0xd00\n"
    "buffer_load_dwordx4 v57, s[60:63], s68 offen offset:0 lds\n"
    "ds_read_b64_tr_b16 a[166:167], v12 offset:61696\n"
    "v_cvt_pk_bf16_f32 v119, 0, v119\n"
    "v_fma_f32 v111, v111, s57, v162\n"
    "s_waitcnt lgkmcnt(2)\n"
    "v_exp_f32 v104, v104\n"
    "v_fma_f32 v112, v112, s57, v164\n"
    "v_exp_f32 v105, v105\n"
    "v_exp_f32 v106, v106\n"
    "v_exp_f32 v107, v107\n"
    "v_exp_f32 v108, v108\n"
    "v_exp_f32 v109, v109\n"
    "v_exp_f32 v110, v110\n"
    "v_exp_f32 v111, v111\n"
    "v_mfma_f32_16x16x32_bf16 a[48:51], a[172:175], v[128:131], a[48:51]\n"
    "v_mfma_f32_16x16x32_bf16 a[52:55], a[172:175], v[132:135], a[52:55]\n"
    "v_cvt_pk_bf16_f32 v148, v104, v105\n"
    "v_cvt_pk_bf16_f32 v149, v106, v107\n"
    "v_cvt_pk_bf16_f32 v150, v108, v109\n"
    "v_cvt_pk_bf16_f32 v151, v110, v111\n"
    "v_mfma_f32_16x16x32_bf16 a[56:59], a[172:175], v[136:139], a[56:59]\n"
    "s_nop 0\n"
    "v_mfma_f32_16x16x32_bf16 v[44:47], a[224:227], v[148:151], v[180:183]\n"
    "ds_read_b64_tr_b16 a[168:169], v12 offset:57856\n"
    "v_fma_f32 v113, v113, s57, v164\n"
    "v_mfma_f32_16x16x32_bf16 a[60:63], a[172:175], v[140:143], a[60:63]\n"
    "ds_read_b64_tr_b16 a[170:171], v12 offset:61952\n"
    "v_fma_f32 v114, v114, s57, v164\n"
    "v_fma_f32 v115, v115, s57, v164\n"
    "v_fma_f32 v116, v116, s57, v164\n"
    "v_fma_f32 v117, v117, s57, v164\n"
    "v_cvt_pk_bf16_f32 v124, 0, v124\n"
    "v_exp_f32 v112, v112\n"
    "v_fma_f32 v118, v118, s57, v164\n"
    "v_cvt_pk_bf16_f32 v125, 0, v125\n"
    "v_mfma_f32_16x16x32_bf16 a[64:67], a[176:179], v[128:131], a[64:67]\n"
    "s_add_u32 s68, s72, 0x2d00\n"
    "buffer_load_dwordx4 v57, s[60:63], s68 offen offset:1024 lds\n"
    "v_exp_f32 v113, v113\n"
    "v_mfma_f32_16x16x32_bf16 a[68:71], a[176:179], v[132:135], a[68:71]\n"
    "v_fma_f32 v119, v119, s57, v164\n"
    "v_cvt_pk_bf16_f32 v152, v112, v113\n"
    "v_mfma_f32_16x16x32_bf16 a[72:75], a[176:179], v[136:139], a[72:75]\n"
    "v_exp_f32 v114, v114\n"
    "v_mfma_f32_16x16x32_bf16 a[76:79], a[176:179], v[140:143], a[76:79]\n"
    "v_exp_f32 v115, v115\n"
    "v_cvt_pk_bf16_f32 v126, 0, v126\n"
    "v_exp_f32 v116, v116\n"
    "v_exp_f32 v117, v117\n"
    "v_exp_f32 v118, v118\n"
    "v_exp_f32 v119, v119\n"
    "v_cvt_pk_bf16_f32 v153, v114, v115\n"
    "v_cvt_pk_bf16_f32 v154, v116, v117\n"
    "v_cvt_pk_bf16_f32 v155, v118, v119\n"
    "ds_read_b64_tr_b16 a[172:173], v12 offset:58112\n"
    "ds_read_b64_tr_b16 a[174:175], v12 offset:62208\n"
    "v_mfma_f32_16x16x32_bf16 v[48:51], a[224:227], v[152:155], v[184:187]\n"
    "ds_read_b64_tr_b16 a[176:177], v12 offset:58368\n"
    "v_mfma_f32_16x16x32_bf16 a[80:83], a[180:183], v[128:131], a[80:83]\n"
    "ds_read_b64_tr_b16 a[178:179], v12 offset:62464\n"
    "v_cvt_pk_bf16_f32 v127, 0, v127\n"
    "v_mfma_f32_16x16x32_bf16 a[84:87], a[180:183], v[132:135], a[84:87]\n"
    "s_add_u32 s68, s72, 0x4d00\n"
    "buffer_load_dwordx4 v57, s[60:63], s68 offen offset:2048 lds\n"
    "v_fma_f32 v120, v120, s57, v166\n"
    "v_fma_f32 v121, v121, s57, v166\n"
    "v_mfma_f32_16x16x32_bf16 a[88:91], a[180:183], v[136:139], a[88:91]\n"
    "v_fma_f32 v122, v122, s57, v166\n"
    "v_fma_f32 v123, v123, s57, v166\n"
    "v_mfma_f32_16x16x32_bf16 a[92:95], a[180:183], v[140:143], a[92:95]\n"
    "v_fma_f32 v124, v124, s57, v166\n"
    "v_fma_f32 v125, v125, s57, v166\n"
    "v_fma_f32 v126, v126, s57, v166\n"
    "v_exp_f32 v120, v120\n"
    "v_fma_f32 v127, v127, s57, v166\n"
    "v_exp_f32 v121, v121\n"
    "v_exp_f32 v122, v122\n"
    "v_cvt_pk_bf16_f32 v156, v120, v121\n"
    "v_mfma_f32_16x16x32_bf16 a[96:99], a[184:187], v[128:131], a[96:99]\n"
    "v_exp_f32 v123, v123\n"
    "v_mfma_f32_16x16x32_bf16 a[100:103], a[184:187], v[132:135], a[100:103]\n"
    "v_exp_f32 v124, v124\n"
    "v_exp_f32 v125, v125\n"
    "v_exp_f32 v126, v126\n"
    "v_exp_f32 v127, v127\n"
    "v_mfma_f32_16x16x32_bf16 a[104:107], a[184:187], v[136:139], a[104:107]\n"
    "v_mfma_f32_16x16x32_bf16 a[108:111], a[184:187], v[140:143], a[108:111]\n"
    "s_add_u32 s68, s72, 0x6d00\n"
    "buffer_load_dwordx4 v57, s[60:63], s68 offen offset:3072 lds\n"
    "s_waitcnt lgkmcnt(0)\n"
    "v_cvt_pk_bf16_f32 v157, v122, v123\n"
    "v_cvt_pk_bf16_f32 v158, v124, v125\n"
    "v_cvt_pk_bf16_f32 v159, v126, v127\n"
    "ds_read_b64_tr_b16 a[180:181], v12 offset:58624\n"
    "s_nop 0\n"
    "v_mfma_f32_16x16x32_bf16 v[52:55], a[224:227], v[156:159], v[188:191]\n"
    "ds_read_b64_tr_b16 a[182:183], v12 offset:62720\n"
    "ds_read_b64_tr_b16 a[184:185], v12 offset:58880\n"
    "ds_read_b64_tr_b16 a[186:187], v12 offset:62976\n"
    "v_mfma_f32_16x16x32_bf16 a[112:115], a[188:191], v[128:131], a[112:115]\n"
    "v_max3_f32 v19, v40, v44, v48\n"
    "v_mfma_f32_16x16x32_bf16 a[116:119], a[188:191], v[132:135], a[116:119]\n"
    "s_nop 1\n"
    "v_max_f32 v19, v19, v52\n"
    "v_mfma_f32_16x16x32_bf16 a[120:123], a[188:191], v[136:139], a[120:123]\n"
    "v_cmp_lt_f32 vcc, s25, v19\n"
    "v_mfma_f32_16x16x32_bf16 a[124:127], a[188:191], v[140:143], a[124:127]\n"
    "ds_read_b64_tr_b16 a[188:189], v12 offset:59136\n"
    "ds_read_b64_tr_b16 a[190:191], v12 offset:63232\n"
    "s_cbranch_vccnz RARE_t63_%=\n"
    "RARE_t63_%=_ret:\n"
    "s_waitcnt lgkmcnt(0)\n"
    "v_mfma_f32_16x16x32_bf16 a[0:3], a[160:163], v[144:147], a[0:3]\n"
    "v_mfma_f32_16x16x32_bf16 a[4:7], a[160:163], v[148:151], a[4:7]\n"
    "v_mfma_f32_16x16x32_bf16 a[8:11], a[160:163], v[152:155], a[8:11]\n"
    "v_mfma_f32_16x16x32_bf16 a[12:15], a[160:163], v[156:159], a[12:15]\n"
    "v_mfma_f32_16x16x32_bf16 a[16:19], a[164:167], v[144:147], a[16:19]\n"
    "v_mfma_f32_16x16x32_bf16 a[20:23], a[164:167], v[148:151], a[20:23]\n"
    "v_mfma_f32_16x16x32_bf16 a[24:27], a[164:167], v[152:155], a[24:27]\n"
    "v_mfma_f32_16x16x32_bf16 a[28:31], a[164:167], v[156:159], a[28:31]\n"
    "v_mfma_f32_16x16x32_bf16 a[32:35], a[168:171], v[144:147], a[32:35]\n"
    "v_mfma_f32_16x16x32_bf16 a[36:39], a[168:171], v[148:151], a[36:39]\n"
    "v_mfma_f32_16x16x32_bf16 a[40:43], a[168:171], v[152:155], a[40:43]\n"
    "v_mfma_f32_16x16x32_bf16 a[44:47], a[168:171], v[156:159], a[44:47]\n"
    "v_mfma_f32_16x16x32_bf16 a[48:51], a[172:175], v[144:147], a[48:51]\n"
    "s_add_u32 m0, s73, 0x11000\n"
    "s_add_u32 s68, s72, 0x9d00\n"
    "buffer_load_dwordx4 v57, s[60:63], s68 offen offset:0 lds\n"
    "v_mfma_f32_16x16x32_bf16 a[52:55], a[172:175], v[148:151], a[52:55]\n"
    "v_mfma_f32_16x16x32_bf16 a[56:59], a[172:175], v[152:155], a[56:59]\n"
    "v_mfma_f32_16x16x32_bf16 a[60:63], a[172:175], v[156:159], a[60:63]\n"
    "v_mfma_f32_16x16x32_bf16 a[64:67], a[176:179], v[144:147], a[64:67]\n"
    "v_mfma_f32_16x16x32_bf16 a[68:71], a[176:179], v[148:151], a[68:71]\n"
    "v_mfma_f32_16x16x32_bf16 a[72:75], a[176:179], v[152:155], a[72:75]\n"
    "s_add_u32 s68, s72, 0xbd00\n"
    "buffer_load_dwordx4 v57, s[60:63], s68 offen offset:1024 lds\n"
    "v_mfma_f32_16x16x32_bf16 a[76:79], a[176:179], v[156:159], a[76:79]\n"
    "v_mfma_f32_16x16x32_bf16 a[80:83], a[180:183], v[144:147], a[80:83]\n"
    "v_mfma_f32_16x16x32_bf16 a[84:87], a[180:183], v[148:151], a[84:87]\n"
    "v_mfma_f32_16x16x32_bf16 a[88:91], a[180:183], v[152:155], a[88:91]\n"
    "v_mfma_f32_16x16x32_bf16 a[92:95], a[180:183], v[156:159], a[92:95]\n"
    "v_mfma_f32_16x16x32_bf16 a[96:99], a[184:187], v[144:147], a[96:99]\n"
    "s_add_u32 s68, s72, 0xdd00\n"
    "buffer_load_dwordx4 v57, s[60:63], s68 offen offset:2048 lds\n"
    "v_mfma_f32_16x16x32_bf16 a[100:103], a[184:187], v[148:151], a[100:103]\n"
    "v_mfma_f32_16x16x32_bf16 a[104:107], a[184:187], v[152:155], a[104:107]\n"
    "v_mfma_f32_16x16x32_bf16 a[108:111], a[184:187], v[156:159], a[108:111]\n"
    "v_mfma_f32_16x16x32_bf16 a[112:115], a[188:191], v[144:147], a[112:115]\n"
    "v_mfma_f32_16x16x32_bf16 a[116:119], a[188:191], v[148:151], a[116:119]\n"
    "v_mfma_f32_16x16x32_bf16 a[120:123], a[188:191], v[152:155], a[120:123]\n"
    "s_add_u32 s68, s72, 0xfd00\n"
    "buffer_load_dwordx4 v57, s[60:63], s68 offen offset:3072 lds\n"
    "v_mfma_f32_16x16x32_bf16 a[124:127], a[188:191], v[156:159], a[124:127]\n"
    "s_waitcnt vmcnt(16)\n"
    "s_barrier\n"
    "ds_read_b64_tr_b16 a[128:129], v8 offset:0\n"
    "ds_read_b64_tr_b16 a[130:131], v8 offset:2048\n"
    "ds_read_b64_tr_b16 a[132:133], v8 offset:256\n"
    "ds_read_b64_tr_b16 a[134:135], v8 offset:2304\n"
    "ds_read_b64_tr_b16 a[136:137], v8 offset:512\n"
    "ds_read_b64_tr_b16 a[138:139], v8 offset:2560\n"
    "ds_read_b64_tr_b16 a[140:141], v8 offset:768\n"
    "ds_read_b64_tr_b16 a[142:143], v8 offset:2816\n"
    "ds_read_b64_tr_b16 a[144:145], v8 offset:4096\n"
    "ds_read_b64_tr_b16 a[146:147], v8 offset:6144\n"
    "ds_read_b64_tr_b16 a[148:149], v8 offset:4352\n"
    "ds_read_b64_tr_b16 a[150:151], v8 offset:6400\n"
    "ds_read_b64_tr_b16 a[152:153], v8 offset:4608\n"
    "ds_read_b64_tr_b16 a[154:155], v8 offset:6656\n"
    "ds_read_b64_tr_b16 a[156:157], v8 offset:4864\n"
    "ds_read_b64_tr_b16 a[158:159], v8 offset:6912\n"
    "ds_read_b64_tr_b16 a[192:193], v8 offset:8192\n"
    "ds_read_b64_tr_b16 a[194:195], v8 offset:10240\n"
    "ds_read_b64_tr_b16 a[196:197], v8 offset:8448\n"
    "ds_read_b64_tr_b16 a[198:199], v8 offset:10496\n"
    "ds_read_b64_tr_b16 a[200:201], v8 offset:8704\n"
    "ds_read_b64_tr_b16 a[202:203], v8 offset:10752\n"
    "ds_read_b64_tr_b16 a[204:205], v8 offset:8960\n"
    "ds_read_b64_tr_b16 a[206:207], v8 offset:11008\n"
    "ds_read_b64_tr_b16 a[208:209], v8 offset:12288\n"
    "ds_read_b64_tr_b16 a[210:211], v8 offset:14336\n"
    "ds_read_b64_tr_b16 a[212:213], v8 offset:12544\n"
    "ds_read_b64_tr_b16 a[214:215], v8 offset:14592\n"
    "ds_read_b64_tr_b16 a[216:217], v8 offset:12800\n"
    "ds_read_b64_tr_b16 a[218:219], v8 offset:14848\n"
    "ds_read_b64_tr_b16 a[220:221], v8 offset:13056\n"
    "ds_read_b64_tr_b16 a[222:223], v8 offset:15104\n"
    "s_add_u32 m0, s73, 0x8000\n"
    "s_add_u32 s68, s72, 0xd80\n"
    "buffer_load_dwordx4 v57, s[60:63], s68 offen offset:0 lds\n"
    "s_add_u32 s68, s72, 0x2d80\n"
    "buffer_load_dwordx4 v57, s[60:63], s68 offen offset:1024 lds\n"
    "s_add_u32 s68, s72, 0x4d80\n"
    "buffer_load_dwordx4 v57, s[60:63], s68 offen offset:2048 lds\n"
    "s_add_u32 s68, s72, 0x6d80\n"
    "buffer_load_dwordx4 v57, s[60:63], s68 offen offset:3072 lds\n"
    "s_add_u32 m0, s73, 0x9000\n"
    "s_add_u32 s68, s72, 0x9d80\n"
    "buffer_load_dwordx4 v57, s[60:63], s68 offen offset:0 lds\n"
    "s_add_u32 s68, s72, 0xbd80\n"
    "buffer_load_dwordx4 v57, s[60:63], s68 offen offset:1024 lds\n"
    "s_add_u32 s68, s72, 0xdd80\n"
    "buffer_load_dwordx4 v57, s[60:63], s68 offen offset:2048 lds\n"
    "s_add_u32 s68, s72, 0xfd80\n"
    "buffer_load_dwordx4 v57, s[60:63], s68 offen offset:3072 lds\n"
    "v_rcp_f32 v24, v40\n"
    "v_rcp_f32 v25, v44\n"
    "v_rcp_f32 v26, v48\n"
    "v_rcp_f32 v27, v52\n"
    "v_accvgpr_read_b32 v128, a0\n"
    "v_accvgpr_read_b32 v129, a1\n"
    "v_accvgpr_read_b32 v130, a2\n"
    "v_accvgpr_read_b32 v131, a3\n"
    "v_accvgpr_read_b32 v132, a16\n"
    "v_accvgpr_read_b32 v133, a17\n"
    "v_accvgpr_read_b32 v134, a18\n"
    "v_accvgpr_read_b32 v135, a19\n"
    "v_mul_f32 v128, v128, v24\n"
    "v_mul_f32 v129, v129, v24\n"
    "v_mul_f32 v130, v130, v24\n"
    "v_mul_f32 v131, v131, v24\n"
    "v_mul_f32 v132, v132, v24\n"
    "v_mul_f32 v133, v133, v24\n"
    "v_mul_f32 v134, v134, v24\n"
    "v_mul_f32 v135, v135, v24\n"
    "v_cvt_pk_bf16_f32 v192, v128, v129\n"
    "v_cvt_pk_bf16_f32 v193, v130, v131\n"
    "v_cvt_pk_bf16_f32 v194, v132, v133\n"
    "v_cvt_pk_bf16_f32 v195, v134, v135\n"
    "v_accvgpr_read_b32 v136, a4\n"
    "v_accvgpr_read_b32 v137, a5\n"
    "v_accvgpr_read_b32 v138, a6\n"
    "v_accvgpr_read_b32 v139, a7\n"
    "v_accvgpr_read_b32 v140, a20\n"
    "v_accvgpr_read_b32 v141, a21\n"
    "v_accvgpr_read_b32 v142, a22\n"
    "v_accvgpr_read_b32 v143, a23\n"
    "v_mul_f32 v136, v136, v25\n"
    "v_mul_f32 v137, v137, v25\n"
    "v_mul_f32 v138, v138, v25\n"
    "v_mul_f32 v139, v139, v25\n"
    "v_mul_f32 v140, v140, v25\n"
    "v_mul_f32 v141, v141, v25\n"
    "v_mul_f32 v142, v142, v25\n"
    "v_mul_f32 v143, v143, v25\n"
    "v_cvt_pk_bf16_f32 v196, v136, v137\n"
    "v_cvt_pk_bf16_f32 v197, v138, v139\n"
    "v_cvt_pk_bf16_f32 v198, v140, v141\n"
    "v_cvt_pk_bf16_f32 v199, v142, v143\n"
    "v_accvgpr_read_b32 v144, a32\n"
    "v_accvgpr_read_b32 v145, a33\n"
    "v_accvgpr_read_b32 v146, a34\n"
    "v_accvgpr_read_b32 v147, a35\n"
    "v_accvgpr_read_b32 v148, a48\n"
    "v_accvgpr_read_b32 v149, a49\n"
    "v_accvgpr_read_b32 v150, a50\n"
    "v_accvgpr_read_b32 v151, a51\n"
    "v_mul_f32 v144, v144, v24\n"
    "v_mul_f32 v145, v145, v24\n"
    "v_mul_f32 v146, v146, v24\n"
    "v_mul_f32 v147, v147, v24\n"
    "v_mul_f32 v148, v148, v24\n"
    "v_mul_f32 v149, v149, v24\n"
    "v_mul_f32 v150, v150, v24\n"
    "v_mul_f32 v151, v151, v24\n"
    "v_cvt_pk_bf16_f32 v200, v144, v145\n"
    "v_cvt_pk_bf16_f32 v201, v146, v147\n"
    "v_cvt_pk_bf16_f32 v202, v148, v149\n"
    "v_cvt_pk_bf16_f32 v203, v150, v151\n"
    "v_accvgpr_read_b32 v152, a36\n"
    "v_accvgpr_read_b32 v153, a37\n"
    "v_accvgpr_read_b32 v154, a38\n"
    "v_accvgpr_read_b32 v155, a39\n"
    "v_accvgpr_read_b32 v156, a52\n"
    "v_accvgpr_read_b32 v157, a53\n"
    "v_accvgpr_read_b32 v158, a54\n"
    "v_accvgpr_read_b32 v159, a55\n"
    "v_mul_f32 v152, v152, v25\n"
    "v_mul_f32 v153, v153, v25\n"
    "v_mul_f32 v154, v154, v25\n"
    "v_mul_f32 v155, v155, v25\n"
    "v_mul_f32 v156, v156, v25\n"
    "v_mul_f32 v157, v157, v25\n"
    "v_mul_f32 v158, v158, v25\n"
    "v_mul_f32 v159, v159, v25\n"
    "v_cvt_pk_bf16_f32 v204, v152, v153\n"
    "v_cvt_pk_bf16_f32 v205, v154, v155\n"
    "v_cvt_pk_bf16_f32 v206, v156, v157\n"
    "v_cvt_pk_bf16_f32 v207, v158, v159\n"
    "s_waitcnt lgkmcnt(0)\n"
    "v_mfma_f32_16x16x32_bf16 v[64:67], a[128:131], v[192:195], 0\n"
    "v_accvgpr_read_b32 v128, a64\n"
    "v_accvgpr_read_b32 v129, a65\n"
    "v_mfma_f32_16x16x32_bf16 v[68:71], a[128:131], v[196:199], 0\n"
    "ds_read_b64_tr_b16 a[128:129], v8 offset:16384\n"
    "v_accvgpr_read_b32 v130, a66\n"
    "v_mfma_f32_16x16x32_bf16 v[72:75], a[132:135], v[192:195], 0\n"
    "ds_read_b64_tr_b16 a[130:131], v8 offset:18432\n"
    "v_accvgpr_read_b32 v131, a67\n"
    "v_mfma_f32_16x16x32_bf16 v[76:79], a[132:135], v[196:199], 0\n"
    "ds_read_b64_tr_b16 a[132:133], v8 offset:16640\n"
    "v_accvgpr_read_b32 v132, a80\n"
    "v_mfma_f32_16x16x32_bf16 v[80:83], a[136:139], v[192:195], 0\n"
    "ds_read_b64_tr_b16 a[134:135], v8 offset:18688\n"
    "v_accvgpr_read_b32 v133, a81\n"
    "v_mfma_f32_16x16x32_bf16 v[84:87], a[136:139], v[196:199], 0\n"
    "ds_read_b64_tr_b16 a[136:137], v8 offset:16896\n"
    "v_accvgpr_read_b32 v134, a82\n"
    "v_mfma_f32_16x16x32_bf16 v[88:91], a[140:143], v[192:195], 0\n"
    "ds_read_b64_tr_b16 a[138:139], v8 offset:18944\n"
    "v_accvgpr_read_b32 v135, a83\n"
    "v_mfma_f32_16x16x32_bf16 v[92:95], a[140:143], v[196:199], 0\n"
    "ds_read_b64_tr_b16 a[140:141], v8 offset:17152\n"
    "v_mul_f32 v128, v128, v24\n"
    "v_mfma_f32_16x16x32_bf16 v[64:67], a[144:147], v[200:203], v[64:67]\n"
    "ds_read_b64_tr_b16 a[142:143], v8 offset:19200\n"
    "v_mul_f32 v129, v129, v24\n"
    "v_mfma_f32_16x16x32_bf16 v[68:71], a[144:147], v[204:207], v[68:71]\n"
    "ds_read_b64_tr_b16 a[144:145], v8 offset:20480\n"
    "v_mul_f32 v130, v130, v24\n"
    "v_mfma_f32_16x16x32_bf16 v[72:75], a[148:151], v[200:203], v[72:75]\n"
    "ds_read_b64_tr_b16 a[146:147], v8 offset:22528\n"
    "v_mul_f32 v131, v131, v24\n"
    "v_mfma_f32_16x16x32_bf16 v[76:79], a[148:151], v[204:207], v[76:79]\n"
    "v_mul_f32 v132, v132, v24\n"
    "v_mul_f32 v133, v133, v24\n"
    "v_mfma_f32_16x16x32_bf16 v[80:83], a[152:155], v[200:203], v[80:83]\n"
    "v_mul_f32 v134, v134, v24\n"
    "v_mul_f32 v135, v135, v24\n"
    "v_mfma_f32_16x16x32_bf16 v[84:87], a[152:155], v[204:207], v[84:87]\n"
    "v_cvt_pk_bf16_f32 v208, v128, v129\n"
    "v_cvt_pk_bf16_f32 v209, v130, v131\n"
    "v_mfma_f32_16x16x32_bf16 v[88:91], a[156:159], v[200:203], v[88:91]\n"
    "ds_read_b64_tr_b16 a[148:149], v8 offset:20736\n"
    "v_cvt_pk_bf16_f32 v210, v132, v133\n"
    "v_cvt_pk_bf16_f32 v211, v134, v135\n"
    "v_mfma_f32_16x16x32_bf16 v[92:95], a[156:159], v[204:207], v[92:95]\n"
    "ds_read_b64_tr_b16 a[150:151], v8 offset:22784\n"
    "v_accvgpr_read_b32 v136, a68\n"
    "v_accvgpr_read_b32 v137, a69\n"
    "v_accvgpr_read_b32 v138, a70\n"
    "v_accvgpr_read_b32 v139, a71\n"
    "v_accvgpr_read_b32 v140, a84\n"
    "v_accvgpr_read_b32 v141, a85\n"
    "v_accvgpr_read_b32 v142, a86\n"
    "v_accvgpr_read_b32 v143, a87\n"
    "v_mul_f32 v136, v136, v25\n"
    "v_mul_f32 v137, v137, v25\n"
    "v_mul_f32 v138, v138, v25\n"
    "v_mul_f32 v139, v139, v25\n"
    "v_mul_f32 v140, v140, v25\n"
    "v_mul_f32 v141, v141, v25\n"
    "v_mul_f32 v142, v142, v25\n"
    "v_mul_f32 v143, v143, v25\n"
    "v_cvt_pk_bf16_f32 v212, v136, v137\n"
    "v_cvt_pk_bf16_f32 v213, v138, v139\n"
    "v_cvt_pk_bf16_f32 v214, v140, v141\n"
    "v_cvt_pk_bf16_f32 v215, v142, v143\n"
    "v_mfma_f32_16x16x32_bf16 v[64:67], a[192:195], v[208:211], v[64:67]\n"
    "ds_read_b64_tr_b16 a[152:153], v8 offset:20992\n"
    "v_accvgpr_read_b32 v144, a96\n"
    "v_mfma_f32_16x16x32_bf16 v[68:71], a[192:195], v[212:215], v[68:71]\n"
    "ds_read_b64_tr_b16 a[154:155], v8 offset:23040\n"
    "v_accvgpr_read_b32 v145, a97\n"
    "v_mfma_f32_16x16x32_bf16 v[72:75], a[196:199], v[208:211], v[72:75]\n"
    "ds_read_b64_tr_b16 a[156:157], v8 offset:21248\n"
    "v_accvgpr_read_b32 v146, a98\n"
    "v_mfma_f32_16x16x32_bf16 v[76:79], a[196:199], v[212:215], v[76:79]\n"
    "ds_read_b64_tr_b16 a[158:159], v8 offset:23296\n"
    "v_accvgpr_read_b32 v147, a99\n"
    "v_mfma_f32_16x16x32_bf16 v[80:83], a[200:203], v[208:211], v[80:83]\n"
    "ds_read_b64_tr_b16 a[192:193], v8 offset:24576\n"
    "v_accvgpr_read_b32 v148, a112\n"
    "v_mfma_f32_16x16x32_bf16 v[84:87], a[200:203], v[212:215], v[84:87]\n"
    "ds_read_b64_tr_b16 a[194:195], v8 offset:26624\n"
    "v_accvgpr_read_b32 v149, a113\n"
    "v_mfma_f32_16x16x32_bf16 v[88:91], a[204:207], v[208:211], v[88:91]\n"
    "ds_read_b64_tr_b16 a[196:197], v8 offset:24832\n"
    "v_accvgpr_read_b32 v150, a114\n"
    "v_accvgpr_read_b32 v151, a115\n"
    "v_mul_f32 v144, v144, v24\n"
    "v_mul_f32 v145, v145, v24\n"
    "v_mul_f32 v146, v146, v24\n"
    "v_mul_f32 v147, v147, v24\n"
    "v_mul_f32 v148, v148, v24\n"
    "v_mul_f32 v149, v149, v24\n"
    "v_mul_f32 v150, v150, v24\n"
    "v_mul_f32 v151, v151, v24\n"
    "v_cvt_pk_bf16_f32 v216, v144, v145\n"
    "v_cvt_pk_bf16_f32 v217, v146, v147\n"
    "v_cvt_pk_bf16_f32 v218, v148, v149\n"
    "v_cvt_pk_bf16_f32 v219, v150, v151\n"
    "v_mfma_f32_16x16x32_bf16 v[92:95], a[204:207], v[212:215], v[92:95]\n"
    "ds_read_b64_tr_b16 a[198:199], v8 offset:26880\n"
    "v_accvgpr_read_b32 v152, a100\n"
    "v_accvgpr_read_b32 v153, a101\n"
    "v_accvgpr_read_b32 v154, a102\n"
    "v_accvgpr_read_b32 v155, a103\n"
    "v_accvgpr_read_b32 v156, a116\n"
    "v_accvgpr_read_b32 v157, a117\n"
    "v_accvgpr_read_b32 v158, a118\n"
    "v_accvgpr_read_b32 v159, a119\n"
    "v_mul_f32 v152, v152, v25\n"
    "v_mul_f32 v153, v153, v25\n"
    "v_mul_f32 v154, v154, v25\n"
    "v_mul_f32 v155, v155, v25\n"
    "v_mul_f32 v156, v156, v25\n"
    "v_mul_f32 v157, v157, v25\n"
    "v_mul_f32 v158, v158, v25\n"
    "v_mul_f32 v159, v159, v25\n"
    "v_cvt_pk_bf16_f32 v220, v152, v153\n"
    "v_cvt_pk_bf16_f32 v221, v154, v155\n"
    "v_cvt_pk_bf16_f32 v222, v156, v157\n"
    "v_cvt_pk_bf16_f32 v223, v158, v159\n"
    "v_mfma_f32_16x16x32_bf16 v[64:67], a[208:211], v[216:219], v[64:67]\n"
    "ds_read_b64_tr_b16 a[200:201], v8 offset:25088\n"
    "v_accvgpr_read_b32 v128, a8\n"
    "v_mfma_f32_16x16x32_bf16 v[68:71], a[208:211], v[220:223], v[68:71]\n"
    "ds_read_b64_tr_b16 a[202:203], v8 offset:27136\n"
    "v_accvgpr_read_b32 v129, a9\n"
    "v_mfma_f32_16x16x32_bf16 v[72:75], a[212:215], v[216:219], v[72:75]\n"
    "ds_read_b64_tr_b16 a[204:205], v8 offset:25344\n"
    "v_accvgpr_read_b32 v130, a10\n"
    "v_mfma_f32_16x16x32_bf16 v[76:79], a[212:215], v[220:223], v[76:79]\n"
    "ds_read_b64_tr_b16 a[206:207], v8 offset:27392\n"
    "v_accvgpr_read_b32 v131, a11\n"
    "v_mfma_f32_16x16x32_bf16 v[80:83], a[216:219], v[216:219], v[80:83]\n"
    "ds_read_b64_tr_b16 a[208:209], v8 offset:28672\n"
    "v_accvgpr_read_b32 v132, a24\n"
    "v_mfma_f32_16x16x32_bf16 v[84:87], a[216:219], v[220:223], v[84:87]\n"
    "ds_read_b64_tr_b16 a[210:211], v8 offset:30720\n"
    "v_accvgpr_read_b32 v133, a25\n"
    "v_mfma_f32_16x16x32_bf16 v[88:91], a[220:223], v[216:219], v[88:91]\n"
    "ds_read_b64_tr_b16 a[212:213], v8 offset:28928\n"
    "v_accvgpr_read_b32 v134, a26\n"
    "v_accvgpr_read_b32 v135, a27\n"
    "v_mul_f32 v128, v128, v26\n"
    "v_mul_f32 v129, v129, v26\n"
    "v_mul_f32 v130, v130, v26\n"
    "v_mul_f32 v131, v131, v26\n"
    "v_mul_f32 v132, v132, v26\n"
    "v_mul_f32 v133, v133, v26\n"
    "v_mul_f32 v134, v134, v26\n"
    "v_mul_f32 v135, v135, v26\n"
    "v_cvt_pk_bf16_f32 v224, v128, v129\n"
    "v_cvt_pk_bf16_f32 v225, v130, v131\n"
    "v_cvt_pk_bf16_f32 v226, v132, v133\n"
    "v_cvt_pk_bf16_f32 v227, v134, v135\n"
    "v_mfma_f32_16x16x32_bf16 v[92:95], a[220:223], v[220:223], v[92:95]\n"
    "ds_read_b64_tr_b16 a[214:215], v8 offset:30976\n"
    "v_accvgpr_read_b32 v136, a12\n"
    "v_accvgpr_read_b32 v137, a13\n"
    "v_accvgpr_read_b32 v138, a14\n"
    "v_accvgpr_read_b32 v139, a15\n"
    "v_accvgpr_read_b32 v140, a28\n"
    "v_accvgpr_read_b32 v141, a29\n"
    "v_accvgpr_read_b32 v142, a30\n"
    "v_accvgpr_read_b32 v143, a31\n"
    "v_mul_f32 v136, v136, v27\n"
    "v_mul_f32 v137, v137, v27\n"
    "v_mul_f32 v138, v138, v27\n"
    "v_mul_f32 v139, v139, v27\n"
    "v_mul_f32 v140, v140, v27\n"
    "v_mul_f32 v141, v141, v27\n"
    "v_mul_f32 v142, v142, v27\n"
    "v_mul_f32 v143, v143, v27\n"
    "v_cvt_pk_bf16_f32 v228, v136, v137\n"
    "v_cvt_pk_bf16_f32 v229, v138, v139\n"
    "v_cvt_pk_bf16_f32 v230, v140, v141\n"
    "v_cvt_pk_bf16_f32 v231, v142, v143\n"
    "s_waitcnt lgkmcnt(1)\n"
    "v_mfma_f32_16x16x32_bf16 v[64:67], a[128:131], v[224:227], v[64:67]\n"
    "s_waitcnt vmcnt(16)\n"
    "s_barrier\n"
    "ds_read_b64_tr_b16 a[216:217], v8 offset:29184\n"
    "v_accvgpr_read_b32 v144, a40\n"
    "v_mfma_f32_16x16x32_bf16 v[68:71], a[128:131], v[228:231], v[68:71]\n"
    "ds_read_b64_tr_b16 a[218:219], v8 offset:31232\n"
    "v_accvgpr_read_b32 v145, a41\n"
    "v_mfma_f32_16x16x32_bf16 v[72:75], a[132:135], v[224:227], v[72:75]\n"
    "ds_read_b64_tr_b16 a[220:221], v8 offset:29440\n"
    "v_accvgpr_read_b32 v146, a42\n"
    "v_mfma_f32_16x16x32_bf16 v[76:79], a[132:135], v[228:231], v[76:79]\n"
    "ds_read_b64_tr_b16 a[222:223], v8 offset:31488\n"
    "v_accvgpr_read_b32 v147, a43\n"
    "v_mfma_f32_16x16x32_bf16 v[80:83], a[136:139], v[224:227], v[80:83]\n"
    "ds_read_b64_tr_b16 a[128:129], v9 offset:0\n"
    "s_add_u32 m0, s73, 0x18000\n"
    "s_add_u32 s68, s72, 0xe00\n"
    "buffer_load_dwordx4 v57, s[60:63], s68 offen offset:0 lds\n"
    "v_accvgpr_read_b32 v148, a56\n"
    "v_mfma_f32_16x16x32_bf16 v[84:87], a[136:139], v[228:231], v[84:87]\n"
    "ds_read_b64_tr_b16 a[130:131], v9 offset:2048\n"
    "v_accvgpr_read_b32 v149, a57\n"
    "v_mfma_f32_16x16x32_bf16 v[88:91], a[140:143], v[224:227], v[88:91]\n"
    "ds_read_b64_tr_b16 a[132:133], v9 offset:256\n"
    "v_accvgpr_read_b32 v150, a58\n"
    "v_accvgpr_read_b32 v151, a59\n"
    "v_mul_f32 v144, v144, v26\n"
    "v_mul_f32 v145, v145, v26\n"
    "v_mul_f32 v146, v146, v26\n"
    "v_mul_f32 v147, v147, v26\n"
    "v_mul_f32 v148, v148, v26\n"
    "v_mul_f32 v149, v149, v26\n"
    "v_mul_f32 v150, v150, v26\n"
    "v_mul_f32 v151, v151, v26\n"
    "v_cvt_pk_bf16_f32 v232, v144, v145\n"
    "v_cvt_pk_bf16_f32 v233, v146, v147\n"
    "v_cvt_pk_bf16_f32 v234, v148, v149\n"
    "v_cvt_pk_bf16_f32 v235, v150, v151\n"
    "v_mfma_f32_16x16x32_bf16 v[92:95], a[140:143], v[228:231], v[92:95]\n"
    "ds_read_b64_tr_b16 a[134:135], v9 offset:2304\n"
    "s_add_u32 s68, s72, 0x2e00\n"
    "buffer_load_dwordx4 v57, s[60:63], s68 offen offset:1024 lds\n"
    "v_accvgpr_read_b32 v152, a44\n"
    "v_accvgpr_read_b32 v153, a45\n"
    "v_accvgpr_read_b32 v154, a46\n"
    "v_accvgpr_read_b32 v155, a47\n"
    "v_accvgpr_read_b32 v156, a60\n"
    "v_accvgpr_read_b32 v157, a61\n"
    "v_accvgpr_read_b32 v158, a62\n"
    "v_accvgpr_read_b32 v159, a63\n"
    "v_mul_f32 v152, v152, v27\n"
    "v_mul_f32 v153, v153, v27\n"
    "v_mul_f32 v154, v154, v27\n"
    "v_mul_f32 v155, v155, v27\n"
    "v_mul_f32 v156, v156, v27\n"
    "v_mul_f32 v157, v157, v27\n"
    "v_mul_f32 v158, v158, v27\n"
    "v_mul_f32 v159, v159, v27\n"
    "v_cvt_pk_bf16_f32 v236, v152, v153\n"
    "v_cvt_pk_bf16_f32 v237, v154, v155\n"
    "v_cvt_pk_bf16_f32 v238, v156, v157\n"
    "v_cvt_pk_bf16_f32 v239, v158, v159\n"
    "v_mfma_f32_16x16x32_bf16 v[64:67], a[144:147], v[232:235], v[64:67]\n"
    "ds_read_b64_tr_b16 a[136:137], v9 offset:512\n"
    "v_accvgpr_read_b32 v128, a72\n"
    "v_mfma_f32_16x16x32_bf16 v[68:71], a[144:147], v[236:239], v[68:71]\n"
    "ds_read_b64_tr_b16 a[138:139], v9 offset:2560\n"
    "v_accvgpr_read_b32 v129, a73\n"
    "v_mfma_f32_16x16x32_bf16 v[72:75], a[148:151], v[232:235], v[72:75]\n"
    "ds_read_b64_tr_b16 a[140:141], v9 offset:768\n"
    "s_add_u32 s68, s72, 0x4e00\n"
    "buffer_load_dwordx4 v57, s[60:63], s68 offen offset:2048 lds\n"
    "v_accvgpr_read_b32 v130, a74\n"
    "v_mfma_f32_16x16x32_bf16 v[76:79], a[148:151], v[236:239], v[76:79]\n"
    "ds_read_b64_tr_b16 a[142:143], v9 offset:2816\n"
    "v_accvgpr_read_b32 v131, a75\n"
    "v_mfma_f32_16x16x32_bf16 v[80:83], a[152:155], v[232:235], v[80:83]\n"
    "ds_read_b64_tr_b16 a[144:145], v9 offset:4096\n"
    "v_accvgpr_read_b32 v132, a88\n"
    "v_mfma_f32_16x16x32_bf16 v[84:87], a[152:155], v[236:239], v[84:87]\n"
    "ds_read_b64_tr_b16 a[146:147], v9 offset:6144\n"
    "s_add_u32 s68, s72, 0x6e00\n"
    "buffer_load_dwordx4 v57, s[60:63], s68 offen offset:3072 lds\n"
    "v_accvgpr_read_b32 v133, a89\n"
    "v_mfma_f32_16x16x32_bf16 v[88:91], a[156:159], v[232:235], v[88:91]\n"
    "ds_read_b64_tr_b16 a[148:149], v9 offset:4352\n"
    "v_accvgpr_read_b32 v134, a90\n"
    "v_accvgpr_read_b32 v135, a91\n"
    "v_mul_f32 v128, v128, v26\n"
    "v_mul_f32 v129, v129, v26\n"
    "v_mul_f32 v130, v130, v26\n"
    "v_mul_f32 v131, v131, v26\n"
    "v_mul_f32 v132, v132, v26\n"
    "v_mul_f32 v133, v133, v26\n"
    "v_mul_f32 v134, v134, v26\n"
    "v_mul_f32 v135, v135, v26\n"
    "v_cvt_pk_bf16_f32 v240, v128, v129\n"
    "v_cvt_pk_bf16_f32 v241, v130, v131\n"
    "v_cvt_pk_bf16_f32 v242, v132, v133\n"
    "v_cvt_pk_bf16_f32 v243, v134, v135\n"
    "v_mfma_f32_16x16x32_bf16 v[92:95], a[156:159], v[236:239], v[92:95]\n"
    "ds_read_b64_tr_b16 a[150:151], v9 offset:6400\n"
    "v_accvgpr_read_b32 v136, a76\n"
    "v_accvgpr_read_b32 v137, a77\n"
    "v_accvgpr_read_b32 v138, a78\n"
    "v_accvgpr_read_b32 v139, a79\n"
    "v_accvgpr_read_b32 v140, a92\n"
    "v_accvgpr_read_b32 v141, a93\n"
    "v_accvgpr_read_b32 v142, a94\n"
    "v_accvgpr_read_b32 v143, a95\n"
    "v_mul_f32 v136, v136, v27\n"
    "v_mul_f32 v137, v137, v27\n"
    "v_mul_f32 v138, v138, v27\n"
    "v_mul_f32 v139, v139, v27\n"
    "v_mul_f32 v140, v140, v27\n"
    "v_mul_f32 v141, v141, v27\n"
    "v_mul_f32 v142, v142, v27\n"
    "v_mul_f32 v143, v143, v27\n"
    "v_cvt_pk_bf16_f32 v244, v136, v137\n"
    "v_cvt_pk_bf16_f32 v245, v138, v139\n"
    "v_cvt_pk_bf16_f32 v246, v140, v141\n"
    "v_cvt_pk_bf16_f32 v247, v142, v143\n"
    "v_mfma_f32_16x16x32_bf16 v[64:67], a[192:195], v[240:243], v[64:67]\n"
    "ds_read_b64_tr_b16 a[152:153], v9 offset:4608\n"
    "s_add_u32 m0, s73, 0x19000\n"
    "s_add_u32 s68, s72, 0x9e00\n"
    "buffer_load_dwordx4 v57, s[60:63], s68 offen offset:0 lds\n"
    "v_accvgpr_read_b32 v144, a104\n"
    "v_mfma_f32_16x16x32_bf16 v[68:71], a[192:195], v[244:247], v[68:71]\n"
    "ds_read_b64_tr_b16 a[154:155], v9 offset:6656\n"
    "v_accvgpr_read_b32 v145, a105\n"
    "v_mfma_f32_16x16x32_bf16 v[72:75], a[196:199], v[240:243], v[72:75]\n"
    "ds_read_b64_tr_b16 a[156:157], v9 offset:4864\n"
    "v_accvgpr_read_b32 v146, a106\n"
    "v_mfma_f32_16x16x32_bf16 v[76:79], a[196:199], v[244:247], v[76:79]\n"
    "ds_read_b64_tr_b16 a[158:159], v9 offset:6912\n"
    "s_add_u32 s68, s72, 0xbe00\n"
    "buffer_load_dwordx4 v57, s[60:63], s68 offen offset:1024 lds\n"
    "v_accvgpr_read_b32 v147, a107\n"
    "v_mfma_f32_16x16x32_bf16 v[80:83], a[200:203], v[240:243], v[80:83]\n"
    "ds_read_b64_tr_b16 a[192:193], v9 offset:8192\n"
    "v_accvgpr_read_b32 v148, a120\n"
    "v_mfma_f32_16x16x32_bf16 v[84:87], a[200:203], v[244:247], v[84:87]\n"
    "ds_read_b64_tr_b16 a[194:195], v9 offset:10240\n"
    "v_accvgpr_read_b32 v149, a121\n"
    "v_mfma_f32_16x16x32_bf16 v[88:91], a[204:207], v[240:243], v[88:91]\n"
    "ds_read_b64_tr_b16 a[196:197], v9 offset:8448\n"
    "s_add_u32 s68, s72, 0xde00\n"
    "buffer_load_dwordx4 v57, s[60:63], s68 offen offset:2048 lds\n"
    "v_accvgpr_read_b32 v150, a122\n"
    "v_accvgpr_read_b32 v151, a123\n"
    "v_mul_f32 v144, v144, v26\n"
    "v_mul_f32 v145, v145, v26\n"
    "v_mul_f32 v146, v146, v26\n"
    "v_mul_f32 v147, v147, v26\n"
    "v_mul_f32 v148, v148, v26\n"
    "v_mul_f32 v149, v149, v26\n"
    "v_mul_f32 v150, v150, v26\n"
    "v_mul_f32 v151, v151, v26\n"
    "v_cvt_pk_bf16_f32 v248, v144, v145\n"
    "v_cvt_pk_bf16_f32 v249, v146, v147\n"
    "v_cvt_pk_bf16_f32 v250, v148, v149\n"
    "v_cvt_pk_bf16_f32 v251, v150, v151\n"
    "v_mfma_f32_16x16x32_bf16 v[92:95], a[204:207], v[244:247], v[92:95]\n"
    "ds_read_b64_tr_b16 a[198:199], v9 offset:10496\n"
    "v_accvgpr_read_b32 v152, a108\n"
    "v_accvgpr_read_b32 v153, a109\n"
    "v_accvgpr_read_b32 v154, a110\n"
    "v_accvgpr_read_b32 v155, a111\n"
    "v_accvgpr_read_b32 v156, a124\n"
    "v_accvgpr_read_b32 v157, a125\n"
    "v_accvgpr_read_b32 v158, a126\n"
    "v_accvgpr_read_b32 v159, a127\n"
    "v_mul_f32 v152, v152, v27\n"
    "v_mul_f32 v153, v153, v27\n"
    "v_mul_f32 v154, v154, v27\n"
    "v_mul_f32 v155, v155, v27\n"
    "v_mul_f32 v156, v156, v27\n"
    "v_mul_f32 v157, v157, v27\n"
    "v_mul_f32 v158, v158, v27\n"
    "v_mul_f32 v159, v159, v27\n"
    "v_cvt_pk_bf16_f32 v252, v152, v153\n"
    "v_cvt_pk_bf16_f32 v253, v154, v155\n"
    "v_cvt_pk_bf16_f32 v254, v156, v157\n"
    "v_cvt_pk_bf16_f32 v255, v158, v159\n"
    "v_mfma_f32_16x16x32_bf16 v[64:67], a[208:211], v[248:251], v[64:67]\n"
    "ds_read_b64_tr_b16 a[200:201], v9 offset:8704\n"
    "v_mfma_f32_16x16x32_bf16 v[68:71], a[208:211], v[252:255], v[68:71]\n"
    "ds_read_b64_tr_b16 a[202:203], v9 offset:10752\n"
    "s_add_u32 s68, s72, 0xfe00\n"
    "buffer_load_dwordx4 v57, s[60:63], s68 offen offset:3072 lds\n"
    "s_waitcnt lgkmcnt(3)\n"
    "v_mfma_f32_16x16x32_bf16 v[72:75], a[212:215], v[248:251], v[72:75]\n"
    "ds_read_b64_tr_b16 a[204:205], v9 offset:8960\n"
    "v_mfma_f32_16x16x32_bf16 v[76:79], a[212:215], v[252:255], v[76:79]\n"
    "ds_read_b64_tr_b16 a[206:207], v9 offset:11008\n"
    "v_mfma_f32_16x16x32_bf16 v[80:83], a[216:219], v[248:251], v[80:83]\n"
    "ds_read_b64_tr_b16 a[208:209], v9 offset:12288\n"
    "v_mfma_f32_16x16x32_bf16 v[84:87], a[216:219], v[252:255], v[84:87]\n"
    "ds_read_b64_tr_b16 a[210:211], v9 offset:14336\n"
    "v_mfma_f32_16x16x32_bf16 v[88:91], a[220:223], v[248:251], v[88:91]\n"
    "ds_read_b64_tr_b16 a[212:213], v9 offset:12544\n"
    "v_mfma_f32_16x16x32_bf16 v[92:95], a[220:223], v[252:255], v[92:95]\n"
    "ds_read_b64_tr_b16 a[216:217], v9 offset:12800\n"
    "ds_read_b64_tr_b16 a[220:221], v9 offset:13056\n"
    "ds_read_b64_tr_b16 a[214:215], v9 offset:14592\n"
    "ds_read_b64_tr_b16 a[218:219], v9 offset:14848\n"
    "ds_read_b64_tr_b16 a[222:223], v9 offset:15104\n"
    "v_mfma_f32_16x16x32_bf16 v[96:99], a[128:131], v[192:195], 0\n"
    "v_mfma_f32_16x16x32_bf16 v[100:103], a[128:131], v[196:199], 0\n"
    "ds_read_b64_tr_b16 a[128:129], v9 offset:16384\n"
    "v_mfma_f32_16x16x32_bf16 v[104:107], a[132:135], v[192:195], 0\n"
    "ds_read_b64_tr_b16 a[130:131], v9 offset:18432\n"
    "v_mfma_f32_16x16x32_bf16 v[108:111], a[132:135], v[196:199], 0\n"
    "ds_read_b64_tr_b16 a[132:133], v9 offset:16640\n"
    "v_mfma_f32_16x16x32_bf16 v[112:115], a[136:139], v[192:195], 0\n"
    "ds_read_b64_tr_b16 a[134:135], v9 offset:18688\n"
    "v_cvt_pk_bf16_f32 v128, v64, v65\n"
    "v_mfma_f32_16x16x32_bf16 v[116:119], a[136:139], v[196:199], 0\n"
    "ds_read_b64_tr_b16 a[136:137], v9 offset:16896\n"
    "v_cvt_pk_bf16_f32 v129, v66, v67\n"
    "v_mfma_f32_16x16x32_bf16 v[120:123], a[140:143], v[192:195], 0\n"
    "ds_read_b64_tr_b16 a[138:139], v9 offset:18944\n"
    "v_cvt_pk_bf16_f32 v130, v72, v73\n"
    "v_mfma_f32_16x16x32_bf16 v[124:127], a[140:143], v[196:199], 0\n"
    "ds_read_b64_tr_b16 a[140:141], v9 offset:17152\n"
    "v_cvt_pk_bf16_f32 v131, v74, v75\n"
    "v_mfma_f32_16x16x32_bf16 v[96:99], a[144:147], v[200:203], v[96:99]\n"
    "ds_read_b64_tr_b16 a[142:143], v9 offset:19200\n"
    "v_mfma_f32_16x16x32_bf16 v[100:103], a[144:147], v[204:207], v[100:103]\n"
    "ds_read_b64_tr_b16 a[144:145], v9 offset:20480\n"
    "v_mfma_f32_16x16x32_bf16 v[104:107], a[148:151], v[200:203], v[104:107]\n"
    "ds_read_b64_tr_b16 a[146:147], v9 offset:22528\n"
    "v_cvt_pk_bf16_f32 v132, v80, v81\n"
    "v_mfma_f32_16x16x32_bf16 v[108:111], a[148:151], v[204:207], v[108:111]\n"
    "ds_read_b64_tr_b16 a[148:149], v9 offset:20736\n"
    "v_cvt_pk_bf16_f32 v133, v82, v83\n"
    "v_mfma_f32_16x16x32_bf16 v[112:115], a[152:155], v[200:203], v[112:115]\n"
    "ds_read_b64_tr_b16 a[150:151], v9 offset:22784\n"
    "v_cvt_pk_bf16_f32 v134, v88, v89\n"
    "v_mfma_f32_16x16x32_bf16 v[116:119], a[152:155], v[204:207], v[116:119]\n"
    "ds_read_b64_tr_b16 a[152:153], v9 offset:20992\n"
    "v_cvt_pk_bf16_f32 v135, v90, v91\n"
    "v_mfma_f32_16x16x32_bf16 v[120:123], a[156:159], v[200:203], v[120:123]\n"
    "ds_read_b64_tr_b16 a[154:155], v9 offset:23040\n"
    "v_mfma_f32_16x16x32_bf16 v[124:127], a[156:159], v[204:207], v[124:127]\n"
    "ds_read_b64_tr_b16 a[156:157], v9 offset:21248\n"
    "v_mfma_f32_16x16x32_bf16 v[96:99], a[192:195], v[208:211], v[96:99]\n"
    "ds_read_b64_tr_b16 a[158:159], v9 offset:23296\n"
    "v_cvt_pk_bf16_f32 v136, v68, v69\n"
    "v_mfma_f32_16x16x32_bf16 v[100:103], a[192:195], v[212:215], v[100:103]\n"
    "ds_read_b64_tr_b16 a[192:193], v9 offset:24576\n"
    "v_cvt_pk_bf16_f32 v137, v70, v71\n"
    "s_waitcnt lgkmcnt(13)\n"
    "v_mfma_f32_16x16x32_bf16 v[104:107], a[196:199], v[208:211], v[104:107]\n"
    "buffer_store_dwordx4 v[128:131], v60, s[64:67], s71 offen offset:0 sc1\n"
    "v_cvt_pk_bf16_f32 v138, v76, v77\n"
    "v_mfma_f32_16x16x32_bf16 v[108:111], a[196:199], v[212:215], v[108:111]\n"
    "ds_read_b64_tr_b16 a[194:195], v9 offset:26624\n"
    "v_cvt_pk_bf16_f32 v139, v78, v79\n"
    "v_mfma_f32_16x16x32_bf16 v[112:115], a[200:203], v[208:211], v[112:115]\n"
    "ds_read_b64_tr_b16 a[196:197], v9 offset:24832\n"
    "v_mfma_f32_16x16x32_bf16 v[116:119], a[200:203], v[212:215], v[116:119]\n"
    "ds_read_b64_tr_b16 a[198:199], v9 offset:26880\n"
    "v_mfma_f32_16x16x32_bf16 v[120:123], a[204:207], v[208:211], v[120:123]\n"
    "ds_read_b64_tr_b16 a[200:201], v9 offset:25088\n"
    "v_cvt_pk_bf16_f32 v140, v84, v85\n"
    "v_mfma_f32_16x16x32_bf16 v[124:127], a[204:207], v[212:215], v[124:127]\n"
    "ds_read_b64_tr_b16 a[202:203], v9 offset:27136\n"
    "v_cvt_pk_bf16_f32 v141, v86, v87\n"
    "v_mfma_f32_16x16x32_bf16 v[96:99], a[208:211], v[216:219], v[96:99]\n"
    "ds_read_b64_tr_b16 a[204:205], v9 offset:25344\n"
    "v_cvt_pk_bf16_f32 v142, v92, v93\n"
    "v_mfma_f32_16x16x32_bf16 v[100:103], a[208:211], v[220:223], v[100:103]\n"
    "ds_read_b64_tr_b16 a[206:207], v9 offset:27392\n"
    "v_cvt_pk_bf16_f32 v143, v94, v95\n"
    "v_mfma_f32_16x16x32_bf16 v[104:107], a[212:215], v[216:219], v[104:107]\n"
    "ds_read_b64_tr_b16 a[208:209], v9 offset:28672\n"
    "v_mfma_f32_16x16x32_bf16 v[108:111], a[212:215], v[220:223], v[108:111]\n"
    "ds_read_b64_tr_b16 a[210:211], v9 offset:30720\n"
    "v_mfma_f32_16x16x32_bf16 v[112:115], a[216:219], v[216:219], v[112:115]\n"
    "ds_read_b64_tr_b16 a[212:213], v9 offset:28928\n"
    "v_mfma_f32_16x16x32_bf16 v[116:119], a[216:219], v[220:223], v[116:119]\n"
    "buffer_store_dwordx4 v[132:135], v60, s[64:67], s71 offen offset:512 sc1\n"
    "v_mfma_f32_16x16x32_bf16 v[120:123], a[220:223], v[216:219], v[120:123]\n"
    "ds_read_b64_tr_b16 a[214:215], v9 offset:30976\n"
    "v_mfma_f32_16x16x32_bf16 v[124:127], a[220:223], v[220:223], v[124:127]\n"
    "ds_read_b64_tr_b16 a[216:217], v9 offset:29184\n"
    "v_mfma_f32_16x16x32_bf16 v[96:99], a[128:131], v[224:227], v[96:99]\n"
    "s_waitcnt vmcnt(18)\n"
    "s_barrier\n"
    "ds_read_b64_tr_b16 a[218:219], v9 offset:31232\n"
    "v_mfma_f32_16x16x32_bf16 v[100:103], a[128:131], v[228:231], v[100:103]\n"
    "ds_read_b64_tr_b16 a[220:221], v9 offset:29440\n"
    "v_mfma_f32_16x16x32_bf16 v[104:107], a[132:135], v[224:227], v[104:107]\n"
    "ds_read_b64_tr_b16 a[222:223], v9 offset:31488\n"
    "v_mfma_f32_16x16x32_bf16 v[108:111], a[132:135], v[228:231], v[108:111]\n"
    "ds_read_b64_tr_b16 a[128:129], v10 offset:0\n"
    "s_waitcnt lgkmcnt(14)\n"
    "v_mfma_f32_16x16x32_bf16 v[112:115], a[136:139], v[224:227], v[112:115]\n"
    "s_add_u32 m0, s73, 0x20000\n"
    "s_add_u32 s68, s72, 0xe80\n"
    "buffer_load_dwordx4 v57, s[60:63], s68 offen offset:0 lds\n"
    "v_mfma_f32_16x16x32_bf16 v[116:119], a[136:139], v[228:231], v[116:119]\n"
    "ds_read_b64_tr_b16 a[130:131], v10 offset:2048\n"
    "v_mfma_f32_16x16x32_bf16 v[120:123], a[140:143], v[224:227], v[120:123]\n"
    "ds_read_b64_tr_b16 a[132:133], v10 offset:256\n"
    "v_mfma_f32_16x16x32_bf16 v[124:127], a[140:143], v[228:231], v[124:127]\n"
    "ds_read_b64_tr_b16 a[134:135], v10 offset:2304\n"
    "s_add_u32 s68, s72, 0x2e80\n"
    "buffer_load_dwordx4 v57, s[60:63], s68 offen offset:1024 lds\n"
    "v_mfma_f32_16x16x32_bf16 v[96:99], a[144:147], v[232:235], v[96:99]\n"
    "ds_read_b64_tr_b16 a[136:137], v10 offset:512\n"
    "buffer_store_dwordx4 v[136:139], v61, s[64:67], s71 offen offset:0 sc1\n"
    "v_mfma_f32_16x16x32_bf16 v[100:103], a[144:147], v[236:239], v[100:103]\n"
    "ds_read_b64_tr_b16 a[138:139], v10 offset:2560\n"
    "v_mfma_f32_16x16x32_bf16 v[104:107], a[148:151], v[232:235], v[104:107]\n"
    "ds_read_b64_tr_b16 a[140:141], v10 offset:768\n"
    "s_add_u32 s68, s72, 0x4e80\n"
    "buffer_load_dwordx4 v57, s[60:63], s68 offen offset:2048 lds\n"
    "v_mfma_f32_16x16x32_bf16 v[108:111], a[148:151], v[236:239], v[108:111]\n"
    "ds_read_b64_tr_b16 a[142:143], v10 offset:2816\n"
    "v_mfma_f32_16x16x32_bf16 v[112:115], a[152:155], v[232:235], v[112:115]\n"
    "ds_read_b64_tr_b16 a[144:145], v10 offset:4096\n"
    "v_mfma_f32_16x16x32_bf16 v[116:119], a[152:155], v[236:239], v[116:119]\n"
    "ds_read_b64_tr_b16 a[146:147], v10 offset:6144\n"
    "s_add_u32 s68, s72, 0x6e80\n"
    "buffer_load_dwordx4 v57, s[60:63], s68 offen offset:3072 lds\n"
    "v_mfma_f32_16x16x32_bf16 v[120:123], a[156:159], v[232:235], v[120:123]\n"
    "ds_read_b64_tr_b16 a[148:149], v10 offset:4352\n"
    "v_mfma_f32_16x16x32_bf16 v[124:127], a[156:159], v[236:239], v[124:127]\n"
    "ds_read_b64_tr_b16 a[150:151], v10 offset:6400\n"
    "v_mfma_f32_16x16x32_bf16 v[96:99], a[192:195], v[240:243], v[96:99]\n"
    "ds_read_b64_tr_b16 a[152:153], v10 offset:4608\n"
    "s_add_u32 m0, s73, 0x21000\n"
    "s_add_u32 s68, s72, 0x9e80\n"
    "buffer_load_dwordx4 v57, s[60:63], s68 offen offset:0 lds\n"
    "v_mfma_f32_16x16x32_bf16 v[100:103], a[192:195], v[244:247], v[100:103]\n"
    "ds_read_b64_tr_b16 a[154:155], v10 offset:6656\n"
    "s_waitcnt lgkmcnt(13)\n"
    "v_mfma_f32_16x16x32_bf16 v[104:107], a[196:199], v[240:243], v[104:107]\n"
    "ds_read_b64_tr_b16 a[156:157], v10 offset:4864\n"
    "buffer_store_dwordx4 v[140:143], v61, s[64:67], s71 offen offset:512 sc1\n"
    "v_mfma_f32_16x16x32_bf16 v[108:111], a[196:199], v[244:247], v[108:111]\n"
    "s_add_u32 s71, s71, 0x400\n"
    "ds_read_b64_tr_b16 a[158:159], v10 offset:6912\n"
    "s_add_u32 s68, s72, 0xbe80\n"
    "buffer_load_dwordx4 v57, s[60:63], s68 offen offset:1024 lds\n"
    "v_mfma_f32_16x16x32_bf16 v[112:115], a[200:203], v[240:243], v[112:115]\n"
    "ds_read_b64_tr_b16 a[192:193], v10 offset:8192\n"
    "v_mfma_f32_16x16x32_bf16 v[116:119], a[200:203], v[244:247], v[116:119]\n"
    "ds_read_b64_tr_b16 a[194:195], v10 offset:10240\n"
    "v_mfma_f32_16x16x32_bf16 v[120:123], a[204:207], v[240:243], v[120:123]\n"
    "ds_read_b64_tr_b16 a[196:197], v10 offset:8448\n"
    "s_add_u32 s68, s72, 0xde80\n"
    "buffer_load_dwordx4 v57, s[60:63], s68 offen offset:2048 lds\n"
    "v_mfma_f32_16x16x32_bf16 v[124:127], a[204:207], v[244:247], v[124:127]\n"
    "ds_read_b64_tr_b16 a[198:199], v10 offset:10496\n"
    "v_mfma_f32_16x16x32_bf16 v[96:99], a[208:211], v[248:251], v[96:99]\n"
    "ds_read_b64_tr_b16 a[200:201], v10 offset:8704\n"
    "v_mfma_f32_16x16x32_bf16 v[100:103], a[208:211], v[252:255], v[100:103]\n"
    "ds_read_b64_tr_b16 a[202:203], v10 offset:10752\n"
    "s_add_u32 s68, s72, 0xfe80\n"
    "buffer_load_dwordx4 v57, s[60:63], s68 offen offset:3072 lds\n"
    "v_mfma_f32_16x16x32_bf16 v[104:107], a[212:215], v[248:251], v[104:107]\n"
    "ds_read_b64_tr_b16 a[204:205], v10 offset:8960\n"
    "v_mfma_f32_16x16x32_bf16 v[108:111], a[212:215], v[252:255], v[108:111]\n"
    "ds_read_b64_tr_b16 a[206:207], v10 offset:11008\n"
    "v_mfma_f32_16x16x32_bf16 v[112:115], a[216:219], v[248:251], v[112:115]\n"
    "ds_read_b64_tr_b16 a[208:209], v10 offset:12288\n"
    "v_mfma_f32_16x16x32_bf16 v[116:119], a[216:219], v[252:255], v[116:119]\n"
    "ds_read_b64_tr_b16 a[210:211], v10 offset:14336\n"
    "v_mfma_f32_16x16x32_bf16 v[120:123], a[220:223], v[248:251], v[120:123]\n"
    "ds_read_b64_tr_b16 a[212:213], v10 offset:12544\n"
    "v_mfma_f32_16x16x32_bf16 v[124:127], a[220:223], v[252:255], v[124:127]\n"
    "ds_read_b64_tr_b16 a[216:217], v10 offset:12800\n"
    "ds_read_b64_tr_b16 a[220:221], v10 offset:13056\n"
    "ds_read_b64_tr_b16 a[214:215], v10 offset:14592\n"
    "ds_read_b64_tr_b16 a[218:219], v10 offset:14848\n"
    "ds_read_b64_tr_b16 a[222:223], v10 offset:15104\n"
    "s_waitcnt lgkmcnt(14)\n"
    "v_mfma_f32_16x16x32_bf16 v[64:67], a[128:131], v[192:195], 0\n"
    "v_mfma_f32_16x16x32_bf16 v[68:71], a[128:131], v[196:199], 0\n"
    "ds_read_b64_tr_b16 a[128:129], v10 offset:16384\n"
    "v_mfma_f32_16x16x32_bf16 v[72:75], a[132:135], v[192:195], 0\n"
    "ds_read_b64_tr_b16 a[130:131], v10 offset:18432\n"
    "v_mfma_f32_16x16x32_bf16 v[76:79], a[132:135], v[196:199], 0\n"
    "ds_read_b64_tr_b16 a[132:133], v10 offset:16640\n"
    "v_mfma_f32_16x16x32_bf16 v[80:83], a[136:139], v[192:195], 0\n"
    "ds_read_b64_tr_b16 a[134:135], v10 offset:18688\n"
    "v_cvt_pk_bf16_f32 v128, v96, v97\n"
    "v_mfma_f32_16x16x32_bf16 v[84:87], a[136:139], v[196:199], 0\n"
    "ds_read_b64_tr_b16 a[136:137], v10 offset:16896\n"
    "v_cvt_pk_bf16_f32 v129, v98, v99\n"
    "v_mfma_f32_16x16x32_bf16 v[88:91], a[140:143], v[192:195], 0\n"
    "ds_read_b64_tr_b16 a[138:139], v10 offset:18944\n"
    "v_cvt_pk_bf16_f32 v130, v104, v105\n"
    "v_mfma_f32_16x16x32_bf16 v[92:95], a[140:143], v[196:199], 0\n"
    "ds_read_b64_tr_b16 a[140:141], v10 offset:17152\n"
    "v_cvt_pk_bf16_f32 v131, v106, v107\n"
    "v_mfma_f32_16x16x32_bf16 v[64:67], a[144:147], v[200:203], v[64:67]\n"
    "ds_read_b64_tr_b16 a[142:143], v10 offset:19200\n"
    "v_mfma_f32_16x16x32_bf16 v[68:71], a[144:147], v[204:207], v[68:71]\n"
    "ds_read_b64_tr_b16 a[144:145], v10 offset:20480\n"
    "v_mfma_f32_16x16x32_bf16 v[72:75], a[148:151], v[200:203], v[72:75]\n"
    "ds_read_b64_tr_b16 a[146:147], v10 offset:22528\n"
    "v_cvt_pk_bf16_f32 v132, v112, v113\n"
    "v_mfma_f32_16x16x32_bf16 v[76:79], a[148:151], v[204:207], v[76:79]\n"
    "ds_read_b64_tr_b16 a[148:149], v10 offset:20736\n"
    "v_cvt_pk_bf16_f32 v133, v114, v115\n"
    "v_mfma_f32_16x16x32_bf16 v[80:83], a[152:155], v[200:203], v[80:83]\n"
    "ds_read_b64_tr_b16 a[150:151], v10 offset:22784\n"
    "v_cvt_pk_bf16_f32 v134, v120, v121\n"
    "v_mfma_f32_16x16x32_bf16 v[84:87], a[152:155], v[204:207], v[84:87]\n"
    "ds_read_b64_tr_b16 a[152:153], v10 offset:20992\n"
    "v_cvt_pk_bf16_f32 v135, v122, v123\n"
    "v_mfma_f32_16x16x32_bf16 v[88:91], a[156:159], v[200:203], v[88:91]\n"
    "ds_read_b64_tr_b16 a[154:155], v10 offset:23040\n"
    "v_mfma_f32_16x16x32_bf16 v[92:95], a[156:159], v[204:207], v[92:95]\n"
    "ds_read_b64_tr_b16 a[156:157], v10 offset:21248\n"
    "v_mfma_f32_16x16x32_bf16 v[64:67], a[192:195], v[208:211], v[64:67]\n"
    "ds_read_b64_tr_b16 a[158:159], v10 offset:23296\n"
    "v_cvt_pk_bf16_f32 v136, v100, v101\n"
    "v_mfma_f32_16x16x32_bf16 v[68:71], a[192:195], v[212:215], v[68:71]\n"
    "ds_read_b64_tr_b16 a[192:193], v10 offset:24576\n"
    "v_cvt_pk_bf16_f32 v137, v102, v103\n"
    "s_waitcnt lgkmcnt(13)\n"
    "v_mfma_f32_16x16x32_bf16 v[72:75], a[196:199], v[208:211], v[72:75]\n"
    "buffer_store_dwordx4 v[128:131], v60, s[64:67], s71 offen offset:0 sc1\n"
    "v_cvt_pk_bf16_f32 v138, v108, v109\n"
    "v_mfma_f32_16x16x32_bf16 v[76:79], a[196:199], v[212:215], v[76:79]\n"
    "ds_read_b64_tr_b16 a[194:195], v10 offset:26624\n"
    "v_cvt_pk_bf16_f32 v139, v110, v111\n"
    "v_mfma_f32_16x16x32_bf16 v[80:83], a[200:203], v[208:211], v[80:83]\n"
    "ds_read_b64_tr_b16 a[196:197], v10 offset:24832\n"
    "v_mfma_f32_16x16x32_bf16 v[84:87], a[200:203], v[212:215], v[84:87]\n"
    "ds_read_b64_tr_b16 a[198:199], v10 offset:26880\n"
    "v_mfma_f32_16x16x32_bf16 v[88:91], a[204:207], v[208:211], v[88:91]\n"
    "ds_read_b64_tr_b16 a[200:201], v10 offset:25088\n"
    "v_cvt_pk_bf16_f32 v140, v116, v117\n"
    "v_mfma_f32_16x16x32_bf16 v[92:95], a[204:207], v[212:215], v[92:95]\n"
    "ds_read_b64_tr_b16 a[202:203], v10 offset:27136\n"
    "v_cvt_pk_bf16_f32 v141, v118, v119\n"
    "v_mfma_f32_16x16x32_bf16 v[64:67], a[208:211], v[216:219], v[64:67]\n"
    "ds_read_b64_tr_b16 a[204:205], v10 offset:25344\n"
    "v_cvt_pk_bf16_f32 v142, v124, v125\n"
    "v_mfma_f32_16x16x32_bf16 v[68:71], a[208:211], v[220:223], v[68:71]\n"
    "ds_read_b64_tr_b16 a[206:207], v10 offset:27392\n"
    "v_cvt_pk_bf16_f32 v143, v126, v127\n"
    "v_mfma_f32_16x16x32_bf16 v[72:75], a[212:215], v[216:219], v[72:75]\n"
    "ds_read_b64_tr_b16 a[208:209], v10 offset:28672\n"
    "v_mfma_f32_16x16x32_bf16 v[76:79], a[212:215], v[220:223], v[76:79]\n"
    "ds_read_b64_tr_b16 a[210:211], v10 offset:30720\n"
    "v_mfma_f32_16x16x32_bf16 v[80:83], a[216:219], v[216:219], v[80:83]\n"
    "ds_read_b64_tr_b16 a[212:213], v10 offset:28928\n"
    "v_mfma_f32_16x16x32_bf16 v[84:87], a[216:219], v[220:223], v[84:87]\n"
    "buffer_store_dwordx4 v[132:135], v60, s[64:67], s71 offen offset:512 sc1\n"
    "v_mfma_f32_16x16x32_bf16 v[88:91], a[220:223], v[216:219], v[88:91]\n"
    "ds_read_b64_tr_b16 a[214:215], v10 offset:30976\n"
    "v_mfma_f32_16x16x32_bf16 v[92:95], a[220:223], v[220:223], v[92:95]\n"
    "ds_read_b64_tr_b16 a[216:217], v10 offset:29184\n"
    "v_mfma_f32_16x16x32_bf16 v[64:67], a[128:131], v[224:227], v[64:67]\n"
    "s_waitcnt vmcnt(22)\n"
    "s_barrier\n"
    "ds_read_b64_tr_b16 a[218:219], v10 offset:31232\n"
    "v_mfma_f32_16x16x32_bf16 v[68:71], a[128:131], v[228:231], v[68:71]\n"
    "ds_read_b64_tr_b16 a[220:221], v10 offset:29440\n"
    "v_mfma_f32_16x16x32_bf16 v[72:75], a[132:135], v[224:227], v[72:75]\n"
    "ds_read_b64_tr_b16 a[222:223], v10 offset:31488\n"
    "v_mfma_f32_16x16x32_bf16 v[76:79], a[132:135], v[228:231], v[76:79]\n"
    "ds_read_b64_tr_b16 a[128:129], v11 offset:0\n"
    "s_waitcnt lgkmcnt(14)\n"
    "v_mfma_f32_16x16x32_bf16 v[80:83], a[136:139], v[224:227], v[80:83]\n"
    "s_add_u32 m0, s73, 0x0\n"
    "s_add_u32 s68, s72, 0xf00\n"
    "buffer_load_dwordx4 v57, s[60:63], s68 offen offset:0 lds\n"
    "v_mfma_f32_16x16x32_bf16 v[84:87], a[136:139], v[228:231], v[84:87]\n"
    "ds_read_b64_tr_b16 a[130:131], v11 offset:2048\n"
    "v_mfma_f32_16x16x32_bf16 v[88:91], a[140:143], v[224:227], v[88:91]\n"
    "ds_read_b64_tr_b16 a[132:133], v11 offset:256\n"
    "v_mfma_f32_16x16x32_bf16 v[92:95], a[140:143], v[228:231], v[92:95]\n"
    "ds_read_b64_tr_b16 a[134:135], v11 offset:2304\n"
    "s_add_u32 s68, s72, 0x2f00\n"
    "buffer_load_dwordx4 v57, s[60:63], s68 offen offset:1024 lds\n"
    "v_mfma_f32_16x16x32_bf16 v[64:67], a[144:147], v[232:235], v[64:67]\n"
    "ds_read_b64_tr_b16 a[136:137], v11 offset:512\n"
    "buffer_store_dwordx4 v[136:139], v61, s[64:67], s71 offen offset:0 sc1\n"
    "v_mfma_f32_16x16x32_bf16 v[68:71], a[144:147], v[236:239], v[68:71]\n"
    "ds_read_b64_tr_b16 a[138:139], v11 offset:2560\n"
    "v_mfma_f32_16x16x32_bf16 v[72:75], a[148:151], v[232:235], v[72:75]\n"
    "ds_read_b64_tr_b16 a[140:141], v11 offset:768\n"
    "s_add_u32 s68, s72, 0x4f00\n"
    "buffer_load_dwordx4 v57, s[60:63], s68 offen offset:2048 lds\n"
    "v_mfma_f32_16x16x32_bf16 v[76:79], a[148:151], v[236:239], v[76:79]\n"
    "ds_read_b64_tr_b16 a[142:143], v11 offset:2816\n"
    "v_mfma_f32_16x16x32_bf16 v[80:83], a[152:155], v[232:235], v[80:83]\n"
    "ds_read_b64_tr_b16 a[144:145], v11 offset:4096\n"
    "v_mfma_f32_16x16x32_bf16 v[84:87], a[152:155], v[236:239], v[84:87]\n"
    "ds_read_b64_tr_b16 a[146:147], v11 offset:6144\n"
    "s_add_u32 s68, s72, 0x6f00\n"
    "buffer_load_dwordx4 v57, s[60:63], s68 offen offset:3072 lds\n"
    "v_mfma_f32_16x16x32_bf16 v[88:91], a[156:159], v[232:235], v[88:91]\n"
    "ds_read_b64_tr_b16 a[148:149], v11 offset:4352\n"
    "v_mfma_f32_16x16x32_bf16 v[92:95], a[156:159], v[236:239], v[92:95]\n"
    "ds_read_b64_tr_b16 a[150:151], v11 offset:6400\n"
    "v_mfma_f32_16x16x32_bf16 v[64:67], a[192:195], v[240:243], v[64:67]\n"
    "ds_read_b64_tr_b16 a[152:153], v11 offset:4608\n"
    "s_add_u32 m0, s73, 0x1000\n"
    "s_add_u32 s68, s72, 0x9f00\n"
    "buffer_load_dwordx4 v57, s[60:63], s68 offen offset:0 lds\n"
    "v_mfma_f32_16x16x32_bf16 v[68:71], a[192:195], v[244:247], v[68:71]\n"
    "ds_read_b64_tr_b16 a[154:155], v11 offset:6656\n"
    "s_waitcnt lgkmcnt(13)\n"
    "v_mfma_f32_16x16x32_bf16 v[72:75], a[196:199], v[240:243], v[72:75]\n"
    "ds_read_b64_tr_b16 a[156:157], v11 offset:4864\n"
    "buffer_store_dwordx4 v[140:143], v61, s[64:67], s71 offen offset:512 sc1\n"
    "v_mfma_f32_16x16x32_bf16 v[76:79], a[196:199], v[244:247], v[76:79]\n"
    "s_add_u32 s71, s71, 0x400\n"
    "ds_read_b64_tr_b16 a[158:159], v11 offset:6912\n"
    "s_add_u32 s68, s72, 0xbf00\n"
    "buffer_load_dwordx4 v57, s[60:63], s68 offen offset:1024 lds\n"
    "v_mfma_f32_16x16x32_bf16 v[80:83], a[200:203], v[240:243], v[80:83]\n"
    "ds_read_b64_tr_b16 a[192:193], v11 offset:8192\n"
    "v_mfma_f32_16x16x32_bf16 v[84:87], a[200:203], v[244:247], v[84:87]\n"
    "ds_read_b64_tr_b16 a[194:195], v11 offset:10240\n"
    "v_mfma_f32_16x16x32_bf16 v[88:91], a[204:207], v[240:243], v[88:91]\n"
    "ds_read_b64_tr_b16 a[196:197], v11 offset:8448\n"
    "s_add_u32 s68, s72, 0xdf00\n"
    "buffer_load_dwordx4 v57, s[60:63], s68 offen offset:2048 lds\n"
    "v_mfma_f32_16x16x32_bf16 v[92:95], a[204:207], v[244:247], v[92:95]\n"
    "ds_read_b64_tr_b16 a[198:199], v11 offset:10496\n"
    "v_mfma_f32_16x16x32_bf16 v[64:67], a[208:211], v[248:251], v[64:67]\n"
    "ds_read_b64_tr_b16 a[200:201], v11 offset:8704\n"
    "v_mfma_f32_16x16x32_bf16 v[68:71], a[208:211], v[252:255], v[68:71]\n"
    "ds_read_b64_tr_b16 a[202:203], v11 offset:10752\n"
    "s_add_u32 s68, s72, 0xff00\n"
    "buffer_load_dwordx4 v57, s[60:63], s68 offen offset:3072 lds\n"
    "v_mfma_f32_16x16x32_bf16 v[72:75], a[212:215], v[248:251], v[72:75]\n"
    "ds_read_b64_tr_b16 a[204:205], v11 offset:8960\n"
    "v_mfma_f32_16x16x32_bf16 v[76:79], a[212:215], v[252:255], v[76:79]\n"
    "ds_read_b64_tr_b16 a[206:207], v11 offset:11008\n"
    "v_mfma_f32_16x16x32_bf16 v[80:83], a[216:219], v[248:251], v[80:83]\n"
    "ds_read_b64_tr_b16 a[208:209], v11 offset:12288\n"
    "v_mfma_f32_16x16x32_bf16 v[84:87], a[216:219], v[252:255], v[84:87]\n"
    "ds_read_b64_tr_b16 a[210:211], v11 offset:14336\n"
    "v_mfma_f32_16x16x32_bf16 v[88:91], a[220:223], v[248:251], v[88:91]\n"
    "ds_read_b64_tr_b16 a[212:213], v11 offset:12544\n"
    "v_mfma_f32_16x16x32_bf16 v[92:95], a[220:223], v[252:255], v[92:95]\n"
    "ds_read_b64_tr_b16 a[216:217], v11 offset:12800\n"
    "ds_read_b64_tr_b16 a[220:221], v11 offset:13056\n"
    "ds_read_b64_tr_b16 a[214:215], v11 offset:14592\n"
    "ds_read_b64_tr_b16 a[218:219], v11 offset:14848\n"
    "ds_read_b64_tr_b16 a[222:223], v11 offset:15104\n"
    "s_waitcnt lgkmcnt(14)\n"
    "v_mfma_f32_16x16x32_bf16 v[96:99], a[128:131], v[192:195], 0\n"
    "v_mfma_f32_16x16x32_bf16 v[100:103], a[128:131], v[196:199], 0\n"
    "ds_read_b64_tr_b16 a[128:129], v11 offset:16384\n"
    "v_mfma_f32_16x16x32_bf16 v[104:107], a[132:135], v[192:195], 0\n"
    "ds_read_b64_tr_b16 a[130:131], v11 offset:18432\n"
    "v_mfma_f32_16x16x32_bf16 v[108:111], a[132:135], v[196:199], 0\n"
    "ds_read_b64_tr_b16 a[132:133], v11 offset:16640\n"
    "v_mfma_f32_16x16x32_bf16 v[112:115], a[136:139], v[192:195], 0\n"
    "ds_read_b64_tr_b16 a[134:135], v11 offset:18688\n"
    "v_cvt_pk_bf16_f32 v128, v64, v65\n"
    "v_mfma_f32_16x16x32_bf16 v[116:119], a[136:139], v[196:199], 0\n"
    "ds_read_b64_tr_b16 a[136:137], v11 offset:16896\n"
    "v_cvt_pk_bf16_f32 v129, v66, v67\n"
    "v_mfma_f32_16x16x32_bf16 v[120:123], a[140:143], v[192:195], 0\n"
    "ds_read_b64_tr_b16 a[138:139], v11 offset:18944\n"
    "v_cvt_pk_bf16_f32 v130, v72, v73\n"
    "v_mfma_f32_16x16x32_bf16 v[124:127], a[140:143], v[196:199], 0\n"
    "ds_read_b64_tr_b16 a[140:141], v11 offset:17152\n"
    "v_cvt_pk_bf16_f32 v131, v74, v75\n"
    "v_mfma_f32_16x16x32_bf16 v[96:99], a[144:147], v[200:203], v[96:99]\n"
    "ds_read_b64_tr_b16 a[142:143], v11 offset:19200\n"
    "v_mfma_f32_16x16x32_bf16 v[100:103], a[144:147], v[204:207], v[100:103]\n"
    "ds_read_b64_tr_b16 a[144:145], v11 offset:20480\n"
    "v_mfma_f32_16x16x32_bf16 v[104:107], a[148:151], v[200:203], v[104:107]\n"
    "ds_read_b64_tr_b16 a[146:147], v11 offset:22528\n"
    "v_cvt_pk_bf16_f32 v132, v80, v81\n"
    "v_mfma_f32_16x16x32_bf16 v[108:111], a[148:151], v[204:207], v[108:111]\n"
    "ds_read_b64_tr_b16 a[148:149], v11 offset:20736\n"
    "v_cvt_pk_bf16_f32 v133, v82, v83\n"
    "v_mfma_f32_16x16x32_bf16 v[112:115], a[152:155], v[200:203], v[112:115]\n"
    "ds_read_b64_tr_b16 a[150:151], v11 offset:22784\n"
    "v_cvt_pk_bf16_f32 v134, v88, v89\n"
    "v_mfma_f32_16x16x32_bf16 v[116:119], a[152:155], v[204:207], v[116:119]\n"
    "ds_read_b64_tr_b16 a[152:153], v11 offset:20992\n"
    "v_cvt_pk_bf16_f32 v135, v90, v91\n"
    "v_mfma_f32_16x16x32_bf16 v[120:123], a[156:159], v[200:203], v[120:123]\n"
    "ds_read_b64_tr_b16 a[154:155], v11 offset:23040\n"
    "v_mfma_f32_16x16x32_bf16 v[124:127], a[156:159], v[204:207], v[124:127]\n"
    "ds_read_b64_tr_b16 a[156:157], v11 offset:21248\n"
    "v_mfma_f32_16x16x32_bf16 v[96:99], a[192:195], v[208:211], v[96:99]\n"
    "ds_read_b64_tr_b16 a[158:159], v11 offset:23296\n"
    "v_cvt_pk_bf16_f32 v136, v68, v69\n"
    "v_mfma_f32_16x16x32_bf16 v[100:103], a[192:195], v[212:215], v[100:103]\n"
    "ds_read_b64_tr_b16 a[192:193], v11 offset:24576\n"
    "v_cvt_pk_bf16_f32 v137, v70, v71\n"
    "s_waitcnt lgkmcnt(13)\n"
    "v_mfma_f32_16x16x32_bf16 v[104:107], a[196:199], v[208:211], v[104:107]\n"
    "buffer_store_dwordx4 v[128:131], v60, s[64:67], s71 offen offset:0 sc1\n"
    "v_cvt_pk_bf16_f32 v138, v76, v77\n"
    "v_mfma_f32_16x16x32_bf16 v[108:111], a[196:199], v[212:215], v[108:111]\n"
    "ds_read_b64_tr_b16 a[194:195], v11 offset:26624\n"
    "v_cvt_pk_bf16_f32 v139, v78, v79\n"
    "v_mfma_f32_16x16x32_bf16 v[112:115], a[200:203], v[208:211], v[112:115]\n"
    "ds_read_b64_tr_b16 a[196:197], v11 offset:24832\n"
    "v_mfma_f32_16x16x32_bf16 v[116:119], a[200:203], v[212:215], v[116:119]\n"
    "ds_read_b64_tr_b16 a[198:199], v11 offset:26880\n"
    "v_mfma_f32_16x16x32_bf16 v[120:123], a[204:207], v[208:211], v[120:123]\n"
    "ds_read_b64_tr_b16 a[200:201], v11 offset:25088\n"
    "v_cvt_pk_bf16_f32 v140, v84, v85\n"
    "v_mfma_f32_16x16x32_bf16 v[124:127], a[204:207], v[212:215], v[124:127]\n"
    "ds_read_b64_tr_b16 a[202:203], v11 offset:27136\n"
    "v_cvt_pk_bf16_f32 v141, v86, v87\n"
    "v_mfma_f32_16x16x32_bf16 v[96:99], a[208:211], v[216:219], v[96:99]\n"
    "ds_read_b64_tr_b16 a[204:205], v11 offset:25344\n"
    "v_cvt_pk_bf16_f32 v142, v92, v93\n"
    "v_mfma_f32_16x16x32_bf16 v[100:103], a[208:211], v[220:223], v[100:103]\n"
    "ds_read_b64_tr_b16 a[206:207], v11 offset:27392\n"
    "v_cvt_pk_bf16_f32 v143, v94, v95\n"
    "v_mfma_f32_16x16x32_bf16 v[104:107], a[212:215], v[216:219], v[104:107]\n"
    "ds_read_b64_tr_b16 a[208:209], v11 offset:28672\n"
    "v_mfma_f32_16x16x32_bf16 v[108:111], a[212:215], v[220:223], v[108:111]\n"
    "ds_read_b64_tr_b16 a[210:211], v11 offset:30720\n"
    "v_mfma_f32_16x16x32_bf16 v[112:115], a[216:219], v[216:219], v[112:115]\n"
    "ds_read_b64_tr_b16 a[212:213], v11 offset:28928\n"
    "v_mfma_f32_16x16x32_bf16 v[116:119], a[216:219], v[220:223], v[116:119]\n"
    "buffer_store_dwordx4 v[132:135], v60, s[64:67], s71 offen offset:512 sc1\n"
    "v_mfma_f32_16x16x32_bf16 v[120:123], a[220:223], v[216:219], v[120:123]\n"
    "ds_read_b64_tr_b16 a[214:215], v11 offset:30976\n"
    "v_mfma_f32_16x16x32_bf16 v[124:127], a[220:223], v[220:223], v[124:127]\n"
    "ds_read_b64_tr_b16 a[216:217], v11 offset:29184\n"
    "v_mfma_f32_16x16x32_bf16 v[96:99], a[128:131], v[224:227], v[96:99]\n"
    "s_waitcnt vmcnt(26)\n"
    "s_barrier\n"
    "ds_read_b64_tr_b16 a[218:219], v11 offset:31232\n"
    "v_mfma_f32_16x16x32_bf16 v[100:103], a[128:131], v[228:231], v[100:103]\n"
    "ds_read_b64_tr_b16 a[220:221], v11 offset:29440\n"
    "v_mfma_f32_16x16x32_bf16 v[104:107], a[132:135], v[224:227], v[104:107]\n"
    "ds_read_b64_tr_b16 a[222:223], v11 offset:31488\n"
    "v_mfma_f32_16x16x32_bf16 v[108:111], a[132:135], v[228:231], v[108:111]\n"
    "ds_read_b64_tr_b16 a[128:129], v56 offset:0\n"
    "s_waitcnt lgkmcnt(14)\n"
    "v_mfma_f32_16x16x32_bf16 v[112:115], a[136:139], v[224:227], v[112:115]\n"
    "s_add_u32 m0, s73, 0x10000\n"
    "s_add_u32 s68, s72, 0xf80\n"
    "buffer_load_dwordx4 v57, s[60:63], s68 offen offset:0 lds\n"
    "v_mfma_f32_16x16x32_bf16 v[116:119], a[136:139], v[228:231], v[116:119]\n"
    "ds_read_b64_tr_b16 a[130:131], v56 offset:2048\n"
    "v_mfma_f32_16x16x32_bf16 v[120:123], a[140:143], v[224:227], v[120:123]\n"
    "ds_read_b64_tr_b16 a[132:133], v56 offset:256\n"
    "v_mfma_f32_16x16x32_bf16 v[124:127], a[140:143], v[228:231], v[124:127]\n"
    "ds_read_b64_tr_b16 a[134:135], v56 offset:2304\n"
    "s_add_u32 s68, s72, 0x2f80\n"
    "buffer_load_dwordx4 v57, s[60:63], s68 offen offset:1024 lds\n"
    "v_mfma_f32_16x16x32_bf16 v[96:99], a[144:147], v[232:235], v[96:99]\n"
    "ds_read_b64_tr_b16 a[136:137], v56 offset:512\n"
    "buffer_store_dwordx4 v[136:139], v61, s[64:67], s71 offen offset:0 sc1\n"
    "v_mfma_f32_16x16x32_bf16 v[100:103], a[144:147], v[236:239], v[100:103]\n"
    "ds_read_b64_tr_b16 a[138:139], v56 offset:2560\n"
    "v_mfma_f32_16x16x32_bf16 v[104:107], a[148:151], v[232:235], v[104:107]\n"
    "ds_read_b64_tr_b16 a[140:141], v56 offset:768\n"
    "s_add_u32 s68, s72, 0x4f80\n"
    "buffer_load_dwordx4 v57, s[60:63], s68 offen offset:2048 lds\n"
    "v_mfma_f32_16x16x32_bf16 v[108:111], a[148:151], v[236:239], v[108:111]\n"
    "ds_read_b64_tr_b16 a[142:143], v56 offset:2816\n"
    "v_mfma_f32_16x16x32_bf16 v[112:115], a[152:155], v[232:235], v[112:115]\n"
    "ds_read_b64_tr_b16 a[144:145], v56 offset:4096\n"
    "v_mfma_f32_16x16x32_bf16 v[116:119], a[152:155], v[236:239], v[116:119]\n"
    "ds_read_b64_tr_b16 a[146:147], v56 offset:6144\n"
    "s_add_u32 s68, s72, 0x6f80\n"
    "buffer_load_dwordx4 v57, s[60:63], s68 offen offset:3072 lds\n"
    "v_mfma_f32_16x16x32_bf16 v[120:123], a[156:159], v[232:235], v[120:123]\n"
    "ds_read_b64_tr_b16 a[148:149], v56 offset:4352\n"
    "v_mfma_f32_16x16x32_bf16 v[124:127], a[156:159], v[236:239], v[124:127]\n"
    "ds_read_b64_tr_b16 a[150:151], v56 offset:6400\n"
    "v_mfma_f32_16x16x32_bf16 v[96:99], a[192:195], v[240:243], v[96:99]\n"
    "ds_read_b64_tr_b16 a[152:153], v56 offset:4608\n"
    "s_add_u32 m0, s73, 0x11000\n"
    "s_add_u32 s68, s72, 0x9f80\n"
    "buffer_load_dwordx4 v57, s[60:63], s68 offen offset:0 lds\n"
    "v_mfma_f32_16x16x32_bf16 v[100:103], a[192:195], v[244:247], v[100:103]\n"
    "ds_read_b64_tr_b16 a[154:155], v56 offset:6656\n"
    "s_waitcnt lgkmcnt(13)\n"
    "v_mfma_f32_16x16x32_bf16 v[104:107], a[196:199], v[240:243], v[104:107]\n"
    "ds_read_b64_tr_b16 a[156:157], v56 offset:4864\n"
    "buffer_store_dwordx4 v[140:143], v61, s[64:67], s71 offen offset:512 sc1\n"
    "v_mfma_f32_16x16x32_bf16 v[108:111], a[196:199], v[244:247], v[108:111]\n"
    "s_add_u32 s71, s71, 0x400\n"
    "ds_read_b64_tr_b16 a[158:159], v56 offset:6912\n"
    "s_add_u32 s68, s72, 0xbf80\n"
    "buffer_load_dwordx4 v57, s[60:63], s68 offen offset:1024 lds\n"
    "v_mfma_f32_16x16x32_bf16 v[112:115], a[200:203], v[240:243], v[112:115]\n"
    "ds_read_b64_tr_b16 a[192:193], v56 offset:8192\n"
    "v_mfma_f32_16x16x32_bf16 v[116:119], a[200:203], v[244:247], v[116:119]\n"
    "ds_read_b64_tr_b16 a[194:195], v56 offset:10240\n"
    "v_mfma_f32_16x16x32_bf16 v[120:123], a[204:207], v[240:243], v[120:123]\n"
    "ds_read_b64_tr_b16 a[196:197], v56 offset:8448\n"
    "s_add_u32 s68, s72, 0xdf80\n"
    "buffer_load_dwordx4 v57, s[60:63], s68 offen offset:2048 lds\n"
    "v_mfma_f32_16x16x32_bf16 v[124:127], a[204:207], v[244:247], v[124:127]\n"
    "ds_read_b64_tr_b16 a[198:199], v56 offset:10496\n"
    "v_mfma_f32_16x16x32_bf16 v[96:99], a[208:211], v[248:251], v[96:99]\n"
    "ds_read_b64_tr_b16 a[200:201], v56 offset:8704\n"
    "v_mfma_f32_16x16x32_bf16 v[100:103], a[208:211], v[252:255], v[100:103]\n"
    "ds_read_b64_tr_b16 a[202:203], v56 offset:10752\n"
    "s_add_u32 s68, s72, 0xff80\n"
    "buffer_load_dwordx4 v57, s[60:63], s68 offen offset:3072 lds\n"
    "v_mfma_f32_16x16x32_bf16 v[104:107], a[212:215], v[248:251], v[104:107]\n"
    "ds_read_b64_tr_b16 a[204:205], v56 offset:8960\n"
    "v_mfma_f32_16x16x32_bf16 v[108:111], a[212:215], v[252:255], v[108:111]\n"
    "ds_read_b64_tr_b16 a[206:207], v56 offset:11008\n"
    "v_mfma_f32_16x16x32_bf16 v[112:115], a[216:219], v[248:251], v[112:115]\n"
    "ds_read_b64_tr_b16 a[208:209], v56 offset:12288\n"
    "v_mfma_f32_16x16x32_bf16 v[116:119], a[216:219], v[252:255], v[116:119]\n"
    "ds_read_b64_tr_b16 a[210:211], v56 offset:14336\n"
    "v_mfma_f32_16x16x32_bf16 v[120:123], a[220:223], v[248:251], v[120:123]\n"
    "ds_read_b64_tr_b16 a[212:213], v56 offset:12544\n"
    "v_mfma_f32_16x16x32_bf16 v[124:127], a[220:223], v[252:255], v[124:127]\n"
    "ds_read_b64_tr_b16 a[216:217], v56 offset:12800\n"
    "ds_read_b64_tr_b16 a[220:221], v56 offset:13056\n"
    "ds_read_b64_tr_b16 a[214:215], v56 offset:14592\n"
    "ds_read_b64_tr_b16 a[218:219], v56 offset:14848\n"
    "ds_read_b64_tr_b16 a[222:223], v56 offset:15104\n"
    "s_waitcnt lgkmcnt(14)\n"
    "v_mfma_f32_16x16x32_bf16 v[64:67], a[128:131], v[192:195], 0\n"
    "v_mfma_f32_16x16x32_bf16 v[68:71], a[128:131], v[196:199], 0\n"
    "ds_read_b64_tr_b16 a[128:129], v56 offset:16384\n"
    "v_mfma_f32_16x16x32_bf16 v[72:75], a[132:135], v[192:195], 0\n"
    "ds_read_b64_tr_b16 a[130:131], v56 offset:18432\n"
    "v_mfma_f32_16x16x32_bf16 v[76:79], a[132:135], v[196:199], 0\n"
    "ds_read_b64_tr_b16 a[132:133], v56 offset:16640\n"
    "v_mfma_f32_16x16x32_bf16 v[80:83], a[136:139], v[192:195], 0\n"
    "ds_read_b64_tr_b16 a[134:135], v56 offset:18688\n"
    "v_cvt_pk_bf16_f32 v128, v96, v97\n"
    "v_mfma_f32_16x16x32_bf16 v[84:87], a[136:139], v[196:199], 0\n"
    "ds_read_b64_tr_b16 a[136:137], v56 offset:16896\n"
    "v_cvt_pk_bf16_f32 v129, v98, v99\n"
    "v_mfma_f32_16x16x32_bf16 v[88:91], a[140:143], v[192:195], 0\n"
    "ds_read_b64_tr_b16 a[138:139], v56 offset:18944\n"
    "v_cvt_pk_bf16_f32 v130, v104, v105\n"
    "v_mfma_f32_16x16x32_bf16 v[92:95], a[140:143], v[196:199], 0\n"
    "ds_read_b64_tr_b16 a[140:141], v56 offset:17152\n"
    "v_cvt_pk_bf16_f32 v131, v106, v107\n"
    "v_mfma_f32_16x16x32_bf16 v[64:67], a[144:147], v[200:203], v[64:67]\n"
    "ds_read_b64_tr_b16 a[142:143], v56 offset:19200\n"
    "v_mfma_f32_16x16x32_bf16 v[68:71], a[144:147], v[204:207], v[68:71]\n"
    "ds_read_b64_tr_b16 a[144:145], v56 offset:20480\n"
    "v_mfma_f32_16x16x32_bf16 v[72:75], a[148:151], v[200:203], v[72:75]\n"
    "ds_read_b64_tr_b16 a[146:147], v56 offset:22528\n"
    "v_cvt_pk_bf16_f32 v132, v112, v113\n"
    "v_mfma_f32_16x16x32_bf16 v[76:79], a[148:151], v[204:207], v[76:79]\n"
    "ds_read_b64_tr_b16 a[148:149], v56 offset:20736\n"
    "v_cvt_pk_bf16_f32 v133, v114, v115\n"
    "v_mfma_f32_16x16x32_bf16 v[80:83], a[152:155], v[200:203], v[80:83]\n"
    "ds_read_b64_tr_b16 a[150:151], v56 offset:22784\n"
    "v_cvt_pk_bf16_f32 v134, v120, v121\n"
    "v_mfma_f32_16x16x32_bf16 v[84:87], a[152:155], v[204:207], v[84:87]\n"
    "ds_read_b64_tr_b16 a[152:153], v56 offset:20992\n"
    "v_cvt_pk_bf16_f32 v135, v122, v123\n"
    "v_mfma_f32_16x16x32_bf16 v[88:91], a[156:159], v[200:203], v[88:91]\n"
    "ds_read_b64_tr_b16 a[154:155], v56 offset:23040\n"
    "v_mfma_f32_16x16x32_bf16 v[92:95], a[156:159], v[204:207], v[92:95]\n"
    "ds_read_b64_tr_b16 a[156:157], v56 offset:21248\n"
    "v_mfma_f32_16x16x32_bf16 v[64:67], a[192:195], v[208:211], v[64:67]\n"
    "ds_read_b64_tr_b16 a[158:159], v56 offset:23296\n"
    "v_cvt_pk_bf16_f32 v136, v100, v101\n"
    "v_mfma_f32_16x16x32_bf16 v[68:71], a[192:195], v[212:215], v[68:71]\n"
    "ds_read_b64_tr_b16 a[192:193], v56 offset:24576\n"
    "v_cvt_pk_bf16_f32 v137, v102, v103\n"
    "s_waitcnt lgkmcnt(13)\n"
    "v_mfma_f32_16x16x32_bf16 v[72:75], a[196:199], v[208:211], v[72:75]\n"
    "buffer_store_dwordx4 v[128:131], v60, s[64:67], s71 offen offset:0 sc1\n"
    "v_cvt_pk_bf16_f32 v138, v108, v109\n"
    "v_mfma_f32_16x16x32_bf16 v[76:79], a[196:199], v[212:215], v[76:79]\n"
    "ds_read_b64_tr_b16 a[194:195], v56 offset:26624\n"
    "v_cvt_pk_bf16_f32 v139, v110, v111\n"
    "v_mfma_f32_16x16x32_bf16 v[80:83], a[200:203], v[208:211], v[80:83]\n"
    "ds_read_b64_tr_b16 a[196:197], v56 offset:24832\n"
    "v_mfma_f32_16x16x32_bf16 v[84:87], a[200:203], v[212:215], v[84:87]\n"
    "ds_read_b64_tr_b16 a[198:199], v56 offset:26880\n"
    "v_mfma_f32_16x16x32_bf16 v[88:91], a[204:207], v[208:211], v[88:91]\n"
    "ds_read_b64_tr_b16 a[200:201], v56 offset:25088\n"
    "v_cvt_pk_bf16_f32 v140, v116, v117\n"
    "v_mfma_f32_16x16x32_bf16 v[92:95], a[204:207], v[212:215], v[92:95]\n"
    "ds_read_b64_tr_b16 a[202:203], v56 offset:27136\n"
    "v_cvt_pk_bf16_f32 v141, v118, v119\n"
    "v_mfma_f32_16x16x32_bf16 v[64:67], a[208:211], v[216:219], v[64:67]\n"
    "ds_read_b64_tr_b16 a[204:205], v56 offset:25344\n"
    "v_cvt_pk_bf16_f32 v142, v124, v125\n"
    "v_mfma_f32_16x16x32_bf16 v[68:71], a[208:211], v[220:223], v[68:71]\n"
    "ds_read_b64_tr_b16 a[206:207], v56 offset:27392\n"
    "v_cvt_pk_bf16_f32 v143, v126, v127\n"
    "v_mfma_f32_16x16x32_bf16 v[72:75], a[212:215], v[216:219], v[72:75]\n"
    "ds_read_b64_tr_b16 a[208:209], v56 offset:28672\n"
    "v_mfma_f32_16x16x32_bf16 v[76:79], a[212:215], v[220:223], v[76:79]\n"
    "ds_read_b64_tr_b16 a[210:211], v56 offset:30720\n"
    "v_mfma_f32_16x16x32_bf16 v[80:83], a[216:219], v[216:219], v[80:83]\n"
    "ds_read_b64_tr_b16 a[212:213], v56 offset:28928\n"
    "v_mfma_f32_16x16x32_bf16 v[84:87], a[216:219], v[220:223], v[84:87]\n"
    "buffer_store_dwordx4 v[132:135], v60, s[64:67], s71 offen offset:512 sc1\n"
    "v_mfma_f32_16x16x32_bf16 v[88:91], a[220:223], v[216:219], v[88:91]\n"
    "ds_read_b64_tr_b16 a[214:215], v56 offset:30976\n"
    "v_mfma_f32_16x16x32_bf16 v[92:95], a[220:223], v[220:223], v[92:95]\n"
    "ds_read_b64_tr_b16 a[216:217], v56 offset:29184\n"
    "v_mfma_f32_16x16x32_bf16 v[64:67], a[128:131], v[224:227], v[64:67]\n"
    "s_waitcnt vmcnt(26)\n"
    "s_barrier\n"
    "ds_read_b64_tr_b16 a[218:219], v56 offset:31232\n"
    "v_mfma_f32_16x16x32_bf16 v[68:71], a[128:131], v[228:231], v[68:71]\n"
    "ds_read_b64_tr_b16 a[220:221], v56 offset:29440\n"
    "v_mfma_f32_16x16x32_bf16 v[72:75], a[132:135], v[224:227], v[72:75]\n"
    "ds_read_b64_tr_b16 a[222:223], v56 offset:31488\n"
    "v_mfma_f32_16x16x32_bf16 v[76:79], a[132:135], v[228:231], v[76:79]\n"
    "ds_read_b64_tr_b16 a[128:129], v8 offset:0\n"
    "s_waitcnt lgkmcnt(14)\n"
    "v_mfma_f32_16x16x32_bf16 v[80:83], a[136:139], v[224:227], v[80:83]\n"
    "s_add_u32 m0, s73, 0x8000\n"
    "s_add_u32 s68, s72, 0x1000\n"
    "buffer_load_dwordx4 v57, s[60:63], s68 offen offset:0 lds\n"
    "v_mfma_f32_16x16x32_bf16 v[84:87], a[136:139], v[228:231], v[84:87]\n"
    "ds_read_b64_tr_b16 a[130:131], v8 offset:2048\n"
    "v_mfma_f32_16x16x32_bf16 v[88:91], a[140:143], v[224:227], v[88:91]\n"
    "ds_read_b64_tr_b16 a[132:133], v8 offset:256\n"
    "v_mfma_f32_16x16x32_bf16 v[92:95], a[140:143], v[228:231], v[92:95]\n"
    "ds_read_b64_tr_b16 a[134:135], v8 offset:2304\n"
    "s_add_u32 s68, s72, 0x3000\n"
    "buffer_load_dwordx4 v57, s[60:63], s68 offen offset:1024 lds\n"
    "v_mfma_f32_16x16x32_bf16 v[64:67], a[144:147], v[232:235], v[64:67]\n"
    "ds_read_b64_tr_b16 a[136:137], v8 offset:512\n"
    "buffer_store_dwordx4 v[136:139], v61, s[64:67], s71 offen offset:0 sc1\n"
    "v_mfma_f32_16x16x32_bf16 v[68:71], a[144:147], v[236:239], v[68:71]\n"
    "ds_read_b64_tr_b16 a[138:139], v8 offset:2560\n"
    "v_mfma_f32_16x16x32_bf16 v[72:75], a[148:151], v[232:235], v[72:75]\n"
    "ds_read_b64_tr_b16 a[140:141], v8 offset:768\n"
    "s_add_u32 s68, s72, 0x5000\n"
    "buffer_load_dwordx4 v57, s[60:63], s68 offen offset:2048 lds\n"
    "v_mfma_f32_16x16x32_bf16 v[76:79], a[148:151], v[236:239], v[76:79]\n"
    "ds_read_b64_tr_b16 a[142:143], v8 offset:2816\n"
    "v_mfma_f32_16x16x32_bf16 v[80:83], a[152:155], v[232:235], v[80:83]\n"
    "ds_read_b64_tr_b16 a[144:145], v8 offset:4096\n"
    "v_mfma_f32_16x16x32_bf16 v[84:87], a[152:155], v[236:239], v[84:87]\n"
    "ds_read_b64_tr_b16 a[146:147], v8 offset:6144\n"
    "s_add_u32 s68, s72, 0x7000\n"
    "buffer_load_dwordx4 v57, s[60:63], s68 offen offset:3072 lds\n"
    "v_mfma_f32_16x16x32_bf16 v[88:91], a[156:159], v[232:235], v[88:91]\n"
    "ds_read_b64_tr_b16 a[148:149], v8 offset:4352\n"
    "v_mfma_f32_16x16x32_bf16 v[92:95], a[156:159], v[236:239], v[92:95]\n"
    "ds_read_b64_tr_b16 a[150:151], v8 offset:6400\n"
    "v_mfma_f32_16x16x32_bf16 v[64:67], a[192:195], v[240:243], v[64:67]\n"
    "ds_read_b64_tr_b16 a[152:153], v8 offset:4608\n"
    "s_add_u32 m0, s73, 0x9000\n"
    "s_add_u32 s68, s72, 0xa000\n"
    "buffer_load_dwordx4 v57, s[60:63], s68 offen offset:0 lds\n"
    "v_mfma_f32_16x16x32_bf16 v[68:71], a[192:195], v[244:247], v[68:71]\n"
    "ds_read_b64_tr_b16 a[154:155], v8 offset:6656\n"
    "s_waitcnt lgkmcnt(13)\n"
    "v_mfma_f32_16x16x32_bf16 v[72:75], a[196:199], v[240:243], v[72:75]\n"
    "ds_read_b64_tr_b16 a[156:157], v8 offset:4864\n"
    "buffer_store_dwordx4 v[140:143], v61, s[64:67], s71 offen offset:512 sc1\n"
    "v_mfma_f32_16x16x32_bf16 v[76:79], a[196:199], v[244:247], v[76:79]\n"
    "s_add_u32 s71, s71, 0x400\n"
    "ds_read_b64_tr_b16 a[158:159], v8 offset:6912\n"
    "s_add_u32 s68, s72, 0xc000\n"
    "buffer_load_dwordx4 v57, s[60:63], s68 offen offset:1024 lds\n"
    "v_mfma_f32_16x16x32_bf16 v[80:83], a[200:203], v[240:243], v[80:83]\n"
    "ds_read_b64_tr_b16 a[192:193], v8 offset:8192\n"
    "v_mfma_f32_16x16x32_bf16 v[84:87], a[200:203], v[244:247], v[84:87]\n"
    "ds_read_b64_tr_b16 a[194:195], v8 offset:10240\n"
    "v_mfma_f32_16x16x32_bf16 v[88:91], a[204:207], v[240:243], v[88:91]\n"
    "ds_read_b64_tr_b16 a[196:197], v8 offset:8448\n"
    "s_add_u32 s68, s72, 0xe000\n"
    "buffer_load_dwordx4 v57, s[60:63], s68 offen offset:2048 lds\n"
    "v_mfma_f32_16x16x32_bf16 v[92:95], a[204:207], v[244:247], v[92:95]\n"
    "ds_read_b64_tr_b16 a[198:199], v8 offset:10496\n"
    "v_mfma_f32_16x16x32_bf16 v[64:67], a[208:211], v[248:251], v[64:67]\n"
    "ds_read_b64_tr_b16 a[200:201], v8 offset:8704\n"
    "v_mfma_f32_16x16x32_bf16 v[68:71], a[208:211], v[252:255], v[68:71]\n"
    "ds_read_b64_tr_b16 a[202:203], v8 offset:10752\n"
    "s_add_u32 s68, s72, 0x10000\n"
    "buffer_load_dwordx4 v57, s[60:63], s68 offen offset:3072 lds\n"
    "v_mfma_f32_16x16x32_bf16 v[72:75], a[212:215], v[248:251], v[72:75]\n"
    "ds_read_b64_tr_b16 a[204:205], v8 offset:8960\n"
    "v_mfma_f32_16x16x32_bf16 v[76:79], a[212:215], v[252:255], v[76:79]\n"
    "ds_read_b64_tr_b16 a[206:207], v8 offset:11008\n"
    "v_mfma_f32_16x16x32_bf16 v[80:83], a[216:219], v[248:251], v[80:83]\n"
    "ds_read_b64_tr_b16 a[208:209], v8 offset:12288\n"
    "v_mfma_f32_16x16x32_bf16 v[84:87], a[216:219], v[252:255], v[84:87]\n"
    "ds_read_b64_tr_b16 a[210:211], v8 offset:14336\n"
    "v_mfma_f32_16x16x32_bf16 v[88:91], a[220:223], v[248:251], v[88:91]\n"
    "ds_read_b64_tr_b16 a[212:213], v8 offset:12544\n"
    "v_mfma_f32_16x16x32_bf16 v[92:95], a[220:223], v[252:255], v[92:95]\n"
    "ds_read_b64_tr_b16 a[216:217], v8 offset:12800\n"
    "ds_read_b64_tr_b16 a[220:221], v8 offset:13056\n"
    "ds_read_b64_tr_b16 a[214:215], v8 offset:14592\n"
    "ds_read_b64_tr_b16 a[218:219], v8 offset:14848\n"
    "ds_read_b64_tr_b16 a[222:223], v8 offset:15104\n"
    "s_waitcnt lgkmcnt(14)\n"
    "v_mfma_f32_16x16x32_bf16 v[96:99], a[128:131], v[192:195], 0\n"
    "v_mfma_f32_16x16x32_bf16 v[100:103], a[128:131], v[196:199], 0\n"
    "ds_read_b64_tr_b16 a[128:129], v8 offset:16384\n"
    "v_mfma_f32_16x16x32_bf16 v[104:107], a[132:135], v[192:195], 0\n"
    "ds_read_b64_tr_b16 a[130:131], v8 offset:18432\n"
    "v_mfma_f32_16x16x32_bf16 v[108:111], a[132:135], v[196:199], 0\n"
    "ds_read_b64_tr_b16 a[132:133], v8 offset:16640\n"
    "v_mfma_f32_16x16x32_bf16 v[112:115], a[136:139], v[192:195], 0\n"
    "ds_read_b64_tr_b16 a[134:135], v8 offset:18688\n"
    "v_cvt_pk_bf16_f32 v128, v64, v65\n"
    "v_mfma_f32_16x16x32_bf16 v[116:119], a[136:139], v[196:199], 0\n"
    "ds_read_b64_tr_b16 a[136:137], v8 offset:16896\n"
    "v_cvt_pk_bf16_f32 v129, v66, v67\n"
    "v_mfma_f32_16x16x32_bf16 v[120:123], a[140:143], v[192:195], 0\n"
    "ds_read_b64_tr_b16 a[138:139], v8 offset:18944\n"
    "v_cvt_pk_bf16_f32 v130, v72, v73\n"
    "v_mfma_f32_16x16x32_bf16 v[124:127], a[140:143], v[196:199], 0\n"
    "ds_read_b64_tr_b16 a[140:141], v8 offset:17152\n"
    "v_cvt_pk_bf16_f32 v131, v74, v75\n"
    "v_mfma_f32_16x16x32_bf16 v[96:99], a[144:147], v[200:203], v[96:99]\n"
    "ds_read_b64_tr_b16 a[142:143], v8 offset:19200\n"
    "v_mfma_f32_16x16x32_bf16 v[100:103], a[144:147], v[204:207], v[100:103]\n"
    "ds_read_b64_tr_b16 a[144:145], v8 offset:20480\n"
    "v_mfma_f32_16x16x32_bf16 v[104:107], a[148:151], v[200:203], v[104:107]\n"
    "ds_read_b64_tr_b16 a[146:147], v8 offset:22528\n"
    "v_cvt_pk_bf16_f32 v132, v80, v81\n"
    "v_mfma_f32_16x16x32_bf16 v[108:111], a[148:151], v[204:207], v[108:111]\n"
    "ds_read_b64_tr_b16 a[148:149], v8 offset:20736\n"
    "v_cvt_pk_bf16_f32 v133, v82, v83\n"
    "v_mfma_f32_16x16x32_bf16 v[112:115], a[152:155], v[200:203], v[112:115]\n"
    "ds_read_b64_tr_b16 a[150:151], v8 offset:22784\n"
    "v_cvt_pk_bf16_f32 v134, v88, v89\n"
    "v_mfma_f32_16x16x32_bf16 v[116:119], a[152:155], v[204:207], v[116:119]\n"
    "ds_read_b64_tr_b16 a[152:153], v8 offset:20992\n"
    "v_cvt_pk_bf16_f32 v135, v90, v91\n"
    "v_mfma_f32_16x16x32_bf16 v[120:123], a[156:159], v[200:203], v[120:123]\n"
    "ds_read_b64_tr_b16 a[154:155], v8 offset:23040\n"
    "v_mfma_f32_16x16x32_bf16 v[124:127], a[156:159], v[204:207], v[124:127]\n"
    "ds_read_b64_tr_b16 a[156:157], v8 offset:21248\n"
    "v_mfma_f32_16x16x32_bf16 v[96:99], a[192:195], v[208:211], v[96:99]\n"
    "ds_read_b64_tr_b16 a[158:159], v8 offset:23296\n"
    "v_cvt_pk_bf16_f32 v136, v68, v69\n"
    "v_mfma_f32_16x16x32_bf16 v[100:103], a[192:195], v[212:215], v[100:103]\n"
    "ds_read_b64_tr_b16 a[192:193], v8 offset:24576\n"
    "v_cvt_pk_bf16_f32 v137, v70, v71\n"
    "s_waitcnt lgkmcnt(13)\n"
    "v_mfma_f32_16x16x32_bf16 v[104:107], a[196:199], v[208:211], v[104:107]\n"
    "buffer_store_dwordx4 v[128:131], v60, s[64:67], s71 offen offset:0 sc1\n"
    "v_cvt_pk_bf16_f32 v138, v76, v77\n"
    "v_mfma_f32_16x16x32_bf16 v[108:111], a[196:199], v[212:215], v[108:111]\n"
    "ds_read_b64_tr_b16 a[194:195], v8 offset:26624\n"
    "v_cvt_pk_bf16_f32 v139, v78, v79\n"
    "v_mfma_f32_16x16x32_bf16 v[112:115], a[200:203], v[208:211], v[112:115]\n"
    "ds_read_b64_tr_b16 a[196:197], v8 offset:24832\n"
    "v_mfma_f32_16x16x32_bf16 v[116:119], a[200:203], v[212:215], v[116:119]\n"
    "ds_read_b64_tr_b16 a[198:199], v8 offset:26880\n"
    "v_mfma_f32_16x16x32_bf16 v[120:123], a[204:207], v[208:211], v[120:123]\n"
    "ds_read_b64_tr_b16 a[200:201], v8 offset:25088\n"
    "v_cvt_pk_bf16_f32 v140, v84, v85\n"
    "v_mfma_f32_16x16x32_bf16 v[124:127], a[204:207], v[212:215], v[124:127]\n"
    "ds_read_b64_tr_b16 a[202:203], v8 offset:27136\n"
    "v_cvt_pk_bf16_f32 v141, v86, v87\n"
    "v_mfma_f32_16x16x32_bf16 v[96:99], a[208:211], v[216:219], v[96:99]\n"
    "ds_read_b64_tr_b16 a[204:205], v8 offset:25344\n"
    "v_cvt_pk_bf16_f32 v142, v92, v93\n"
    "v_mfma_f32_16x16x32_bf16 v[100:103], a[208:211], v[220:223], v[100:103]\n"
    "ds_read_b64_tr_b16 a[206:207], v8 offset:27392\n"
    "v_cvt_pk_bf16_f32 v143, v94, v95\n"
    "v_mfma_f32_16x16x32_bf16 v[104:107], a[212:215], v[216:219], v[104:107]\n"
    "ds_read_b64_tr_b16 a[208:209], v8 offset:28672\n"
    "v_mfma_f32_16x16x32_bf16 v[108:111], a[212:215], v[220:223], v[108:111]\n"
    "ds_read_b64_tr_b16 a[210:211], v8 offset:30720\n"
    "v_mfma_f32_16x16x32_bf16 v[112:115], a[216:219], v[216:219], v[112:115]\n"
    "ds_read_b64_tr_b16 a[212:213], v8 offset:28928\n"
    "v_mfma_f32_16x16x32_bf16 v[116:119], a[216:219], v[220:223], v[116:119]\n"
    "buffer_store_dwordx4 v[132:135], v60, s[64:67], s71 offen offset:512 sc1\n"
    "v_mfma_f32_16x16x32_bf16 v[120:123], a[220:223], v[216:219], v[120:123]\n"
    "ds_read_b64_tr_b16 a[214:215], v8 offset:30976\n"
    "v_mfma_f32_16x16x32_bf16 v[124:127], a[220:223], v[220:223], v[124:127]\n"
    "ds_read_b64_tr_b16 a[216:217], v8 offset:29184\n"
    "v_mfma_f32_16x16x32_bf16 v[96:99], a[128:131], v[224:227], v[96:99]\n"
    "s_waitcnt vmcnt(26)\n"
    "s_barrier\n"
    "ds_read_b64_tr_b16 a[218:219], v8 offset:31232\n"
    "v_mfma_f32_16x16x32_bf16 v[100:103], a[128:131], v[228:231], v[100:103]\n"
    "ds_read_b64_tr_b16 a[220:221], v8 offset:29440\n"
    "v_mfma_f32_16x16x32_bf16 v[104:107], a[132:135], v[224:227], v[104:107]\n"
    "ds_read_b64_tr_b16 a[222:223], v8 offset:31488\n"
    "v_mfma_f32_16x16x32_bf16 v[108:111], a[132:135], v[228:231], v[108:111]\n"
    "ds_read_b64_tr_b16 a[128:129], v9 offset:0\n"
    "s_waitcnt lgkmcnt(14)\n"
    "v_mfma_f32_16x16x32_bf16 v[112:115], a[136:139], v[224:227], v[112:115]\n"
    "ds_read_b64_tr_b16 a[130:131], v9 offset:2048\n"
    "v_mfma_f32_16x16x32_bf16 v[116:119], a[136:139], v[228:231], v[116:119]\n"
    "ds_read_b64_tr_b16 a[132:133], v9 offset:256\n"
    "v_mfma_f32_16x16x32_bf16 v[120:123], a[140:143], v[224:227], v[120:123]\n"
    "ds_read_b64_tr_b16 a[134:135], v9 offset:2304\n"
    "v_mfma_f32_16x16x32_bf16 v[124:127], a[140:143], v[228:231], v[124:127]\n"
    "ds_read_b64_tr_b16 a[136:137], v9 offset:512\n"
    "v_mfma_f32_16x16x32_bf16 v[96:99], a[144:147], v[232:235], v[96:99]\n"
    "buffer_store_dwordx4 v[136:139], v61, s[64:67], s71 offen offset:0 sc1\n"
    "v_mfma_f32_16x16x32_bf16 v[100:103], a[144:147], v[236:239], v[100:103]\n"
    "ds_read_b64_tr_b16 a[138:139], v9 offset:2560\n"
    "v_mfma_f32_16x16x32_bf16 v[104:107], a[148:151], v[232:235], v[104:107]\n"
    "ds_read_b64_tr_b16 a[140:141], v9 offset:768\n"
    "v_mfma_f32_16x16x32_bf16 v[108:111], a[148:151], v[236:239], v[108:111]\n"
    "ds_read_b64_tr_b16 a[142:143], v9 offset:2816\n"
    "v_mfma_f32_16x16x32_bf16 v[112:115], a[152:155], v[232:235], v[112:115]\n"
    "ds_read_b64_tr_b16 a[144:145], v9 offset:4096\n"
    "v_mfma_f32_16x16x32_bf16 v[116:119], a[152:155], v[236:239], v[116:119]\n"
    "ds_read_b64_tr_b16 a[146:147], v9 offset:6144\n"
    "v_mfma_f32_16x16x32_bf16 v[120:123], a[156:159], v[232:235], v[120:123]\n"
    "ds_read_b64_tr_b16 a[148:149], v9 offset:4352\n"
    "v_mfma_f32_16x16x32_bf16 v[124:127], a[156:159], v[236:239], v[124:127]\n"
    "ds_read_b64_tr_b16 a[150:151], v9 offset:6400\n"
    "v_mfma_f32_16x16x32_bf16 v[96:99], a[192:195], v[240:243], v[96:99]\n"
    "ds_read_b64_tr_b16 a[152:153], v9 offset:4608\n"
    "v_mfma_f32_16x16x32_bf16 v[100:103], a[192:195], v[244:247], v[100:103]\n"
    "ds_read_b64_tr_b16 a[154:155], v9 offset:6656\n"
    "s_waitcnt lgkmcnt(14)\n"
    "v_mfma_f32_16x16x32_bf16 v[104:107], a[196:199], v[240:243], v[104:107]\n"
    "ds_read_b64_tr_b16 a[156:157], v9 offset:4864\n"
    "buffer_store_dwordx4 v[140:143], v61, s[64:67], s71 offen offset:512 sc1\n"
    "v_mfma_f32_16x16x32_bf16 v[108:111], a[196:199], v[244:247], v[108:111]\n"
    "s_add_u32 s71, s71, 0x400\n"
    "ds_read_b64_tr_b16 a[158:159], v9 offset:6912\n"
    "v_mfma_f32_16x16x32_bf16 v[112:115], a[200:203], v[240:243], v[112:115]\n"
    "ds_read_b64_tr_b16 a[192:193], v9 offset:8192\n"
    "v_mfma_f32_16x16x32_bf16 v[116:119], a[200:203], v[244:247], v[116:119]\n"
    "ds_read_b64_tr_b16 a[194:195], v9 offset:10240\n"
    "v_mfma_f32_16x16x32_bf16 v[120:123], a[204:207], v[240:243], v[120:123]\n"
    "ds_read_b64_tr_b16 a[196:197], v9 offset:8448\n"
    "v_mfma_f32_16x16x32_bf16 v[124:127], a[204:207], v[244:247], v[124:127]\n"
    "ds_read_b64_tr_b16 a[198:199], v9 offset:10496\n"
    "v_mfma_f32_16x16x32_bf16 v[96:99], a[208:211], v[248:251], v[96:99]\n"
    "ds_read_b64_tr_b16 a[200:201], v9 offset:8704\n"
    "v_mfma_f32_16x16x32_bf16 v[100:103], a[208:211], v[252:255], v[100:103]\n"
    "ds_read_b64_tr_b16 a[202:203], v9 offset:10752\n"
    "v_mfma_f32_16x16x32_bf16 v[104:107], a[212:215], v[248:251], v[104:107]\n"
    "ds_read_b64_tr_b16 a[204:205], v9 offset:8960\n"
    "v_mfma_f32_16x16x32_bf16 v[108:111], a[212:215], v[252:255], v[108:111]\n"
    "ds_read_b64_tr_b16 a[206:207], v9 offset:11008\n"
    "v_mfma_f32_16x16x32_bf16 v[112:115], a[216:219], v[248:251], v[112:115]\n"
    "ds_read_b64_tr_b16 a[208:209], v9 offset:12288\n"
    "v_mfma_f32_16x16x32_bf16 v[116:119], a[216:219], v[252:255], v[116:119]\n"
    "ds_read_b64_tr_b16 a[210:211], v9 offset:14336\n"
    "v_mfma_f32_16x16x32_bf16 v[120:123], a[220:223], v[248:251], v[120:123]\n"
    "ds_read_b64_tr_b16 a[212:213], v9 offset:12544\n"
    "v_mfma_f32_16x16x32_bf16 v[124:127], a[220:223], v[252:255], v[124:127]\n"
    "ds_read_b64_tr_b16 a[216:217], v9 offset:12800\n"
    "ds_read_b64_tr_b16 a[220:221], v9 offset:13056\n"
    "ds_read_b64_tr_b16 a[214:215], v9 offset:14592\n"
    "ds_read_b64_tr_b16 a[218:219], v9 offset:14848\n"
    "ds_read_b64_tr_b16 a[222:223], v9 offset:15104\n"
    "s_waitcnt lgkmcnt(14)\n"
    "v_mfma_f32_16x16x32_bf16 v[64:67], a[128:131], v[192:195], 0\n"
    "v_mfma_f32_16x16x32_bf16 v[68:71], a[128:131], v[196:199], 0\n"
    "ds_read_b64_tr_b16 a[128:129], v9 offset:16384\n"
    "v_mfma_f32_16x16x32_bf16 v[72:75], a[132:135], v[192:195], 0\n"
    "ds_read_b64_tr_b16 a[130:131], v9 offset:18432\n"
    "v_mfma_f32_16x16x32_bf16 v[76:79], a[132:135], v[196:199], 0\n"
    "ds_read_b64_tr_b16 a[132:133], v9 offset:16640\n"
    "v_mfma_f32_16x16x32_bf16 v[80:83], a[136:139], v[192:195], 0\n"
    "ds_read_b64_tr_b16 a[134:135], v9 offset:18688\n"
    "v_cvt_pk_bf16_f32 v128, v96, v97\n"
    "v_mfma_f32_16x16x32_bf16 v[84:87], a[136:139], v[196:199], 0\n"
    "ds_read_b64_tr_b16 a[136:137], v9 offset:16896\n"
    "v_cvt_pk_bf16_f32 v129, v98, v99\n"
    "v_mfma_f32_16x16x32_bf16 v[88:91], a[140:143], v[192:195], 0\n"
    "ds_read_b64_tr_b16 a[138:139], v9 offset:18944\n"
    "v_cvt_pk_bf16_f32 v130, v104, v105\n"
    "v_mfma_f32_16x16x32_bf16 v[92:95], a[140:143], v[196:199], 0\n"
    "ds_read_b64_tr_b16 a[140:141], v9 offset:17152\n"
    "v_cvt_pk_bf16_f32 v131, v106, v107\n"
    "v_mfma_f32_16x16x32_bf16 v[64:67], a[144:147], v[200:203], v[64:67]\n"
    "ds_read_b64_tr_b16 a[142:143], v9 offset:19200\n"
    "v_mfma_f32_16x16x32_bf16 v[68:71], a[144:147], v[204:207], v[68:71]\n"
    "ds_read_b64_tr_b16 a[144:145], v9 offset:20480\n"
    "v_mfma_f32_16x16x32_bf16 v[72:75], a[148:151], v[200:203], v[72:75]\n"
    "ds_read_b64_tr_b16 a[146:147], v9 offset:22528\n"
    "v_cvt_pk_bf16_f32 v132, v112, v113\n"
    "v_mfma_f32_16x16x32_bf16 v[76:79], a[148:151], v[204:207], v[76:79]\n"
    "ds_read_b64_tr_b16 a[148:149], v9 offset:20736\n"
    "v_cvt_pk_bf16_f32 v133, v114, v115\n"
    "v_mfma_f32_16x16x32_bf16 v[80:83], a[152:155], v[200:203], v[80:83]\n"
    "ds_read_b64_tr_b16 a[150:151], v9 offset:22784\n"
    "v_cvt_pk_bf16_f32 v134, v120, v121\n"
    "v_mfma_f32_16x16x32_bf16 v[84:87], a[152:155], v[204:207], v[84:87]\n"
    "ds_read_b64_tr_b16 a[152:153], v9 offset:20992\n"
    "v_cvt_pk_bf16_f32 v135, v122, v123\n"
    "v_mfma_f32_16x16x32_bf16 v[88:91], a[156:159], v[200:203], v[88:91]\n"
    "ds_read_b64_tr_b16 a[154:155], v9 offset:23040\n"
    "v_mfma_f32_16x16x32_bf16 v[92:95], a[156:159], v[204:207], v[92:95]\n"
    "ds_read_b64_tr_b16 a[156:157], v9 offset:21248\n"
    "v_mfma_f32_16x16x32_bf16 v[64:67], a[192:195], v[208:211], v[64:67]\n"
    "ds_read_b64_tr_b16 a[158:159], v9 offset:23296\n"
    "v_cvt_pk_bf16_f32 v136, v100, v101\n"
    "v_mfma_f32_16x16x32_bf16 v[68:71], a[192:195], v[212:215], v[68:71]\n"
    "ds_read_b64_tr_b16 a[192:193], v9 offset:24576\n"
    "v_cvt_pk_bf16_f32 v137, v102, v103\n"
    "s_waitcnt lgkmcnt(13)\n"
    "v_mfma_f32_16x16x32_bf16 v[72:75], a[196:199], v[208:211], v[72:75]\n"
    "buffer_store_dwordx4 v[128:131], v60, s[64:67], s71 offen offset:0 sc1\n"
    "v_cvt_pk_bf16_f32 v138, v108, v109\n"
    "v_mfma_f32_16x16x32_bf16 v[76:79], a[196:199], v[212:215], v[76:79]\n"
    "ds_read_b64_tr_b16 a[194:195], v9 offset:26624\n"
    "v_cvt_pk_bf16_f32 v139, v110, v111\n"
    "v_mfma_f32_16x16x32_bf16 v[80:83], a[200:203], v[208:211], v[80:83]\n"
    "ds_read_b64_tr_b16 a[196:197], v9 offset:24832\n"
    "v_mfma_f32_16x16x32_bf16 v[84:87], a[200:203], v[212:215], v[84:87]\n"
    "ds_read_b64_tr_b16 a[198:199], v9 offset:26880\n"
    "v_mfma_f32_16x16x32_bf16 v[88:91], a[204:207], v[208:211], v[88:91]\n"
    "ds_read_b64_tr_b16 a[200:201], v9 offset:25088\n"
    "v_cvt_pk_bf16_f32 v140, v116, v117\n"
    "v_mfma_f32_16x16x32_bf16 v[92:95], a[204:207], v[212:215], v[92:95]\n"
    "ds_read_b64_tr_b16 a[202:203], v9 offset:27136\n"
    "v_cvt_pk_bf16_f32 v141, v118, v119\n"
    "v_mfma_f32_16x16x32_bf16 v[64:67], a[208:211], v[216:219], v[64:67]\n"
    "ds_read_b64_tr_b16 a[204:205], v9 offset:25344\n"
    "v_cvt_pk_bf16_f32 v142, v124, v125\n"
    "v_mfma_f32_16x16x32_bf16 v[68:71], a[208:211], v[220:223], v[68:71]\n"
    "ds_read_b64_tr_b16 a[206:207], v9 offset:27392\n"
    "v_cvt_pk_bf16_f32 v143, v126, v127\n"
    "v_mfma_f32_16x16x32_bf16 v[72:75], a[212:215], v[216:219], v[72:75]\n"
    "ds_read_b64_tr_b16 a[208:209], v9 offset:28672\n"
    "v_mfma_f32_16x16x32_bf16 v[76:79], a[212:215], v[220:223], v[76:79]\n"
    "ds_read_b64_tr_b16 a[210:211], v9 offset:30720\n"
    "v_mfma_f32_16x16x32_bf16 v[80:83], a[216:219], v[216:219], v[80:83]\n"
    "ds_read_b64_tr_b16 a[212:213], v9 offset:28928\n"
    "v_mfma_f32_16x16x32_bf16 v[84:87], a[216:219], v[220:223], v[84:87]\n"
    "buffer_store_dwordx4 v[132:135], v60, s[64:67], s71 offen offset:512 sc1\n"
    "v_mfma_f32_16x16x32_bf16 v[88:91], a[220:223], v[216:219], v[88:91]\n"
    "ds_read_b64_tr_b16 a[214:215], v9 offset:30976\n"
    "v_mfma_f32_16x16x32_bf16 v[92:95], a[220:223], v[220:223], v[92:95]\n"
    "ds_read_b64_tr_b16 a[216:217], v9 offset:29184\n"
    "v_mfma_f32_16x16x32_bf16 v[64:67], a[128:131], v[224:227], v[64:67]\n"
    "s_waitcnt vmcnt(18)\n"
    "s_barrier\n"
    "ds_read_b64_tr_b16 a[218:219], v9 offset:31232\n"
    "v_mfma_f32_16x16x32_bf16 v[68:71], a[128:131], v[228:231], v[68:71]\n"
    "ds_read_b64_tr_b16 a[220:221], v9 offset:29440\n"
    "v_mfma_f32_16x16x32_bf16 v[72:75], a[132:135], v[224:227], v[72:75]\n"
    "ds_read_b64_tr_b16 a[222:223], v9 offset:31488\n"
    "v_mfma_f32_16x16x32_bf16 v[76:79], a[132:135], v[228:231], v[76:79]\n"
    "ds_read_b64_tr_b16 a[128:129], v10 offset:0\n"
    "s_waitcnt lgkmcnt(14)\n"
    "v_mfma_f32_16x16x32_bf16 v[80:83], a[136:139], v[224:227], v[80:83]\n"
    "ds_read_b64_tr_b16 a[130:131], v10 offset:2048\n"
    "v_mfma_f32_16x16x32_bf16 v[84:87], a[136:139], v[228:231], v[84:87]\n"
    "ds_read_b64_tr_b16 a[132:133], v10 offset:256\n"
    "v_mfma_f32_16x16x32_bf16 v[88:91], a[140:143], v[224:227], v[88:91]\n"
    "ds_read_b64_tr_b16 a[134:135], v10 offset:2304\n"
    "v_mfma_f32_16x16x32_bf16 v[92:95], a[140:143], v[228:231], v[92:95]\n"
    "ds_read_b64_tr_b16 a[136:137], v10 offset:512\n"
    "v_mfma_f32_16x16x32_bf16 v[64:67], a[144:147], v[232:235], v[64:67]\n"
    "buffer_store_dwordx4 v[136:139], v61, s[64:67], s71 offen offset:0 sc1\n"
    "v_mfma_f32_16x16x32_bf16 v[68:71], a[144:147], v[236:239], v[68:71]\n"
    "ds_read_b64_tr_b16 a[138:139], v10 offset:2560\n"
    "v_mfma_f32_16x16x32_bf16 v[72:75], a[148:151], v[232:235], v[72:75]\n"
    "ds_read_b64_tr_b16 a[140:141], v10 offset:768\n"
    "v_mfma_f32_16x16x32_bf16 v[76:79], a[148:151], v[236:239], v[76:79]\n"
    "ds_read_b64_tr_b16 a[142:143], v10 offset:2816\n"
    "v_mfma_f32_16x16x32_bf16 v[80:83], a[152:155], v[232:235], v[80:83]\n"
    "ds_read_b64_tr_b16 a[144:145], v10 offset:4096\n"
    "v_mfma_f32_16x16x32_bf16 v[84:87], a[152:155], v[236:239], v[84:87]\n"
    "ds_read_b64_tr_b16 a[146:147], v10 offset:6144\n"
    "v_mfma_f32_16x16x32_bf16 v[88:91], a[156:159], v[232:235], v[88:91]\n"
    "ds_read_b64_tr_b16 a[148:149], v10 offset:4352\n"
    "v_mfma_f32_16x16x32_bf16 v[92:95], a[156:159], v[236:239], v[92:95]\n"
    "ds_read_b64_tr_b16 a[150:151], v10 offset:6400\n"
    "v_mfma_f32_16x16x32_bf16 v[64:67], a[192:195], v[240:243], v[64:67]\n"
    "ds_read_b64_tr_b16 a[152:153], v10 offset:4608\n"
    "v_mfma_f32_16x16x32_bf16 v[68:71], a[192:195], v[244:247], v[68:71]\n"
    "ds_read_b64_tr_b16 a[154:155], v10 offset:6656\n"
    "s_waitcnt lgkmcnt(14)\n"
    "v_mfma_f32_16x16x32_bf16 v[72:75], a[196:199], v[240:243], v[72:75]\n"
    "ds_read_b64_tr_b16 a[156:157], v10 offset:4864\n"
    "buffer_store_dwordx4 v[140:143], v61, s[64:67], s71 offen offset:512 sc1\n"
    "v_mfma_f32_16x16x32_bf16 v[76:79], a[196:199], v[244:247], v[76:79]\n"
    "s_add_u32 s71, s71, 0x400\n"
    "ds_read_b64_tr_b16 a[158:159], v10 offset:6912\n"
    "v_mfma_f32_16x16x32_bf16 v[80:83], a[200:203], v[240:243], v[80:83]\n"
    "ds_read_b64_tr_b16 a[192:193], v10 offset:8192\n"
    "v_mfma_f32_16x16x32_bf16 v[84:87], a[200:203], v[244:247], v[84:87]\n"
    "ds_read_b64_tr_b16 a[194:195], v10 offset:10240\n"
    "v_mfma_f32_16x16x32_bf16 v[88:91], a[204:207], v[240:243], v[88:91]\n"
    "ds_read_b64_tr_b16 a[196:197], v10 offset:8448\n"
    "v_mfma_f32_16x16x32_bf16 v[92:95], a[204:207], v[244:247], v[92:95]\n"
    "ds_read_b64_tr_b16 a[198:199], v10 offset:10496\n"
    "v_mfma_f32_16x16x32_bf16 v[64:67], a[208:211], v[248:251], v[64:67]\n"
    "ds_read_b64_tr_b16 a[200:201], v10 offset:8704\n"
    "v_mfma_f32_16x16x32_bf16 v[68:71], a[208:211], v[252:255], v[68:71]\n"
    "ds_read_b64_tr_b16 a[202:203], v10 offset:10752\n"
    "v_mfma_f32_16x16x32_bf16 v[72:75], a[212:215], v[248:251], v[72:75]\n"
    "ds_read_b64_tr_b16 a[204:205], v10 offset:8960\n"
    "v_mfma_f32_16x16x32_bf16 v[76:79], a[212:215], v[252:255], v[76:79]\n"
    "ds_read_b64_tr_b16 a[206:207], v10 offset:11008\n"
    "v_mfma_f32_16x16x32_bf16 v[80:83], a[216:219], v[248:251], v[80:83]\n"
    "ds_read_b64_tr_b16 a[208:209], v10 offset:12288\n"
    "v_mfma_f32_16x16x32_bf16 v[84:87], a[216:219], v[252:255], v[84:87]\n"
    "ds_read_b64_tr_b16 a[210:211], v10 offset:14336\n"
    "v_mfma_f32_16x16x32_bf16 v[88:91], a[220:223], v[248:251], v[88:91]\n"
    "ds_read_b64_tr_b16 a[212:213], v10 offset:12544\n"
    "v_mfma_f32_16x16x32_bf16 v[92:95], a[220:223], v[252:255], v[92:95]\n"
    "ds_read_b64_tr_b16 a[216:217], v10 offset:12800\n"
    "ds_read_b64_tr_b16 a[220:221], v10 offset:13056\n"
    "ds_read_b64_tr_b16 a[214:215], v10 offset:14592\n"
    "ds_read_b64_tr_b16 a[218:219], v10 offset:14848\n"
    "ds_read_b64_tr_b16 a[222:223], v10 offset:15104\n"
    "s_waitcnt lgkmcnt(14)\n"
    "v_mfma_f32_16x16x32_bf16 v[96:99], a[128:131], v[192:195], 0\n"
    "v_mfma_f32_16x16x32_bf16 v[100:103], a[128:131], v[196:199], 0\n"
    "ds_read_b64_tr_b16 a[128:129], v10 offset:16384\n"
    "v_mfma_f32_16x16x32_bf16 v[104:107], a[132:135], v[192:195], 0\n"
    "ds_read_b64_tr_b16 a[130:131], v10 offset:18432\n"
    "v_mfma_f32_16x16x32_bf16 v[108:111], a[132:135], v[196:199], 0\n"
    "ds_read_b64_tr_b16 a[132:133], v10 offset:16640\n"
    "v_mfma_f32_16x16x32_bf16 v[112:115], a[136:139], v[192:195], 0\n"
    "ds_read_b64_tr_b16 a[134:135], v10 offset:18688\n"
    "v_cvt_pk_bf16_f32 v128, v64, v65\n"
    "v_mfma_f32_16x16x32_bf16 v[116:119], a[136:139], v[196:199], 0\n"
    "ds_read_b64_tr_b16 a[136:137], v10 offset:16896\n"
    "v_cvt_pk_bf16_f32 v129, v66, v67\n"
    "v_mfma_f32_16x16x32_bf16 v[120:123], a[140:143], v[192:195], 0\n"
    "ds_read_b64_tr_b16 a[138:139], v10 offset:18944\n"
    "v_cvt_pk_bf16_f32 v130, v72, v73\n"
    "v_mfma_f32_16x16x32_bf16 v[124:127], a[140:143], v[196:199], 0\n"
    "ds_read_b64_tr_b16 a[140:141], v10 offset:17152\n"
    "v_cvt_pk_bf16_f32 v131, v74, v75\n"
    "v_mfma_f32_16x16x32_bf16 v[96:99], a[144:147], v[200:203], v[96:99]\n"
    "ds_read_b64_tr_b16 a[142:143], v10 offset:19200\n"
    "v_mfma_f32_16x16x32_bf16 v[100:103], a[144:147], v[204:207], v[100:103]\n"
    "ds_read_b64_tr_b16 a[144:145], v10 offset:20480\n"
    "v_mfma_f32_16x16x32_bf16 v[104:107], a[148:151], v[200:203], v[104:107]\n"
    "ds_read_b64_tr_b16 a[146:147], v10 offset:22528\n"
    "v_cvt_pk_bf16_f32 v132, v80, v81\n"
    "v_mfma_f32_16x16x32_bf16 v[108:111], a[148:151], v[204:207], v[108:111]\n"
    "ds_read_b64_tr_b16 a[148:149], v10 offset:20736\n"
    "v_cvt_pk_bf16_f32 v133, v82, v83\n"
    "v_mfma_f32_16x16x32_bf16 v[112:115], a[152:155], v[200:203], v[112:115]\n"
    "ds_read_b64_tr_b16 a[150:151], v10 offset:22784\n"
    "v_cvt_pk_bf16_f32 v134, v88, v89\n"
    "v_mfma_f32_16x16x32_bf16 v[116:119], a[152:155], v[204:207], v[116:119]\n"
    "ds_read_b64_tr_b16 a[152:153], v10 offset:20992\n"
    "v_cvt_pk_bf16_f32 v135, v90, v91\n"
    "v_mfma_f32_16x16x32_bf16 v[120:123], a[156:159], v[200:203], v[120:123]\n"
    "ds_read_b64_tr_b16 a[154:155], v10 offset:23040\n"
    "v_mfma_f32_16x16x32_bf16 v[124:127], a[156:159], v[204:207], v[124:127]\n"
    "ds_read_b64_tr_b16 a[156:157], v10 offset:21248\n"
    "v_mfma_f32_16x16x32_bf16 v[96:99], a[192:195], v[208:211], v[96:99]\n"
    "ds_read_b64_tr_b16 a[158:159], v10 offset:23296\n"
    "v_cvt_pk_bf16_f32 v136, v68, v69\n"
    "v_mfma_f32_16x16x32_bf16 v[100:103], a[192:195], v[212:215], v[100:103]\n"
    "ds_read_b64_tr_b16 a[192:193], v10 offset:24576\n"
    "v_cvt_pk_bf16_f32 v137, v70, v71\n"
    "s_waitcnt lgkmcnt(13)\n"
    "v_mfma_f32_16x16x32_bf16 v[104:107], a[196:199], v[208:211], v[104:107]\n"
    "buffer_store_dwordx4 v[128:131], v60, s[64:67], s71 offen offset:0 sc1\n"
    "v_cvt_pk_bf16_f32 v138, v76, v77\n"
    "v_mfma_f32_16x16x32_bf16 v[108:111], a[196:199], v[212:215], v[108:111]\n"
    "ds_read_b64_tr_b16 a[194:195], v10 offset:26624\n"
    "v_cvt_pk_bf16_f32 v139, v78, v79\n"
    "v_mfma_f32_16x16x32_bf16 v[112:115], a[200:203], v[208:211], v[112:115]\n"
    "ds_read_b64_tr_b16 a[196:197], v10 offset:24832\n"
    "v_mfma_f32_16x16x32_bf16 v[116:119], a[200:203], v[212:215], v[116:119]\n"
    "ds_read_b64_tr_b16 a[198:199], v10 offset:26880\n"
    "v_mfma_f32_16x16x32_bf16 v[120:123], a[204:207], v[208:211], v[120:123]\n"
    "ds_read_b64_tr_b16 a[200:201], v10 offset:25088\n"
    "v_cvt_pk_bf16_f32 v140, v84, v85\n"
    "v_mfma_f32_16x16x32_bf16 v[124:127], a[204:207], v[212:215], v[124:127]\n"
    "ds_read_b64_tr_b16 a[202:203], v10 offset:27136\n"
    "v_cvt_pk_bf16_f32 v141, v86, v87\n"
    "v_mfma_f32_16x16x32_bf16 v[96:99], a[208:211], v[216:219], v[96:99]\n"
    "ds_read_b64_tr_b16 a[204:205], v10 offset:25344\n"
    "v_cvt_pk_bf16_f32 v142, v92, v93\n"
    "v_mfma_f32_16x16x32_bf16 v[100:103], a[208:211], v[220:223], v[100:103]\n"
    "ds_read_b64_tr_b16 a[206:207], v10 offset:27392\n"
    "v_cvt_pk_bf16_f32 v143, v94, v95\n"
    "v_mfma_f32_16x16x32_bf16 v[104:107], a[212:215], v[216:219], v[104:107]\n"
    "ds_read_b64_tr_b16 a[208:209], v10 offset:28672\n"
    "v_mfma_f32_16x16x32_bf16 v[108:111], a[212:215], v[220:223], v[108:111]\n"
    "ds_read_b64_tr_b16 a[210:211], v10 offset:30720\n"
    "v_mfma_f32_16x16x32_bf16 v[112:115], a[216:219], v[216:219], v[112:115]\n"
    "ds_read_b64_tr_b16 a[212:213], v10 offset:28928\n"
    "v_mfma_f32_16x16x32_bf16 v[116:119], a[216:219], v[220:223], v[116:119]\n"
    "buffer_store_dwordx4 v[132:135], v60, s[64:67], s71 offen offset:512 sc1\n"
    "v_mfma_f32_16x16x32_bf16 v[120:123], a[220:223], v[216:219], v[120:123]\n"
    "ds_read_b64_tr_b16 a[214:215], v10 offset:30976\n"
    "v_mfma_f32_16x16x32_bf16 v[124:127], a[220:223], v[220:223], v[124:127]\n"
    "ds_read_b64_tr_b16 a[216:217], v10 offset:29184\n"
    "v_mfma_f32_16x16x32_bf16 v[96:99], a[128:131], v[224:227], v[96:99]\n"
    "s_waitcnt vmcnt(10)\n"
    "s_barrier\n"
    "ds_read_b64_tr_b16 a[218:219], v10 offset:31232\n"
    "v_mfma_f32_16x16x32_bf16 v[100:103], a[128:131], v[228:231], v[100:103]\n"
    "ds_read_b64_tr_b16 a[220:221], v10 offset:29440\n"
    "v_mfma_f32_16x16x32_bf16 v[104:107], a[132:135], v[224:227], v[104:107]\n"
    "ds_read_b64_tr_b16 a[222:223], v10 offset:31488\n"
    "v_mfma_f32_16x16x32_bf16 v[108:111], a[132:135], v[228:231], v[108:111]\n"
    "ds_read_b64_tr_b16 a[128:129], v11 offset:0\n"
    "s_waitcnt lgkmcnt(14)\n"
    "v_mfma_f32_16x16x32_bf16 v[112:115], a[136:139], v[224:227], v[112:115]\n"
    "ds_read_b64_tr_b16 a[130:131], v11 offset:2048\n"
    "v_mfma_f32_16x16x32_bf16 v[116:119], a[136:139], v[228:231], v[116:119]\n"
    "ds_read_b64_tr_b16 a[132:133], v11 offset:256\n"
    "v_mfma_f32_16x16x32_bf16 v[120:123], a[140:143], v[224:227], v[120:123]\n"
    "ds_read_b64_tr_b16 a[134:135], v11 offset:2304\n"
    "v_mfma_f32_16x16x32_bf16 v[124:127], a[140:143], v[228:231], v[124:127]\n"
    "ds_read_b64_tr_b16 a[136:137], v11 offset:512\n"
    "v_mfma_f32_16x16x32_bf16 v[96:99], a[144:147], v[232:235], v[96:99]\n"
    "buffer_store_dwordx4 v[136:139], v61, s[64:67], s71 offen offset:0 sc1\n"
    "v_mfma_f32_16x16x32_bf16 v[100:103], a[144:147], v[236:239], v[100:103]\n"
    "ds_read_b64_tr_b16 a[138:139], v11 offset:2560\n"
    "v_mfma_f32_16x16x32_bf16 v[104:107], a[148:151], v[232:235], v[104:107]\n"
    "ds_read_b64_tr_b16 a[140:141], v11 offset:768\n"
    "v_mfma_f32_16x16x32_bf16 v[108:111], a[148:151], v[236:239], v[108:111]\n"
    "ds_read_b64_tr_b16 a[142:143], v11 offset:2816\n"
    "v_mfma_f32_16x16x32_bf16 v[112:115], a[152:155], v[232:235], v[112:115]\n"
    "ds_read_b64_tr_b16 a[144:145], v11 offset:4096\n"
    "v_mfma_f32_16x16x32_bf16 v[116:119], a[152:155], v[236:239], v[116:119]\n"
    "ds_read_b64_tr_b16 a[146:147], v11 offset:6144\n"
    "v_mfma_f32_16x16x32_bf16 v[120:123], a[156:159], v[232:235], v[120:123]\n"
    "ds_read_b64_tr_b16 a[148:149], v11 offset:4352\n"
    "v_mfma_f32_16x16x32_bf16 v[124:127], a[156:159], v[236:239], v[124:127]\n"
    "ds_read_b64_tr_b16 a[150:151], v11 offset:6400\n"
    "v_mfma_f32_16x16x32_bf16 v[96:99], a[192:195], v[240:243], v[96:99]\n"
    "ds_read_b64_tr_b16 a[152:153], v11 offset:4608\n"
    "v_mfma_f32_16x16x32_bf16 v[100:103], a[192:195], v[244:247], v[100:103]\n"
    "ds_read_b64_tr_b16 a[154:155], v11 offset:6656\n"
    "s_waitcnt lgkmcnt(14)\n"
    "v_mfma_f32_16x16x32_bf16 v[104:107], a[196:199], v[240:243], v[104:107]\n"
    "ds_read_b64_tr_b16 a[156:157], v11 offset:4864\n"
    "buffer_store_dwordx4 v[140:143], v61, s[64:67], s71 offen offset:512 sc1\n"
    "v_mfma_f32_16x16x32_bf16 v[108:111], a[196:199], v[244:247], v[108:111]\n"
    "s_add_u32 s71, s71, 0x400\n"
    "ds_read_b64_tr_b16 a[158:159], v11 offset:6912\n"
    "v_mfma_f32_16x16x32_bf16 v[112:115], a[200:203], v[240:243], v[112:115]\n"
    "ds_read_b64_tr_b16 a[192:193], v11 offset:8192\n"
    "v_mfma_f32_16x16x32_bf16 v[116:119], a[200:203], v[244:247], v[116:119]\n"
    "ds_read_b64_tr_b16 a[194:195], v11 offset:10240\n"
    "v_mfma_f32_16x16x32_bf16 v[120:123], a[204:207], v[240:243], v[120:123]\n"
    "ds_read_b64_tr_b16 a[196:197], v11 offset:8448\n"
    "v_mfma_f32_16x16x32_bf16 v[124:127], a[204:207], v[244:247], v[124:127]\n"
    "ds_read_b64_tr_b16 a[198:199], v11 offset:10496\n"
    "v_mfma_f32_16x16x32_bf16 v[96:99], a[208:211], v[248:251], v[96:99]\n"
    "ds_read_b64_tr_b16 a[200:201], v11 offset:8704\n"
    "v_mfma_f32_16x16x32_bf16 v[100:103], a[208:211], v[252:255], v[100:103]\n"
    "ds_read_b64_tr_b16 a[202:203], v11 offset:10752\n"
    "v_mfma_f32_16x16x32_bf16 v[104:107], a[212:215], v[248:251], v[104:107]\n"
    "ds_read_b64_tr_b16 a[204:205], v11 offset:8960\n"
    "v_mfma_f32_16x16x32_bf16 v[108:111], a[212:215], v[252:255], v[108:111]\n"
    "ds_read_b64_tr_b16 a[206:207], v11 offset:11008\n"
    "v_mfma_f32_16x16x32_bf16 v[112:115], a[216:219], v[248:251], v[112:115]\n"
    "ds_read_b64_tr_b16 a[208:209], v11 offset:12288\n"
    "v_mfma_f32_16x16x32_bf16 v[116:119], a[216:219], v[252:255], v[116:119]\n"
    "ds_read_b64_tr_b16 a[210:211], v11 offset:14336\n"
    "v_mfma_f32_16x16x32_bf16 v[120:123], a[220:223], v[248:251], v[120:123]\n"
    "ds_read_b64_tr_b16 a[212:213], v11 offset:12544\n"
    "v_mfma_f32_16x16x32_bf16 v[124:127], a[220:223], v[252:255], v[124:127]\n"
    "ds_read_b64_tr_b16 a[216:217], v11 offset:12800\n"
    "ds_read_b64_tr_b16 a[220:221], v11 offset:13056\n"
    "ds_read_b64_tr_b16 a[214:215], v11 offset:14592\n"
    "ds_read_b64_tr_b16 a[218:219], v11 offset:14848\n"
    "ds_read_b64_tr_b16 a[222:223], v11 offset:15104\n"
    "s_waitcnt lgkmcnt(14)\n"
    "v_mfma_f32_16x16x32_bf16 v[64:67], a[128:131], v[192:195], 0\n"
    "v_mfma_f32_16x16x32_bf16 v[68:71], a[128:131], v[196:199], 0\n"
    "ds_read_b64_tr_b16 a[128:129], v11 offset:16384\n"
    "v_mfma_f32_16x16x32_bf16 v[72:75], a[132:135], v[192:195], 0\n"
    "ds_read_b64_tr_b16 a[130:131], v11 offset:18432\n"
    "v_mfma_f32_16x16x32_bf16 v[76:79], a[132:135], v[196:199], 0\n"
    "ds_read_b64_tr_b16 a[132:133], v11 offset:16640\n"
    "v_mfma_f32_16x16x32_bf16 v[80:83], a[136:139], v[192:195], 0\n"
    "ds_read_b64_tr_b16 a[134:135], v11 offset:18688\n"
    "v_cvt_pk_bf16_f32 v128, v96, v97\n"
    "v_mfma_f32_16x16x32_bf16 v[84:87], a[136:139], v[196:199], 0\n"
    "ds_read_b64_tr_b16 a[136:137], v11 offset:16896\n"
    "v_cvt_pk_bf16_f32 v129, v98, v99\n"
    "v_mfma_f32_16x16x32_bf16 v[88:91], a[140:143], v[192:195], 0\n"
    "ds_read_b64_tr_b16 a[138:139], v11 offset:18944\n"
    "v_cvt_pk_bf16_f32 v130, v104, v105\n"
    "v_mfma_f32_16x16x32_bf16 v[92:95], a[140:143], v[196:199], 0\n"
    "ds_read_b64_tr_b16 a[140:141], v11 offset:17152\n"
    "v_cvt_pk_bf16_f32 v131, v106, v107\n"
    "v_mfma_f32_16x16x32_bf16 v[64:67], a[144:147], v[200:203], v[64:67]\n"
    "ds_read_b64_tr_b16 a[142:143], v11 offset:19200\n"
    "v_mfma_f32_16x16x32_bf16 v[68:71], a[144:147], v[204:207], v[68:71]\n"
    "ds_read_b64_tr_b16 a[144:145], v11 offset:20480\n"
    "v_mfma_f32_16x16x32_bf16 v[72:75], a[148:151], v[200:203], v[72:75]\n"
    "ds_read_b64_tr_b16 a[146:147], v11 offset:22528\n"
    "v_cvt_pk_bf16_f32 v132, v112, v113\n"
    "v_mfma_f32_16x16x32_bf16 v[76:79], a[148:151], v[204:207], v[76:79]\n"
    "ds_read_b64_tr_b16 a[148:149], v11 offset:20736\n"
    "v_cvt_pk_bf16_f32 v133, v114, v115\n"
    "v_mfma_f32_16x16x32_bf16 v[80:83], a[152:155], v[200:203], v[80:83]\n"
    "ds_read_b64_tr_b16 a[150:151], v11 offset:22784\n"
    "v_cvt_pk_bf16_f32 v134, v120, v121\n"
    "v_mfma_f32_16x16x32_bf16 v[84:87], a[152:155], v[204:207], v[84:87]\n"
    "ds_read_b64_tr_b16 a[152:153], v11 offset:20992\n"
    "v_cvt_pk_bf16_f32 v135, v122, v123\n"
    "v_mfma_f32_16x16x32_bf16 v[88:91], a[156:159], v[200:203], v[88:91]\n"
    "ds_read_b64_tr_b16 a[154:155], v11 offset:23040\n"
    "v_mfma_f32_16x16x32_bf16 v[92:95], a[156:159], v[204:207], v[92:95]\n"
    "ds_read_b64_tr_b16 a[156:157], v11 offset:21248\n"
    "v_mfma_f32_16x16x32_bf16 v[64:67], a[192:195], v[208:211], v[64:67]\n"
    "ds_read_b64_tr_b16 a[158:159], v11 offset:23296\n"
    "v_cvt_pk_bf16_f32 v136, v100, v101\n"
    "v_mfma_f32_16x16x32_bf16 v[68:71], a[192:195], v[212:215], v[68:71]\n"
    "ds_read_b64_tr_b16 a[192:193], v11 offset:24576\n"
    "v_cvt_pk_bf16_f32 v137, v102, v103\n"
    "s_waitcnt lgkmcnt(13)\n"
    "v_mfma_f32_16x16x32_bf16 v[72:75], a[196:199], v[208:211], v[72:75]\n"
    "buffer_store_dwordx4 v[128:131], v60, s[64:67], s71 offen offset:0 sc1\n"
    "v_cvt_pk_bf16_f32 v138, v108, v109\n"
    "v_mfma_f32_16x16x32_bf16 v[76:79], a[196:199], v[212:215], v[76:79]\n"
    "ds_read_b64_tr_b16 a[194:195], v11 offset:26624\n"
    "v_cvt_pk_bf16_f32 v139, v110, v111\n"
    "v_mfma_f32_16x16x32_bf16 v[80:83], a[200:203], v[208:211], v[80:83]\n"
    "ds_read_b64_tr_b16 a[196:197], v11 offset:24832\n"
    "v_mfma_f32_16x16x32_bf16 v[84:87], a[200:203], v[212:215], v[84:87]\n"
    "ds_read_b64_tr_b16 a[198:199], v11 offset:26880\n"
    "v_mfma_f32_16x16x32_bf16 v[88:91], a[204:207], v[208:211], v[88:91]\n"
    "ds_read_b64_tr_b16 a[200:201], v11 offset:25088\n"
    "v_cvt_pk_bf16_f32 v140, v116, v117\n"
    "v_mfma_f32_16x16x32_bf16 v[92:95], a[204:207], v[212:215], v[92:95]\n"
    "ds_read_b64_tr_b16 a[202:203], v11 offset:27136\n"
    "v_cvt_pk_bf16_f32 v141, v118, v119\n"
    "v_mfma_f32_16x16x32_bf16 v[64:67], a[208:211], v[216:219], v[64:67]\n"
    "ds_read_b64_tr_b16 a[204:205], v11 offset:25344\n"
    "v_cvt_pk_bf16_f32 v142, v124, v125\n"
    "v_mfma_f32_16x16x32_bf16 v[68:71], a[208:211], v[220:223], v[68:71]\n"
    "ds_read_b64_tr_b16 a[206:207], v11 offset:27392\n"
    "v_cvt_pk_bf16_f32 v143, v126, v127\n"
    "v_mfma_f32_16x16x32_bf16 v[72:75], a[212:215], v[216:219], v[72:75]\n"
    "ds_read_b64_tr_b16 a[208:209], v11 offset:28672\n"
    "v_mfma_f32_16x16x32_bf16 v[76:79], a[212:215], v[220:223], v[76:79]\n"
    "ds_read_b64_tr_b16 a[210:211], v11 offset:30720\n"
    "v_mfma_f32_16x16x32_bf16 v[80:83], a[216:219], v[216:219], v[80:83]\n"
    "ds_read_b64_tr_b16 a[212:213], v11 offset:28928\n"
    "v_mfma_f32_16x16x32_bf16 v[84:87], a[216:219], v[220:223], v[84:87]\n"
    "buffer_store_dwordx4 v[132:135], v60, s[64:67], s71 offen offset:512 sc1\n"
    "v_mfma_f32_16x16x32_bf16 v[88:91], a[220:223], v[216:219], v[88:91]\n"
    "ds_read_b64_tr_b16 a[214:215], v11 offset:30976\n"
    "v_mfma_f32_16x16x32_bf16 v[92:95], a[220:223], v[220:223], v[92:95]\n"
    "ds_read_b64_tr_b16 a[216:217], v11 offset:29184\n"
    "v_mfma_f32_16x16x32_bf16 v[64:67], a[128:131], v[224:227], v[64:67]\n"
    "ds_read_b64_tr_b16 a[218:219], v11 offset:31232\n"
    "v_mfma_f32_16x16x32_bf16 v[68:71], a[128:131], v[228:231], v[68:71]\n"
    "ds_read_b64_tr_b16 a[220:221], v11 offset:29440\n"
    "v_mfma_f32_16x16x32_bf16 v[72:75], a[132:135], v[224:227], v[72:75]\n"
    "ds_read_b64_tr_b16 a[222:223], v11 offset:31488\n"
    "v_mfma_f32_16x16x32_bf16 v[76:79], a[132:135], v[228:231], v[76:79]\n"
    "buffer_store_dwordx4 v[136:139], v61, s[64:67], s71 offen offset:0 sc1\n"
    "s_waitcnt lgkmcnt(14)\n"
    "v_mfma_f32_16x16x32_bf16 v[80:83], a[136:139], v[224:227], v[80:83]\n"
    "buffer_store_dwordx4 v[140:143], v61, s[64:67], s71 offen offset:512 sc1\n"
    "v_mfma_f32_16x16x32_bf16 v[84:87], a[136:139], v[228:231], v[84:87]\n"
    "s_add_u32 s71, s71, 0x400\n"
    "v_mfma_f32_16x16x32_bf16 v[88:91], a[140:143], v[224:227], v[88:91]\n"
    "v_mfma_f32_16x16x32_bf16 v[92:95], a[140:143], v[228:231], v[92:95]\n"
    "v_mfma_f32_16x16x32_bf16 v[64:67], a[144:147], v[232:235], v[64:67]\n"
    "v_mfma_f32_16x16x32_bf16 v[68:71], a[144:147], v[236:239], v[68:71]\n"
    "v_mfma_f32_16x16x32_bf16 v[72:75], a[148:151], v[232:235], v[72:75]\n"
    "v_mfma_f32_16x16x32_bf16 v[76:79], a[148:151], v[236:239], v[76:79]\n"
    "v_mfma_f32_16x16x32_bf16 v[80:83], a[152:155], v[232:235], v[80:83]\n"
    "v_mfma_f32_16x16x32_bf16 v[84:87], a[152:155], v[236:239], v[84:87]\n"
    "v_mfma_f32_16x16x32_bf16 v[88:91], a[156:159], v[232:235], v[88:91]\n"
    "v_mfma_f32_16x16x32_bf16 v[92:95], a[156:159], v[236:239], v[92:95]\n"
    "v_mfma_f32_16x16x32_bf16 v[64:67], a[192:195], v[240:243], v[64:67]\n"
    "v_mfma_f32_16x16x32_bf16 v[68:71], a[192:195], v[244:247], v[68:71]\n"
    "s_waitcnt lgkmcnt(8)\n"
    "v_mfma_f32_16x16x32_bf16 v[72:75], a[196:199], v[240:243], v[72:75]\n"
    "v_mfma_f32_16x16x32_bf16 v[76:79], a[196:199], v[244:247], v[76:79]\n"
    "v_mfma_f32_16x16x32_bf16 v[80:83], a[200:203], v[240:243], v[80:83]\n"
    "v_mfma_f32_16x16x32_bf16 v[84:87], a[200:203], v[244:247], v[84:87]\n"
    "v_mfma_f32_16x16x32_bf16 v[88:91], a[204:207], v[240:243], v[88:91]\n"
    "v_mfma_f32_16x16x32_bf16 v[92:95], a[204:207], v[244:247], v[92:95]\n"
    "s_waitcnt lgkmcnt(5)\n"
    "v_mfma_f32_16x16x32_bf16 v[64:67], a[208:211], v[248:251], v[64:67]\n"
    "v_mfma_f32_16x16x32_bf16 v[68:71], a[208:211], v[252:255], v[68:71]\n"
    "s_waitcnt lgkmcnt(4)\n"
    "v_mfma_f32_16x16x32_bf16 v[72:75], a[212:215], v[248:251], v[72:75]\n"
    "v_mfma_f32_16x16x32_bf16 v[76:79], a[212:215], v[252:255], v[76:79]\n"
    "s_waitcnt lgkmcnt(2)\n"
    "v_mfma_f32_16x16x32_bf16 v[80:83], a[216:219], v[248:251], v[80:83]\n"
    "v_mfma_f32_16x16x32_bf16 v[84:87], a[216:219], v[252:255], v[84:87]\n"
    "s_waitcnt lgkmcnt(0)\n"
    "v_mfma_f32_16x16x32_bf16 v[88:91], a[220:223], v[248:251], v[88:91]\n"
    "v_mfma_f32_16x16x32_bf16 v[92:95], a[220:223], v[252:255], v[92:95]\n"
    "s_nop 7\n"
    "v_cvt_pk_bf16_f32 v128, v64, v65\n"
    "v_cvt_pk_bf16_f32 v129, v66, v67\n"
    "v_cvt_pk_bf16_f32 v130, v72, v73\n"
    "v_cvt_pk_bf16_f32 v131, v74, v75\n"
    "buffer_store_dwordx4 v[128:131], v60, s[64:67], s71 offen offset:0 sc1\n"
    "v_cvt_pk_bf16_f32 v132, v80, v81\n"
    "v_cvt_pk_bf16_f32 v133, v82, v83\n"
    "v_cvt_pk_bf16_f32 v134, v88, v89\n"
    "v_cvt_pk_bf16_f32 v135, v90, v91\n"
    "buffer_store_dwordx4 v[132:135], v60, s[64:67], s71 offen offset:512 sc1\n"
    "v_cvt_pk_bf16_f32 v136, v68, v69\n"
    "v_cvt_pk_bf16_f32 v137, v70, v71\n"
    "v_cvt_pk_bf16_f32 v138, v76, v77\n"
    "v_cvt_pk_bf16_f32 v139, v78, v79\n"
    "buffer_store_dwordx4 v[136:139], v61, s[64:67], s71 offen offset:0 sc1\n"
    "v_cvt_pk_bf16_f32 v140, v84, v85\n"
    "v_cvt_pk_bf16_f32 v141, v86, v87\n"
    "v_cvt_pk_bf16_f32 v142, v92, v93\n"
    "v_cvt_pk_bf16_f32 v143, v94, v95\n"
    "buffer_store_dwordx4 v[140:143], v61, s[64:67], s71 offen offset:512 sc1\n"
    "s_waitcnt vmcnt(0)\n"
    "s_branch END_%=\n"
    "RARE_p0_%=:\n"
    "s_mov_b32 s40, 0xffff0000\n"
    "s_getpc_b64 s[30:31]\n"
    "s_branch RARE_ROUTINE_0_%=\n"
    "s_branch RARE_p0_%=_ret\n"
    "RARE_p1_%=:\n"
    "s_mov_b32 s40, 0xffff2000\n"
    "s_getpc_b64 s[30:31]\n"
    "s_branch RARE_ROUTINE_1_%=\n"
    "s_branch RARE_p1_%=_ret\n"
    "RARE_b2_%=:\n"
    "s_mov_b32 s40, 0xffff0000\n"
    "s_getpc_b64 s[30:31]\n"
    "s_branch RARE_ROUTINE_0_%=\n"
    "s_branch RARE_b2_%=_ret\n"
    "RARE_b3_%=:\n"
    "s_mov_b32 s40, 0xffff2000\n"
    "s_getpc_b64 s[30:31]\n"
    "s_branch RARE_ROUTINE_1_%=\n"
    "s_branch RARE_b3_%=_ret\n"
    "RARE_b4_%=:\n"
    "s_mov_b32 s40, 0xffff0000\n"
    "s_getpc_b64 s[30:31]\n"
    "s_branch RARE_ROUTINE_0_%=\n"
    "s_branch RARE_b4_%=_ret\n"
    "RARE_b5_%=:\n"
    "s_mov_b32 s40, 0xffff2000\n"
    "s_getpc_b64 s[30:31]\n"
    "s_branch RARE_ROUTINE_1_%=\n"
    "s_branch RARE_b5_%=_ret\n"
    "RARE_b6_%=:\n"
    "s_mov_b32 s40, 0xffff0000\n"
    "s_getpc_b64 s[30:31]\n"
    "s_branch RARE_ROUTINE_0_%=\n"
    "s_branch RARE_b6_%=_ret\n"
    "RARE_b7_%=:\n"
    "s_mov_b32 s40, 0xffff2000\n"
    "s_getpc_b64 s[30:31]\n"
    "s_branch RARE_ROUTINE_1_%=\n"
    "s_branch RARE_b7_%=_ret\n"
    "RARE_b8_%=:\n"
    "s_mov_b32 s40, 0xffff0000\n"
    "s_getpc_b64 s[30:31]\n"
    "s_branch RARE_ROUTINE_0_%=\n"
    "s_branch RARE_b8_%=_ret\n"
    "RARE_b9_%=:\n"
    "s_mov_b32 s40, 0xffff2000\n"
    "s_getpc_b64 s[30:31]\n"
    "s_branch RARE_ROUTINE_1_%=\n"
    "s_branch RARE_b9_%=_ret\n"
    "RARE_t58_%=:\n"
    "s_mov_b32 s40, 0xffff4000\n"
    "s_getpc_b64 s[30:31]\n"
    "s_branch RARE_ROUTINE_0_%=\n"
    "s_branch RARE_t58_%=_ret\n"
    "RARE_t59_%=:\n"
    "s_mov_b32 s40, 0xffff6000\n"
    "s_getpc_b64 s[30:31]\n"
    "s_branch RARE_ROUTINE_1_%=\n"
    "s_branch RARE_t59_%=_ret\n"
    "RARE_t60_%=:\n"
    "s_mov_b32 s40, 0xffff8000\n"
    "s_getpc_b64 s[30:31]\n"
    "s_branch RARE_ROUTINE_0_%=\n"
    "s_branch RARE_t60_%=_ret\n"
    "RARE_t61_%=:\n"
    "s_mov_b32 s40, 0xffffa000\n"
    "s_getpc_b64 s[30:31]\n"
    "s_branch RARE_ROUTINE_1_%=\n"
    "s_branch RARE_t61_%=_ret\n"
    "RARE_t62_%=:\n"
    "s_mov_b32 s40, 0xffffc000\n"
    "s_getpc_b64 s[30:31]\n"
    "s_branch RARE_ROUTINE_0_%=\n"
    "s_branch RARE_t62_%=_ret\n"
    "RARE_t63_%=:\n"
    "s_mov_b32 s40, 0xffffe000\n"
    "s_getpc_b64 s[30:31]\n"
    "s_branch RARE_ROUTINE_1_%=\n"
    "s_branch RARE_t63_%=_ret\n"
    "RARE_ROUTINE_0_%=:\n"
    "s_nop 15\n"
    "s_add_u32 s40, s40, s20\n"
    "s_sub_u32 s40, s40, s26\n"
    "v_and_b32 v32, 15, v1\n"
    "v_lshlrev_b32 v32, 8, v32\n"
    "v_lshrrev_b32 v33, 4, v1\n"
    "v_lshl_add_u32 v32, v33, 4, v32\n"
    "v_add_u32 v33, 0x1000, v32\n"
    "buffer_load_dwordx4 a[192:195], v32, s[8:11], s40 offen offset:0\n"
    "buffer_load_dwordx4 a[196:199], v32, s[8:11], s40 offen offset:64\n"
    "buffer_load_dwordx4 a[200:203], v32, s[8:11], s40 offen offset:128\n"
    "buffer_load_dwordx4 a[204:207], v32, s[8:11], s40 offen offset:192\n"
    "buffer_load_dwordx4 a[208:211], v33, s[8:11], s40 offen offset:0\n"
    "buffer_load_dwordx4 a[212:215], v33, s[8:11], s40 offen offset:64\n"
    "buffer_load_dwordx4 a[216:219], v33, s[8:11], s40 offen offset:128\n"
    "buffer_load_dwordx4 a[220:223], v33, s[8:11], s40 offen offset:192\n"
    "s_waitcnt vmcnt(0)\n"
    "v_mfma_f32_16x16x32_bf16 v[64:67], a[192:195], v[192:195], 0\n"
    "v_mfma_f32_16x16x32_bf16 v[72:75], a[192:195], v[208:211], 0\n"
    "v_mfma_f32_16x16x32_bf16 v[80:83], a[192:195], v[224:227], 0\n"
    "v_mfma_f32_16x16x32_bf16 v[88:91], a[192:195], v[240:243], 0\n"
    "v_mfma_f32_16x16x32_bf16 v[64:67], a[196:199], v[196:199], v[64:67]\n"
    "v_mfma_f32_16x16x32_bf16 v[72:75], a[196:199], v[212:215], v[72:75]\n"
    "v_mfma_f32_16x16x32_bf16 v[80:83], a[196:199], v[228:231], v[80:83]\n"
    "v_mfma_f32_16x16x32_bf16 v[88:91], a[196:199], v[244:247], v[88:91]\n"
    "v_mfma_f32_16x16x32_bf16 v[64:67], a[200:203], v[200:203], v[64:67]\n"
    "v_mfma_f32_16x16x32_bf16 v[72:75], a[200:203], v[216:219], v[72:75]\n"
    "v_mfma_f32_16x16x32_bf16 v[80:83], a[200:203], v[232:235], v[80:83]\n"
    "v_mfma_f32_16x16x32_bf16 v[88:91], a[200:203], v[248:251], v[88:91]\n"
    "v_mfma_f32_16x16x32_bf16 v[64:67], a[204:207], v[204:207], v[64:67]\n"
    "v_mfma_f32_16x16x32_bf16 v[72:75], a[204:207], v[220:223], v[72:75]\n"
    "v_mfma_f32_16x16x32_bf16 v[80:83], a[204:207], v[236:239], v[80:83]\n"
    "v_mfma_f32_16x16x32_bf16 v[88:91], a[204:207], v[252:255], v[88:91]\n"
    "v_mfma_f32_16x16x32_bf16 v[68:71], a[208:211], v[192:195], 0\n"
    "v_mfma_f32_16x16x32_bf16 v[76:79], a[208:211], v[208:211], 0\n"
    "v_mfma_f32_16x16x32_bf16 v[84:87], a[208:211], v[224:227], 0\n"
    "v_mfma_f32_16x16x32_bf16 v[92:95], a[208:211], v[240:243], 0\n"
    "v_mfma_f32_16x16x32_bf16 v[68:71], a[212:215], v[196:199], v[68:71]\n"
    "v_mfma_f32_16x16x32_bf16 v[76:79], a[212:215], v[212:215], v[76:79]\n"
    "v_mfma_f32_16x16x32_bf16 v[84:87], a[212:215], v[228:231], v[84:87]\n"
    "v_mfma_f32_16x16x32_bf16 v[92:95], a[212:215], v[244:247], v[92:95]\n"
    "v_mfma_f32_16x16x32_bf16 v[68:71], a[216:219], v[200:203], v[68:71]\n"
    "v_mfma_f32_16x16x32_bf16 v[76:79], a[216:219], v[216:219], v[76:79]\n"
    "v_mfma_f32_16x16x32_bf16 v[84:87], a[216:219], v[232:235], v[84:87]\n"
    "v_mfma_f32_16x16x32_bf16 v[92:95], a[216:219], v[248:251], v[92:95]\n"
    "v_mfma_f32_16x16x32_bf16 v[68:71], a[220:223], v[204:207], v[68:71]\n"
    "v_mfma_f32_16x16x32_bf16 v[76:79], a[220:223], v[220:223], v[76:79]\n"
    "v_mfma_f32_16x16x32_bf16 v[84:87], a[220:223], v[236:239], v[84:87]\n"
    "v_mfma_f32_16x16x32_bf16 v[92:95], a[220:223], v[252:255], v[92:95]\n"
    "s_nop 15\n"
    "v_cvt_pk_bf16_f32 v64, 0, v64\n"
    "v_cvt_pk_bf16_f32 v65, 0, v65\n"
    "v_cvt_pk_bf16_f32 v66, 0, v66\n"
    "v_cvt_pk_bf16_f32 v67, 0, v67\n"
    "v_cvt_pk_bf16_f32 v68, 0, v68\n"
    "v_cvt_pk_bf16_f32 v69, 0, v69\n"
    "v_cvt_pk_bf16_f32 v70, 0, v70\n"
    "v_cvt_pk_bf16_f32 v71, 0, v71\n"
    "v_cvt_pk_bf16_f32 v72, 0, v72\n"
    "v_cvt_pk_bf16_f32 v73, 0, v73\n"
    "v_cvt_pk_bf16_f32 v74, 0, v74\n"
    "v_cvt_pk_bf16_f32 v75, 0, v75\n"
    "v_cvt_pk_bf16_f32 v76, 0, v76\n"
    "v_cvt_pk_bf16_f32 v77, 0, v77\n"
    "v_cvt_pk_bf16_f32 v78, 0, v78\n"
    "v_cvt_pk_bf16_f32 v79, 0, v79\n"
    "v_cvt_pk_bf16_f32 v80, 0, v80\n"
    "v_cvt_pk_bf16_f32 v81, 0, v81\n"
    "v_cvt_pk_bf16_f32 v82, 0, v82\n"
    "v_cvt_pk_bf16_f32 v83, 0, v83\n"
    "v_cvt_pk_bf16_f32 v84, 0, v84\n"
    "v_cvt_pk_bf16_f32 v85, 0, v85\n"
    "v_cvt_pk_bf16_f32 v86, 0, v86\n"
    "v_cvt_pk_bf16_f32 v87, 0, v87\n"
    "v_cvt_pk_bf16_f32 v88, 0, v88\n"
    "v_cvt_pk_bf16_f32 v89, 0, v89\n"
    "v_cvt_pk_bf16_f32 v90, 0, v90\n"
    "v_cvt_pk_bf16_f32 v91, 0, v91\n"
    "v_cvt_pk_bf16_f32 v92, 0, v92\n"
    "v_cvt_pk_bf16_f32 v93, 0, v93\n"
    "v_cvt_pk_bf16_f32 v94, 0, v94\n"
    "v_cvt_pk_bf16_f32 v95, 0, v95\n"
    "v_max3_f32 v34, v64, v65, v66\n"
    "v_max3_f32 v34, v34, v67, v68\n"
    "v_max3_f32 v34, v34, v69, v70\n"
    "v_max_f32 v34, v34, v71\n"
    "v_mov_b32 v35, v34\n"
    "s_nop 1\n"
    "v_permlane16_swap_b32 v34, v35\n"
    "s_nop 0\n"
    "v_max_f32 v34, v34, v35\n"
    "v_mov_b32 v35, v34\n"
    "s_nop 1\n"
    "v_permlane32_swap_b32 v34, v35\n"
    "s_nop 0\n"
    "v_max_f32 v34, v34, v35\n"
    "v_mul_f32 v34, s57, v34\n"
    "v_add_f32 v34, s41, v34\n"
    "v_max_f32 v34, v34, v28\n"
    "v_sub_f32 v36, v28, v34\n"
    "v_mov_b32 v28, v34\n"
    "v_exp_f32 v37, v36\n"
    "v_sub_f32 v160, 0, v34\n"
    "v_sub_f32 v161, 0, v34\n"
    "v_fma_f32 v64, v64, s57, v160\n"
    "v_fma_f32 v65, v65, s57, v160\n"
    "v_fma_f32 v66, v66, s57, v160\n"
    "v_fma_f32 v67, v67, s57, v160\n"
    "v_fma_f32 v68, v68, s57, v160\n"
    "v_fma_f32 v69, v69, s57, v160\n"
    "v_fma_f32 v70, v70, s57, v160\n"
    "v_fma_f32 v71, v71, s57, v160\n"
    "v_exp_f32 v64, v64\n"
    "v_exp_f32 v65, v65\n"
    "v_exp_f32 v66, v66\n"
    "v_exp_f32 v67, v67\n"
    "v_exp_f32 v68, v68\n"
    "v_exp_f32 v69, v69\n"
    "v_exp_f32 v70, v70\n"
    "v_exp_f32 v71, v71\n"
    "s_nop 0\n"
    "v_cvt_pk_bf16_f32 v128, v64, v65\n"
    "v_cvt_pk_bf16_f32 v129, v66, v67\n"
    "v_cvt_pk_bf16_f32 v130, v68, v69\n"
    "v_cvt_pk_bf16_f32 v131, v70, v71\n"
    "v_mul_f32 v40, v40, v37\n"
    "v_mul_f32 v41, v41, v37\n"
    "v_mul_f32 v42, v42, v37\n"
    "v_mul_f32 v43, v43, v37\n"
    "s_nop 1\n"
    "v_mfma_f32_16x16x32_bf16 v[176:179], a[224:227], v[128:131], v[40:43]\n"
    "v_accvgpr_read_b32 v35, a0\n"
    "v_mul_f32 v35, v35, v37\n"
    "v_accvgpr_write_b32 a0, v35\n"
    "v_accvgpr_read_b32 v35, a1\n"
    "v_mul_f32 v35, v35, v37\n"
    "v_accvgpr_write_b32 a1, v35\n"
    "v_accvgpr_read_b32 v35, a2\n"
    "v_mul_f32 v35, v35, v37\n"
    "v_accvgpr_write_b32 a2, v35\n"
    "v_accvgpr_read_b32 v35, a3\n"
    "v_mul_f32 v35, v35, v37\n"
    "v_accvgpr_write_b32 a3, v35\n"
    "v_accvgpr_read_b32 v35, a16\n"
    "v_mul_f32 v35, v35, v37\n"
    "v_accvgpr_write_b32 a16, v35\n"
    "v_accvgpr_read_b32 v35, a17\n"
    "v_mul_f32 v35, v35, v37\n"
    "v_accvgpr_write_b32 a17, v35\n"
    "v_accvgpr_read_b32 v35, a18\n"
    "v_mul_f32 v35, v35, v37\n"
    "v_accvgpr_write_b32 a18, v35\n"
    "v_accvgpr_read_b32 v35, a19\n"
    "v_mul_f32 v35, v35, v37\n"
    "v_accvgpr_write_b32 a19, v35\n"
    "v_accvgpr_read_b32 v35, a32\n"
    "v_mul_f32 v35, v35, v37\n"
    "v_accvgpr_write_b32 a32, v35\n"
    "v_accvgpr_read_b32 v35, a33\n"
    "v_mul_f32 v35, v35, v37\n"
    "v_accvgpr_write_b32 a33, v35\n"
    "v_accvgpr_read_b32 v35, a34\n"
    "v_mul_f32 v35, v35, v37\n"
    "v_accvgpr_write_b32 a34, v35\n"
    "v_accvgpr_read_b32 v35, a35\n"
    "v_mul_f32 v35, v35, v37\n"
    "v_accvgpr_write_b32 a35, v35\n"
    "v_accvgpr_read_b32 v35, a48\n"
    "v_mul_f32 v35, v35, v37\n"
    "v_accvgpr_write_b32 a48, v35\n"
    "v_accvgpr_read_b32 v35, a49\n"
    "v_mul_f32 v35, v35, v37\n"
    "v_accvgpr_write_b32 a49, v35\n"
    "v_accvgpr_read_b32 v35, a50\n"
    "v_mul_f32 v35, v35, v37\n"
    "v_accvgpr_write_b32 a50, v35\n"
    "v_accvgpr_read_b32 v35, a51\n"
    "v_mul_f32 v35, v35, v37\n"
    "v_accvgpr_write_b32 a51, v35\n"
    "v_accvgpr_read_b32 v35, a64\n"
    "v_mul_f32 v35, v35, v37\n"
    "v_accvgpr_write_b32 a64, v35\n"
    "v_accvgpr_read_b32 v35, a65\n"
    "v_mul_f32 v35, v35, v37\n"
    "v_accvgpr_write_b32 a65, v35\n"
    "v_accvgpr_read_b32 v35, a66\n"
    "v_mul_f32 v35, v35, v37\n"
    "v_accvgpr_write_b32 a66, v35\n"
    "v_accvgpr_read_b32 v35, a67\n"
    "v_mul_f32 v35, v35, v37\n"
    "v_accvgpr_write_b32 a67, v35\n"
    "v_accvgpr_read_b32 v35, a80\n"
    "v_mul_f32 v35, v35, v37\n"
    "v_accvgpr_write_b32 a80, v35\n"
    "v_accvgpr_read_b32 v35, a81\n"
    "v_mul_f32 v35, v35, v37\n"
    "v_accvgpr_write_b32 a81, v35\n"
    "v_accvgpr_read_b32 v35, a82\n"
    "v_mul_f32 v35, v35, v37\n"
    "v_accvgpr_write_b32 a82, v35\n"
    "v_accvgpr_read_b32 v35, a83\n"
    "v_mul_f32 v35, v35, v37\n"
    "v_accvgpr_write_b32 a83, v35\n"
    "v_accvgpr_read_b32 v35, a96\n"
    "v_mul_f32 v35, v35, v37\n"
    "v_accvgpr_write_b32 a96, v35\n"
    "v_accvgpr_read_b32 v35, a97\n"
    "v_mul_f32 v35, v35, v37\n"
    "v_accvgpr_write_b32 a97, v35\n"
    "v_accvgpr_read_b32 v35, a98\n"
    "v_mul_f32 v35, v35, v37\n"
    "v_accvgpr_write_b32 a98, v35\n"
    "v_accvgpr_read_b32 v35, a99\n"
    "v_mul_f32 v35, v35, v37\n"
    "v_accvgpr_write_b32 a99, v35\n"
    "v_accvgpr_read_b32 v35, a112\n"
    "v_mul_f32 v35, v35, v37\n"
    "v_accvgpr_write_b32 a112, v35\n"
    "v_accvgpr_read_b32 v35, a113\n"
    "v_mul_f32 v35, v35, v37\n"
    "v_accvgpr_write_b32 a113, v35\n"
    "v_accvgpr_read_b32 v35, a114\n"
    "v_mul_f32 v35, v35, v37\n"
    "v_accvgpr_write_b32 a114, v35\n"
    "v_accvgpr_read_b32 v35, a115\n"
    "v_mul_f32 v35, v35, v37\n"
    "v_accvgpr_write_b32 a115, v35\n"
    "v_max3_f32 v34, v72, v73, v74\n"
    "v_max3_f32 v34, v34, v75, v76\n"
    "v_max3_f32 v34, v34, v77, v78\n"
    "v_max_f32 v34, v34, v79\n"
    "v_mov_b32 v35, v34\n"
    "s_nop 1\n"
    "v_permlane16_swap_b32 v34, v35\n"
    "s_nop 0\n"
    "v_max_f32 v34, v34, v35\n"
    "v_mov_b32 v35, v34\n"
    "s_nop 1\n"
    "v_permlane32_swap_b32 v34, v35\n"
    "s_nop 0\n"
    "v_max_f32 v34, v34, v35\n"
    "v_mul_f32 v34, s57, v34\n"
    "v_add_f32 v34, s41, v34\n"
    "v_max_f32 v34, v34, v29\n"
    "v_sub_f32 v36, v29, v34\n"
    "v_mov_b32 v29, v34\n"
    "v_exp_f32 v37, v36\n"
    "v_sub_f32 v162, 0, v34\n"
    "v_sub_f32 v163, 0, v34\n"
    "v_fma_f32 v72, v72, s57, v162\n"
    "v_fma_f32 v73, v73, s57, v162\n"
    "v_fma_f32 v74, v74, s57, v162\n"
    "v_fma_f32 v75, v75, s57, v162\n"
    "v_fma_f32 v76, v76, s57, v162\n"
    "v_fma_f32 v77, v77, s57, v162\n"
    "v_fma_f32 v78, v78, s57, v162\n"
    "v_fma_f32 v79, v79, s57, v162\n"
    "v_exp_f32 v72, v72\n"
    "v_exp_f32 v73, v73\n"
    "v_exp_f32 v74, v74\n"
    "v_exp_f32 v75, v75\n"
    "v_exp_f32 v76, v76\n"
    "v_exp_f32 v77, v77\n"
    "v_exp_f32 v78, v78\n"
    "v_exp_f32 v79, v79\n"
    "s_nop 0\n"
    "v_cvt_pk_bf16_f32 v132, v72, v73\n"
    "v_cvt_pk_bf16_f32 v133, v74, v75\n"
    "v_cvt_pk_bf16_f32 v134, v76, v77\n"
    "v_cvt_pk_bf16_f32 v135, v78, v79\n"
    "v_mul_f32 v44, v44, v37\n"
    "v_mul_f32 v45, v45, v37\n"
    "v_mul_f32 v46, v46, v37\n"
    "v_mul_f32 v47, v47, v37\n"
    "s_nop 1\n"
    "v_mfma_f32_16x16x32_bf16 v[180:183], a[224:227], v[132:135], v[44:47]\n"
    "v_accvgpr_read_b32 v35, a4\n"
    "v_mul_f32 v35, v35, v37\n"
    "v_accvgpr_write_b32 a4, v35\n"
    "v_accvgpr_read_b32 v35, a5\n"
    "v_mul_f32 v35, v35, v37\n"
    "v_accvgpr_write_b32 a5, v35\n"
    "v_accvgpr_read_b32 v35, a6\n"
    "v_mul_f32 v35, v35, v37\n"
    "v_accvgpr_write_b32 a6, v35\n"
    "v_accvgpr_read_b32 v35, a7\n"
    "v_mul_f32 v35, v35, v37\n"
    "v_accvgpr_write_b32 a7, v35\n"
    "v_accvgpr_read_b32 v35, a20\n"
    "v_mul_f32 v35, v35, v37\n"
    "v_accvgpr_write_b32 a20, v35\n"
    "v_accvgpr_read_b32 v35, a21\n"
    "v_mul_f32 v35, v35, v37\n"
    "v_accvgpr_write_b32 a21, v35\n"
    "v_accvgpr_read_b32 v35, a22\n"
    "v_mul_f32 v35, v35, v37\n"
    "v_accvgpr_write_b32 a22, v35\n"
    "v_accvgpr_read_b32 v35, a23\n"
    "v_mul_f32 v35, v35, v37\n"
    "v_accvgpr_write_b32 a23, v35\n"
    "v_accvgpr_read_b32 v35, a36\n"
    "v_mul_f32 v35, v35, v37\n"
    "v_accvgpr_write_b32 a36, v35\n"
    "v_accvgpr_read_b32 v35, a37\n"
    "v_mul_f32 v35, v35, v37\n"
    "v_accvgpr_write_b32 a37, v35\n"
    "v_accvgpr_read_b32 v35, a38\n"
    "v_mul_f32 v35, v35, v37\n"
    "v_accvgpr_write_b32 a38, v35\n"
    "v_accvgpr_read_b32 v35, a39\n"
    "v_mul_f32 v35, v35, v37\n"
    "v_accvgpr_write_b32 a39, v35\n"
    "v_accvgpr_read_b32 v35, a52\n"
    "v_mul_f32 v35, v35, v37\n"
    "v_accvgpr_write_b32 a52, v35\n"
    "v_accvgpr_read_b32 v35, a53\n"
    "v_mul_f32 v35, v35, v37\n"
    "v_accvgpr_write_b32 a53, v35\n"
    "v_accvgpr_read_b32 v35, a54\n"
    "v_mul_f32 v35, v35, v37\n"
    "v_accvgpr_write_b32 a54, v35\n"
    "v_accvgpr_read_b32 v35, a55\n"
    "v_mul_f32 v35, v35, v37\n"
    "v_accvgpr_write_b32 a55, v35\n"
    "v_accvgpr_read_b32 v35, a68\n"
    "v_mul_f32 v35, v35, v37\n"
    "v_accvgpr_write_b32 a68, v35\n"
    "v_accvgpr_read_b32 v35, a69\n"
    "v_mul_f32 v35, v35, v37\n"
    "v_accvgpr_write_b32 a69, v35\n"
    "v_accvgpr_read_b32 v35, a70\n"
    "v_mul_f32 v35, v35, v37\n"
    "v_accvgpr_write_b32 a70, v35\n"
    "v_accvgpr_read_b32 v35, a71\n"
    "v_mul_f32 v35, v35, v37\n"
    "v_accvgpr_write_b32 a71, v35\n"
    "v_accvgpr_read_b32 v35, a84\n"
    "v_mul_f32 v35, v35, v37\n"
    "v_accvgpr_write_b32 a84, v35\n"
    "v_accvgpr_read_b32 v35, a85\n"
    "v_mul_f32 v35, v35, v37\n"
    "v_accvgpr_write_b32 a85, v35\n"
    "v_accvgpr_read_b32 v35, a86\n"
    "v_mul_f32 v35, v35, v37\n"
    "v_accvgpr_write_b32 a86, v35\n"
    "v_accvgpr_read_b32 v35, a87\n"
    "v_mul_f32 v35, v35, v37\n"
    "v_accvgpr_write_b32 a87, v35\n"
    "v_accvgpr_read_b32 v35, a100\n"
    "v_mul_f32 v35, v35, v37\n"
    "v_accvgpr_write_b32 a100, v35\n"
    "v_accvgpr_read_b32 v35, a101\n"
    "v_mul_f32 v35, v35, v37\n"
    "v_accvgpr_write_b32 a101, v35\n"
    "v_accvgpr_read_b32 v35, a102\n"
    "v_mul_f32 v35, v35, v37\n"
    "v_accvgpr_write_b32 a102, v35\n"
    "v_accvgpr_read_b32 v35, a103\n"
    "v_mul_f32 v35, v35, v37\n"
    "v_accvgpr_write_b32 a103, v35\n"
    "v_accvgpr_read_b32 v35, a116\n"
    "v_mul_f32 v35, v35, v37\n"
    "v_accvgpr_write_b32 a116, v35\n"
    "v_accvgpr_read_b32 v35, a117\n"
    "v_mul_f32 v35, v35, v37\n"
    "v_accvgpr_write_b32 a117, v35\n"
    "v_accvgpr_read_b32 v35, a118\n"
    "v_mul_f32 v35, v35, v37\n"
    "v_accvgpr_write_b32 a118, v35\n"
    "v_accvgpr_read_b32 v35, a119\n"
    "v_mul_f32 v35, v35, v37\n"
    "v_accvgpr_write_b32 a119, v35\n"
    "v_max3_f32 v34, v80, v81, v82\n"
    "v_max3_f32 v34, v34, v83, v84\n"
    "v_max3_f32 v34, v34, v85, v86\n"
    "v_max_f32 v34, v34, v87\n"
    "v_mov_b32 v35, v34\n"
    "s_nop 1\n"
    "v_permlane16_swap_b32 v34, v35\n"
    "s_nop 0\n"
    "v_max_f32 v34, v34, v35\n"
    "v_mov_b32 v35, v34\n"
    "s_nop 1\n"
    "v_permlane32_swap_b32 v34, v35\n"
    "s_nop 0\n"
    "v_max_f32 v34, v34, v35\n"
    "v_mul_f32 v34, s57, v34\n"
    "v_add_f32 v34, s41, v34\n"
    "v_max_f32 v34, v34, v30\n"
    "v_sub_f32 v36, v30, v34\n"
    "v_mov_b32 v30, v34\n"
    "v_exp_f32 v37, v36\n"
    "v_sub_f32 v164, 0, v34\n"
    "v_sub_f32 v165, 0, v34\n"
    "v_fma_f32 v80, v80, s57, v164\n"
    "v_fma_f32 v81, v81, s57, v164\n"
    "v_fma_f32 v82, v82, s57, v164\n"
    "v_fma_f32 v83, v83, s57, v164\n"
    "v_fma_f32 v84, v84, s57, v164\n"
    "v_fma_f32 v85, v85, s57, v164\n"
    "v_fma_f32 v86, v86, s57, v164\n"
    "v_fma_f32 v87, v87, s57, v164\n"
    "v_exp_f32 v80, v80\n"
    "v_exp_f32 v81, v81\n"
    "v_exp_f32 v82, v82\n"
    "v_exp_f32 v83, v83\n"
    "v_exp_f32 v84, v84\n"
    "v_exp_f32 v85, v85\n"
    "v_exp_f32 v86, v86\n"
    "v_exp_f32 v87, v87\n"
    "s_nop 0\n"
    "v_cvt_pk_bf16_f32 v136, v80, v81\n"
    "v_cvt_pk_bf16_f32 v137, v82, v83\n"
    "v_cvt_pk_bf16_f32 v138, v84, v85\n"
    "v_cvt_pk_bf16_f32 v139, v86, v87\n"
    "v_mul_f32 v48, v48, v37\n"
    "v_mul_f32 v49, v49, v37\n"
    "v_mul_f32 v50, v50, v37\n"
    "v_mul_f32 v51, v51, v37\n"
    "s_nop 1\n"
    "v_mfma_f32_16x16x32_bf16 v[184:187], a[224:227], v[136:139], v[48:51]\n"
    "v_accvgpr_read_b32 v35, a8\n"
    "v_mul_f32 v35, v35, v37\n"
    "v_accvgpr_write_b32 a8, v35\n"
    "v_accvgpr_read_b32 v35, a9\n"
    "v_mul_f32 v35, v35, v37\n"
    "v_accvgpr_write_b32 a9, v35\n"
    "v_accvgpr_read_b32 v35, a10\n"
    "v_mul_f32 v35, v35, v37\n"
    "v_accvgpr_write_b32 a10, v35\n"
    "v_accvgpr_read_b32 v35, a11\n"
    "v_mul_f32 v35, v35, v37\n"
    "v_accvgpr_write_b32 a11, v35\n"
    "v_accvgpr_read_b32 v35, a24\n"
    "v_mul_f32 v35, v35, v37\n"
    "v_accvgpr_write_b32 a24, v35\n"
    "v_accvgpr_read_b32 v35, a25\n"
    "v_mul_f32 v35, v35, v37\n"
    "v_accvgpr_write_b32 a25, v35\n"
    "v_accvgpr_read_b32 v35, a26\n"
    "v_mul_f32 v35, v35, v37\n"
    "v_accvgpr_write_b32 a26, v35\n"
    "v_accvgpr_read_b32 v35, a27\n"
    "v_mul_f32 v35, v35, v37\n"
    "v_accvgpr_write_b32 a27, v35\n"
    "v_accvgpr_read_b32 v35, a40\n"
    "v_mul_f32 v35, v35, v37\n"
    "v_accvgpr_write_b32 a40, v35\n"
    "v_accvgpr_read_b32 v35, a41\n"
    "v_mul_f32 v35, v35, v37\n"
    "v_accvgpr_write_b32 a41, v35\n"
    "v_accvgpr_read_b32 v35, a42\n"
    "v_mul_f32 v35, v35, v37\n"
    "v_accvgpr_write_b32 a42, v35\n"
    "v_accvgpr_read_b32 v35, a43\n"
    "v_mul_f32 v35, v35, v37\n"
    "v_accvgpr_write_b32 a43, v35\n"
    "v_accvgpr_read_b32 v35, a56\n"
    "v_mul_f32 v35, v35, v37\n"
    "v_accvgpr_write_b32 a56, v35\n"
    "v_accvgpr_read_b32 v35, a57\n"
    "v_mul_f32 v35, v35, v37\n"
    "v_accvgpr_write_b32 a57, v35\n"
    "v_accvgpr_read_b32 v35, a58\n"
    "v_mul_f32 v35, v35, v37\n"
    "v_accvgpr_write_b32 a58, v35\n"
    "v_accvgpr_read_b32 v35, a59\n"
    "v_mul_f32 v35, v35, v37\n"
    "v_accvgpr_write_b32 a59, v35\n"
    "v_accvgpr_read_b32 v35, a72\n"
    "v_mul_f32 v35, v35, v37\n"
    "v_accvgpr_write_b32 a72, v35\n"
    "v_accvgpr_read_b32 v35, a73\n"
    "v_mul_f32 v35, v35, v37\n"
    "v_accvgpr_write_b32 a73, v35\n"
    "v_accvgpr_read_b32 v35, a74\n"
    "v_mul_f32 v35, v35, v37\n"
    "v_accvgpr_write_b32 a74, v35\n"
    "v_accvgpr_read_b32 v35, a75\n"
    "v_mul_f32 v35, v35, v37\n"
    "v_accvgpr_write_b32 a75, v35\n"
    "v_accvgpr_read_b32 v35, a88\n"
    "v_mul_f32 v35, v35, v37\n"
    "v_accvgpr_write_b32 a88, v35\n"
    "v_accvgpr_read_b32 v35, a89\n"
    "v_mul_f32 v35, v35, v37\n"
    "v_accvgpr_write_b32 a89, v35\n"
    "v_accvgpr_read_b32 v35, a90\n"
    "v_mul_f32 v35, v35, v37\n"
    "v_accvgpr_write_b32 a90, v35\n"
    "v_accvgpr_read_b32 v35, a91\n"
    "v_mul_f32 v35, v35, v37\n"
    "v_accvgpr_write_b32 a91, v35\n"
    "v_accvgpr_read_b32 v35, a104\n"
    "v_mul_f32 v35, v35, v37\n"
    "v_accvgpr_write_b32 a104, v35\n"
    "v_accvgpr_read_b32 v35, a105\n"
    "v_mul_f32 v35, v35, v37\n"
    "v_accvgpr_write_b32 a105, v35\n"
    "v_accvgpr_read_b32 v35, a106\n"
    "v_mul_f32 v35, v35, v37\n"
    "v_accvgpr_write_b32 a106, v35\n"
    "v_accvgpr_read_b32 v35, a107\n"
    "v_mul_f32 v35, v35, v37\n"
    "v_accvgpr_write_b32 a107, v35\n"
    "v_accvgpr_read_b32 v35, a120\n"
    "v_mul_f32 v35, v35, v37\n"
    "v_accvgpr_write_b32 a120, v35\n"
    "v_accvgpr_read_b32 v35, a121\n"
    "v_mul_f32 v35, v35, v37\n"
    "v_accvgpr_write_b32 a121, v35\n"
    "v_accvgpr_read_b32 v35, a122\n"
    "v_mul_f32 v35, v35, v37\n"
    "v_accvgpr_write_b32 a122, v35\n"
    "v_accvgpr_read_b32 v35, a123\n"
    "v_mul_f32 v35, v35, v37\n"
    "v_accvgpr_write_b32 a123, v35\n"
    "v_max3_f32 v34, v88, v89, v90\n"
    "v_max3_f32 v34, v34, v91, v92\n"
    "v_max3_f32 v34, v34, v93, v94\n"
    "v_max_f32 v34, v34, v95\n"
    "v_mov_b32 v35, v34\n"
    "s_nop 1\n"
    "v_permlane16_swap_b32 v34, v35\n"
    "s_nop 0\n"
    "v_max_f32 v34, v34, v35\n"
    "v_mov_b32 v35, v34\n"
    "s_nop 1\n"
    "v_permlane32_swap_b32 v34, v35\n"
    "s_nop 0\n"
    "v_max_f32 v34, v34, v35\n"
    "v_mul_f32 v34, s57, v34\n"
    "v_add_f32 v34, s41, v34\n"
    "v_max_f32 v34, v34, v31\n"
    "v_sub_f32 v36, v31, v34\n"
    "v_mov_b32 v31, v34\n"
    "v_exp_f32 v37, v36\n"
    "v_sub_f32 v166, 0, v34\n"
    "v_sub_f32 v167, 0, v34\n"
    "v_fma_f32 v88, v88, s57, v166\n"
    "v_fma_f32 v89, v89, s57, v166\n"
    "v_fma_f32 v90, v90, s57, v166\n"
    "v_fma_f32 v91, v91, s57, v166\n"
    "v_fma_f32 v92, v92, s57, v166\n"
    "v_fma_f32 v93, v93, s57, v166\n"
    "v_fma_f32 v94, v94, s57, v166\n"
    "v_fma_f32 v95, v95, s57, v166\n"
    "v_exp_f32 v88, v88\n"
    "v_exp_f32 v89, v89\n"
    "v_exp_f32 v90, v90\n"
    "v_exp_f32 v91, v91\n"
    "v_exp_f32 v92, v92\n"
    "v_exp_f32 v93, v93\n"
    "v_exp_f32 v94, v94\n"
    "v_exp_f32 v95, v95\n"
    "s_nop 0\n"
    "v_cvt_pk_bf16_f32 v140, v88, v89\n"
    "v_cvt_pk_bf16_f32 v141, v90, v91\n"
    "v_cvt_pk_bf16_f32 v142, v92, v93\n"
    "v_cvt_pk_bf16_f32 v143, v94, v95\n"
    "v_mul_f32 v52, v52, v37\n"
    "v_mul_f32 v53, v53, v37\n"
    "v_mul_f32 v54, v54, v37\n"
    "v_mul_f32 v55, v55, v37\n"
    "s_nop 1\n"
    "v_mfma_f32_16x16x32_bf16 v[188:191], a[224:227], v[140:143], v[52:55]\n"
    "v_accvgpr_read_b32 v35, a12\n"
    "v_mul_f32 v35, v35, v37\n"
    "v_accvgpr_write_b32 a12, v35\n"
    "v_accvgpr_read_b32 v35, a13\n"
    "v_mul_f32 v35, v35, v37\n"
    "v_accvgpr_write_b32 a13, v35\n"
    "v_accvgpr_read_b32 v35, a14\n"
    "v_mul_f32 v35, v35, v37\n"
    "v_accvgpr_write_b32 a14, v35\n"
    "v_accvgpr_read_b32 v35, a15\n"
    "v_mul_f32 v35, v35, v37\n"
    "v_accvgpr_write_b32 a15, v35\n"
    "v_accvgpr_read_b32 v35, a28\n"
    "v_mul_f32 v35, v35, v37\n"
    "v_accvgpr_write_b32 a28, v35\n"
    "v_accvgpr_read_b32 v35, a29\n"
    "v_mul_f32 v35, v35, v37\n"
    "v_accvgpr_write_b32 a29, v35\n"
    "v_accvgpr_read_b32 v35, a30\n"
    "v_mul_f32 v35, v35, v37\n"
    "v_accvgpr_write_b32 a30, v35\n"
    "v_accvgpr_read_b32 v35, a31\n"
    "v_mul_f32 v35, v35, v37\n"
    "v_accvgpr_write_b32 a31, v35\n"
    "v_accvgpr_read_b32 v35, a44\n"
    "v_mul_f32 v35, v35, v37\n"
    "v_accvgpr_write_b32 a44, v35\n"
    "v_accvgpr_read_b32 v35, a45\n"
    "v_mul_f32 v35, v35, v37\n"
    "v_accvgpr_write_b32 a45, v35\n"
    "v_accvgpr_read_b32 v35, a46\n"
    "v_mul_f32 v35, v35, v37\n"
    "v_accvgpr_write_b32 a46, v35\n"
    "v_accvgpr_read_b32 v35, a47\n"
    "v_mul_f32 v35, v35, v37\n"
    "v_accvgpr_write_b32 a47, v35\n"
    "v_accvgpr_read_b32 v35, a60\n"
    "v_mul_f32 v35, v35, v37\n"
    "v_accvgpr_write_b32 a60, v35\n"
    "v_accvgpr_read_b32 v35, a61\n"
    "v_mul_f32 v35, v35, v37\n"
    "v_accvgpr_write_b32 a61, v35\n"
    "v_accvgpr_read_b32 v35, a62\n"
    "v_mul_f32 v35, v35, v37\n"
    "v_accvgpr_write_b32 a62, v35\n"
    "v_accvgpr_read_b32 v35, a63\n"
    "v_mul_f32 v35, v35, v37\n"
    "v_accvgpr_write_b32 a63, v35\n"
    "v_accvgpr_read_b32 v35, a76\n"
    "v_mul_f32 v35, v35, v37\n"
    "v_accvgpr_write_b32 a76, v35\n"
    "v_accvgpr_read_b32 v35, a77\n"
    "v_mul_f32 v35, v35, v37\n"
    "v_accvgpr_write_b32 a77, v35\n"
    "v_accvgpr_read_b32 v35, a78\n"
    "v_mul_f32 v35, v35, v37\n"
    "v_accvgpr_write_b32 a78, v35\n"
    "v_accvgpr_read_b32 v35, a79\n"
    "v_mul_f32 v35, v35, v37\n"
    "v_accvgpr_write_b32 a79, v35\n"
    "v_accvgpr_read_b32 v35, a92\n"
    "v_mul_f32 v35, v35, v37\n"
    "v_accvgpr_write_b32 a92, v35\n"
    "v_accvgpr_read_b32 v35, a93\n"
    "v_mul_f32 v35, v35, v37\n"
    "v_accvgpr_write_b32 a93, v35\n"
    "v_accvgpr_read_b32 v35, a94\n"
    "v_mul_f32 v35, v35, v37\n"
    "v_accvgpr_write_b32 a94, v35\n"
    "v_accvgpr_read_b32 v35, a95\n"
    "v_mul_f32 v35, v35, v37\n"
    "v_accvgpr_write_b32 a95, v35\n"
    "v_accvgpr_read_b32 v35, a108\n"
    "v_mul_f32 v35, v35, v37\n"
    "v_accvgpr_write_b32 a108, v35\n"
    "v_accvgpr_read_b32 v35, a109\n"
    "v_mul_f32 v35, v35, v37\n"
    "v_accvgpr_write_b32 a109, v35\n"
    "v_accvgpr_read_b32 v35, a110\n"
    "v_mul_f32 v35, v35, v37\n"
    "v_accvgpr_write_b32 a110, v35\n"
    "v_accvgpr_read_b32 v35, a111\n"
    "v_mul_f32 v35, v35, v37\n"
    "v_accvgpr_write_b32 a111, v35\n"
    "v_accvgpr_read_b32 v35, a124\n"
    "v_mul_f32 v35, v35, v37\n"
    "v_accvgpr_write_b32 a124, v35\n"
    "v_accvgpr_read_b32 v35, a125\n"
    "v_mul_f32 v35, v35, v37\n"
    "v_accvgpr_write_b32 a125, v35\n"
    "v_accvgpr_read_b32 v35, a126\n"
    "v_mul_f32 v35, v35, v37\n"
    "v_accvgpr_write_b32 a126, v35\n"
    "v_accvgpr_read_b32 v35, a127\n"
    "v_mul_f32 v35, v35, v37\n"
    "v_accvgpr_write_b32 a127, v35\n"
    "s_nop 7\n"
    "s_add_u32 s30, s30, 4\n"
    "s_addc_u32 s31, s31, 0\n"
    "s_setpc_b64 s[30:31]\n"
    "RARE_ROUTINE_1_%=:\n"
    "s_nop 15\n"
    "s_add_u32 s40, s40, s20\n"
    "s_sub_u32 s40, s40, s26\n"
    "v_and_b32 v32, 15, v1\n"
    "v_lshlrev_b32 v32, 8, v32\n"
    "v_lshrrev_b32 v33, 4, v1\n"
    "v_lshl_add_u32 v32, v33, 4, v32\n"
    "v_add_u32 v33, 0x1000, v32\n"
    "buffer_load_dwordx4 a[192:195], v32, s[8:11], s40 offen offset:0\n"
    "buffer_load_dwordx4 a[196:199], v32, s[8:11], s40 offen offset:64\n"
    "buffer_load_dwordx4 a[200:203], v32, s[8:11], s40 offen offset:128\n"
    "buffer_load_dwordx4 a[204:207], v32, s[8:11], s40 offen offset:192\n"
    "buffer_load_dwordx4 a[208:211], v33, s[8:11], s40 offen offset:0\n"
    "buffer_load_dwordx4 a[212:215], v33, s[8:11], s40 offen offset:64\n"
    "buffer_load_dwordx4 a[216:219], v33, s[8:11], s40 offen offset:128\n"
    "buffer_load_dwordx4 a[220:223], v33, s[8:11], s40 offen offset:192\n"
    "s_waitcnt vmcnt(0)\n"
    "v_mfma_f32_16x16x32_bf16 v[96:99], a[192:195], v[192:195], 0\n"
    "v_mfma_f32_16x16x32_bf16 v[104:107], a[192:195], v[208:211], 0\n"
    "v_mfma_f32_16x16x32_bf16 v[112:115], a[192:195], v[224:227], 0\n"
    "v_mfma_f32_16x16x32_bf16 v[120:123], a[192:195], v[240:243], 0\n"
    "v_mfma_f32_16x16x32_bf16 v[96:99], a[196:199], v[196:199], v[96:99]\n"
    "v_mfma_f32_16x16x32_bf16 v[104:107], a[196:199], v[212:215], v[104:107]\n"
    "v_mfma_f32_16x16x32_bf16 v[112:115], a[196:199], v[228:231], v[112:115]\n"
    "v_mfma_f32_16x16x32_bf16 v[120:123], a[196:199], v[244:247], v[120:123]\n"
    "v_mfma_f32_16x16x32_bf16 v[96:99], a[200:203], v[200:203], v[96:99]\n"
    "v_mfma_f32_16x16x32_bf16 v[104:107], a[200:203], v[216:219], v[104:107]\n"
    "v_mfma_f32_16x16x32_bf16 v[112:115], a[200:203], v[232:235], v[112:115]\n"
    "v_mfma_f32_16x16x32_bf16 v[120:123], a[200:203], v[248:251], v[120:123]\n"
    "v_mfma_f32_16x16x32_bf16 v[96:99], a[204:207], v[204:207], v[96:99]\n"
    "v_mfma_f32_16x16x32_bf16 v[104:107], a[204:207], v[220:223], v[104:107]\n"
    "v_mfma_f32_16x16x32_bf16 v[112:115], a[204:207], v[236:239], v[112:115]\n"
    "v_mfma_f32_16x16x32_bf16 v[120:123], a[204:207], v[252:255], v[120:123]\n"
    "v_mfma_f32_16x16x32_bf16 v[100:103], a[208:211], v[192:195], 0\n"
    "v_mfma_f32_16x16x32_bf16 v[108:111], a[208:211], v[208:211], 0\n"
    "v_mfma_f32_16x16x32_bf16 v[116:119], a[208:211], v[224:227], 0\n"
    "v_mfma_f32_16x16x32_bf16 v[124:127], a[208:211], v[240:243], 0\n"
    "v_mfma_f32_16x16x32_bf16 v[100:103], a[212:215], v[196:199], v[100:103]\n"
    "v_mfma_f32_16x16x32_bf16 v[108:111], a[212:215], v[212:215], v[108:111]\n"
    "v_mfma_f32_16x16x32_bf16 v[116:119], a[212:215], v[228:231], v[116:119]\n"
    "v_mfma_f32_16x16x32_bf16 v[124:127], a[212:215], v[244:247], v[124:127]\n"
    "v_mfma_f32_16x16x32_bf16 v[100:103], a[216:219], v[200:203], v[100:103]\n"
    "v_mfma_f32_16x16x32_bf16 v[108:111], a[216:219], v[216:219], v[108:111]\n"
    "v_mfma_f32_16x16x32_bf16 v[116:119], a[216:219], v[232:235], v[116:119]\n"
    "v_mfma_f32_16x16x32_bf16 v[124:127], a[216:219], v[248:251], v[124:127]\n"
    "v_mfma_f32_16x16x32_bf16 v[100:103], a[220:223], v[204:207], v[100:103]\n"
    "v_mfma_f32_16x16x32_bf16 v[108:111], a[220:223], v[220:223], v[108:111]\n"
    "v_mfma_f32_16x16x32_bf16 v[116:119], a[220:223], v[236:239], v[116:119]\n"
    "v_mfma_f32_16x16x32_bf16 v[124:127], a[220:223], v[252:255], v[124:127]\n"
    "s_nop 15\n"
    "v_cvt_pk_bf16_f32 v96, 0, v96\n"
    "v_cvt_pk_bf16_f32 v97, 0, v97\n"
    "v_cvt_pk_bf16_f32 v98, 0, v98\n"
    "v_cvt_pk_bf16_f32 v99, 0, v99\n"
    "v_cvt_pk_bf16_f32 v100, 0, v100\n"
    "v_cvt_pk_bf16_f32 v101, 0, v101\n"
    "v_cvt_pk_bf16_f32 v102, 0, v102\n"
    "v_cvt_pk_bf16_f32 v103, 0, v103\n"
    "v_cvt_pk_bf16_f32 v104, 0, v104\n"
    "v_cvt_pk_bf16_f32 v105, 0, v105\n"
    "v_cvt_pk_bf16_f32 v106, 0, v106\n"
    "v_cvt_pk_bf16_f32 v107, 0, v107\n"
    "v_cvt_pk_bf16_f32 v108, 0, v108\n"
    "v_cvt_pk_bf16_f32 v109, 0, v109\n"
    "v_cvt_pk_bf16_f32 v110, 0, v110\n"
    "v_cvt_pk_bf16_f32 v111, 0, v111\n"
    "v_cvt_pk_bf16_f32 v112, 0, v112\n"
    "v_cvt_pk_bf16_f32 v113, 0, v113\n"
    "v_cvt_pk_bf16_f32 v114, 0, v114\n"
    "v_cvt_pk_bf16_f32 v115, 0, v115\n"
    "v_cvt_pk_bf16_f32 v116, 0, v116\n"
    "v_cvt_pk_bf16_f32 v117, 0, v117\n"
    "v_cvt_pk_bf16_f32 v118, 0, v118\n"
    "v_cvt_pk_bf16_f32 v119, 0, v119\n"
    "v_cvt_pk_bf16_f32 v120, 0, v120\n"
    "v_cvt_pk_bf16_f32 v121, 0, v121\n"
    "v_cvt_pk_bf16_f32 v122, 0, v122\n"
    "v_cvt_pk_bf16_f32 v123, 0, v123\n"
    "v_cvt_pk_bf16_f32 v124, 0, v124\n"
    "v_cvt_pk_bf16_f32 v125, 0, v125\n"
    "v_cvt_pk_bf16_f32 v126, 0, v126\n"
    "v_cvt_pk_bf16_f32 v127, 0, v127\n"
    "v_max3_f32 v34, v96, v97, v98\n"
    "v_max3_f32 v34, v34, v99, v100\n"
    "v_max3_f32 v34, v34, v101, v102\n"
    "v_max_f32 v34, v34, v103\n"
    "v_mov_b32 v35, v34\n"
    "s_nop 1\n"
    "v_permlane16_swap_b32 v34, v35\n"
    "s_nop 0\n"
    "v_max_f32 v34, v34, v35\n"
    "v_mov_b32 v35, v34\n"
    "s_nop 1\n"
    "v_permlane32_swap_b32 v34, v35\n"
    "s_nop 0\n"
    "v_max_f32 v34, v34, v35\n"
    "v_mul_f32 v34, s57, v34\n"
    "v_add_f32 v34, s41, v34\n"
    "v_max_f32 v34, v34, v28\n"
    "v_sub_f32 v36, v28, v34\n"
    "v_mov_b32 v28, v34\n"
    "v_exp_f32 v37, v36\n"
    "v_sub_f32 v160, 0, v34\n"
    "v_sub_f32 v161, 0, v34\n"
    "v_fma_f32 v96, v96, s57, v160\n"
    "v_fma_f32 v97, v97, s57, v160\n"
    "v_fma_f32 v98, v98, s57, v160\n"
    "v_fma_f32 v99, v99, s57, v160\n"
    "v_fma_f32 v100, v100, s57, v160\n"
    "v_fma_f32 v101, v101, s57, v160\n"
    "v_fma_f32 v102, v102, s57, v160\n"
    "v_fma_f32 v103, v103, s57, v160\n"
    "v_exp_f32 v96, v96\n"
    "v_exp_f32 v97, v97\n"
    "v_exp_f32 v98, v98\n"
    "v_exp_f32 v99, v99\n"
    "v_exp_f32 v100, v100\n"
    "v_exp_f32 v101, v101\n"
    "v_exp_f32 v102, v102\n"
    "v_exp_f32 v103, v103\n"
    "s_nop 0\n"
    "v_cvt_pk_bf16_f32 v144, v96, v97\n"
    "v_cvt_pk_bf16_f32 v145, v98, v99\n"
    "v_cvt_pk_bf16_f32 v146, v100, v101\n"
    "v_cvt_pk_bf16_f32 v147, v102, v103\n"
    "v_mul_f32 v176, v176, v37\n"
    "v_mul_f32 v177, v177, v37\n"
    "v_mul_f32 v178, v178, v37\n"
    "v_mul_f32 v179, v179, v37\n"
    "s_nop 1\n"
    "v_mfma_f32_16x16x32_bf16 v[40:43], a[224:227], v[144:147], v[176:179]\n"
    "v_accvgpr_read_b32 v35, a0\n"
    "v_mul_f32 v35, v35, v37\n"
    "v_accvgpr_write_b32 a0, v35\n"
    "v_accvgpr_read_b32 v35, a1\n"
    "v_mul_f32 v35, v35, v37\n"
    "v_accvgpr_write_b32 a1, v35\n"
    "v_accvgpr_read_b32 v35, a2\n"
    "v_mul_f32 v35, v35, v37\n"
    "v_accvgpr_write_b32 a2, v35\n"
    "v_accvgpr_read_b32 v35, a3\n"
    "v_mul_f32 v35, v35, v37\n"
    "v_accvgpr_write_b32 a3, v35\n"
    "v_accvgpr_read_b32 v35, a16\n"
    "v_mul_f32 v35, v35, v37\n"
    "v_accvgpr_write_b32 a16, v35\n"
    "v_accvgpr_read_b32 v35, a17\n"
    "v_mul_f32 v35, v35, v37\n"
    "v_accvgpr_write_b32 a17, v35\n"
    "v_accvgpr_read_b32 v35, a18\n"
    "v_mul_f32 v35, v35, v37\n"
    "v_accvgpr_write_b32 a18, v35\n"
    "v_accvgpr_read_b32 v35, a19\n"
    "v_mul_f32 v35, v35, v37\n"
    "v_accvgpr_write_b32 a19, v35\n"
    "v_accvgpr_read_b32 v35, a32\n"
    "v_mul_f32 v35, v35, v37\n"
    "v_accvgpr_write_b32 a32, v35\n"
    "v_accvgpr_read_b32 v35, a33\n"
    "v_mul_f32 v35, v35, v37\n"
    "v_accvgpr_write_b32 a33, v35\n"
    "v_accvgpr_read_b32 v35, a34\n"
    "v_mul_f32 v35, v35, v37\n"
    "v_accvgpr_write_b32 a34, v35\n"
    "v_accvgpr_read_b32 v35, a35\n"
    "v_mul_f32 v35, v35, v37\n"
    "v_accvgpr_write_b32 a35, v35\n"
    "v_accvgpr_read_b32 v35, a48\n"
    "v_mul_f32 v35, v35, v37\n"
    "v_accvgpr_write_b32 a48, v35\n"
    "v_accvgpr_read_b32 v35, a49\n"
    "v_mul_f32 v35, v35, v37\n"
    "v_accvgpr_write_b32 a49, v35\n"
    "v_accvgpr_read_b32 v35, a50\n"
    "v_mul_f32 v35, v35, v37\n"
    "v_accvgpr_write_b32 a50, v35\n"
    "v_accvgpr_read_b32 v35, a51\n"
    "v_mul_f32 v35, v35, v37\n"
    "v_accvgpr_write_b32 a51, v35\n"
    "v_accvgpr_read_b32 v35, a64\n"
    "v_mul_f32 v35, v35, v37\n"
    "v_accvgpr_write_b32 a64, v35\n"
    "v_accvgpr_read_b32 v35, a65\n"
    "v_mul_f32 v35, v35, v37\n"
    "v_accvgpr_write_b32 a65, v35\n"
    "v_accvgpr_read_b32 v35, a66\n"
    "v_mul_f32 v35, v35, v37\n"
    "v_accvgpr_write_b32 a66, v35\n"
    "v_accvgpr_read_b32 v35, a67\n"
    "v_mul_f32 v35, v35, v37\n"
    "v_accvgpr_write_b32 a67, v35\n"
    "v_accvgpr_read_b32 v35, a80\n"
    "v_mul_f32 v35, v35, v37\n"
    "v_accvgpr_write_b32 a80, v35\n"
    "v_accvgpr_read_b32 v35, a81\n"
    "v_mul_f32 v35, v35, v37\n"
    "v_accvgpr_write_b32 a81, v35\n"
    "v_accvgpr_read_b32 v35, a82\n"
    "v_mul_f32 v35, v35, v37\n"
    "v_accvgpr_write_b32 a82, v35\n"
    "v_accvgpr_read_b32 v35, a83\n"
    "v_mul_f32 v35, v35, v37\n"
    "v_accvgpr_write_b32 a83, v35\n"
    "v_accvgpr_read_b32 v35, a96\n"
    "v_mul_f32 v35, v35, v37\n"
    "v_accvgpr_write_b32 a96, v35\n"
    "v_accvgpr_read_b32 v35, a97\n"
    "v_mul_f32 v35, v35, v37\n"
    "v_accvgpr_write_b32 a97, v35\n"
    "v_accvgpr_read_b32 v35, a98\n"
    "v_mul_f32 v35, v35, v37\n"
    "v_accvgpr_write_b32 a98, v35\n"
    "v_accvgpr_read_b32 v35, a99\n"
    "v_mul_f32 v35, v35, v37\n"
    "v_accvgpr_write_b32 a99, v35\n"
    "v_accvgpr_read_b32 v35, a112\n"
    "v_mul_f32 v35, v35, v37\n"
    "v_accvgpr_write_b32 a112, v35\n"
    "v_accvgpr_read_b32 v35, a113\n"
    "v_mul_f32 v35, v35, v37\n"
    "v_accvgpr_write_b32 a113, v35\n"
    "v_accvgpr_read_b32 v35, a114\n"
    "v_mul_f32 v35, v35, v37\n"
    "v_accvgpr_write_b32 a114, v35\n"
    "v_accvgpr_read_b32 v35, a115\n"
    "v_mul_f32 v35, v35, v37\n"
    "v_accvgpr_write_b32 a115, v35\n"
    "v_max3_f32 v34, v104, v105, v106\n"
    "v_max3_f32 v34, v34, v107, v108\n"
    "v_max3_f32 v34, v34, v109, v110\n"
    "v_max_f32 v34, v34, v111\n"
    "v_mov_b32 v35, v34\n"
    "s_nop 1\n"
    "v_permlane16_swap_b32 v34, v35\n"
    "s_nop 0\n"
    "v_max_f32 v34, v34, v35\n"
    "v_mov_b32 v35, v34\n"
    "s_nop 1\n"
    "v_permlane32_swap_b32 v34, v35\n"
    "s_nop 0\n"
    "v_max_f32 v34, v34, v35\n"
    "v_mul_f32 v34, s57, v34\n"
    "v_add_f32 v34, s41, v34\n"
    "v_max_f32 v34, v34, v29\n"
    "v_sub_f32 v36, v29, v34\n"
    "v_mov_b32 v29, v34\n"
    "v_exp_f32 v37, v36\n"
    "v_sub_f32 v162, 0, v34\n"
    "v_sub_f32 v163, 0, v34\n"
    "v_fma_f32 v104, v104, s57, v162\n"
    "v_fma_f32 v105, v105, s57, v162\n"
    "v_fma_f32 v106, v106, s57, v162\n"
    "v_fma_f32 v107, v107, s57, v162\n"
    "v_fma_f32 v108, v108, s57, v162\n"
    "v_fma_f32 v109, v109, s57, v162\n"
    "v_fma_f32 v110, v110, s57, v162\n"
    "v_fma_f32 v111, v111, s57, v162\n"
    "v_exp_f32 v104, v104\n"
    "v_exp_f32 v105, v105\n"
    "v_exp_f32 v106, v106\n"
    "v_exp_f32 v107, v107\n"
    "v_exp_f32 v108, v108\n"
    "v_exp_f32 v109, v109\n"
    "v_exp_f32 v110, v110\n"
    "v_exp_f32 v111, v111\n"
    "s_nop 0\n"
    "v_cvt_pk_bf16_f32 v148, v104, v105\n"
    "v_cvt_pk_bf16_f32 v149, v106, v107\n"
    "v_cvt_pk_bf16_f32 v150, v108, v109\n"
    "v_cvt_pk_bf16_f32 v151, v110, v111\n"
    "v_mul_f32 v180, v180, v37\n"
    "v_mul_f32 v181, v181, v37\n"
    "v_mul_f32 v182, v182, v37\n"
    "v_mul_f32 v183, v183, v37\n"
    "s_nop 1\n"
    "v_mfma_f32_16x16x32_bf16 v[44:47], a[224:227], v[148:151], v[180:183]\n"
    "v_accvgpr_read_b32 v35, a4\n"
    "v_mul_f32 v35, v35, v37\n"
    "v_accvgpr_write_b32 a4, v35\n"
    "v_accvgpr_read_b32 v35, a5\n"
    "v_mul_f32 v35, v35, v37\n"
    "v_accvgpr_write_b32 a5, v35\n"
    "v_accvgpr_read_b32 v35, a6\n"
    "v_mul_f32 v35, v35, v37\n"
    "v_accvgpr_write_b32 a6, v35\n"
    "v_accvgpr_read_b32 v35, a7\n"
    "v_mul_f32 v35, v35, v37\n"
    "v_accvgpr_write_b32 a7, v35\n"
    "v_accvgpr_read_b32 v35, a20\n"
    "v_mul_f32 v35, v35, v37\n"
    "v_accvgpr_write_b32 a20, v35\n"
    "v_accvgpr_read_b32 v35, a21\n"
    "v_mul_f32 v35, v35, v37\n"
    "v_accvgpr_write_b32 a21, v35\n"
    "v_accvgpr_read_b32 v35, a22\n"
    "v_mul_f32 v35, v35, v37\n"
    "v_accvgpr_write_b32 a22, v35\n"
    "v_accvgpr_read_b32 v35, a23\n"
    "v_mul_f32 v35, v35, v37\n"
    "v_accvgpr_write_b32 a23, v35\n"
    "v_accvgpr_read_b32 v35, a36\n"
    "v_mul_f32 v35, v35, v37\n"
    "v_accvgpr_write_b32 a36, v35\n"
    "v_accvgpr_read_b32 v35, a37\n"
    "v_mul_f32 v35, v35, v37\n"
    "v_accvgpr_write_b32 a37, v35\n"
    "v_accvgpr_read_b32 v35, a38\n"
    "v_mul_f32 v35, v35, v37\n"
    "v_accvgpr_write_b32 a38, v35\n"
    "v_accvgpr_read_b32 v35, a39\n"
    "v_mul_f32 v35, v35, v37\n"
    "v_accvgpr_write_b32 a39, v35\n"
    "v_accvgpr_read_b32 v35, a52\n"
    "v_mul_f32 v35, v35, v37\n"
    "v_accvgpr_write_b32 a52, v35\n"
    "v_accvgpr_read_b32 v35, a53\n"
    "v_mul_f32 v35, v35, v37\n"
    "v_accvgpr_write_b32 a53, v35\n"
    "v_accvgpr_read_b32 v35, a54\n"
    "v_mul_f32 v35, v35, v37\n"
    "v_accvgpr_write_b32 a54, v35\n"
    "v_accvgpr_read_b32 v35, a55\n"
    "v_mul_f32 v35, v35, v37\n"
    "v_accvgpr_write_b32 a55, v35\n"
    "v_accvgpr_read_b32 v35, a68\n"
    "v_mul_f32 v35, v35, v37\n"
    "v_accvgpr_write_b32 a68, v35\n"
    "v_accvgpr_read_b32 v35, a69\n"
    "v_mul_f32 v35, v35, v37\n"
    "v_accvgpr_write_b32 a69, v35\n"
    "v_accvgpr_read_b32 v35, a70\n"
    "v_mul_f32 v35, v35, v37\n"
    "v_accvgpr_write_b32 a70, v35\n"
    "v_accvgpr_read_b32 v35, a71\n"
    "v_mul_f32 v35, v35, v37\n"
    "v_accvgpr_write_b32 a71, v35\n"
    "v_accvgpr_read_b32 v35, a84\n"
    "v_mul_f32 v35, v35, v37\n"
    "v_accvgpr_write_b32 a84, v35\n"
    "v_accvgpr_read_b32 v35, a85\n"
    "v_mul_f32 v35, v35, v37\n"
    "v_accvgpr_write_b32 a85, v35\n"
    "v_accvgpr_read_b32 v35, a86\n"
    "v_mul_f32 v35, v35, v37\n"
    "v_accvgpr_write_b32 a86, v35\n"
    "v_accvgpr_read_b32 v35, a87\n"
    "v_mul_f32 v35, v35, v37\n"
    "v_accvgpr_write_b32 a87, v35\n"
    "v_accvgpr_read_b32 v35, a100\n"
    "v_mul_f32 v35, v35, v37\n"
    "v_accvgpr_write_b32 a100, v35\n"
    "v_accvgpr_read_b32 v35, a101\n"
    "v_mul_f32 v35, v35, v37\n"
    "v_accvgpr_write_b32 a101, v35\n"
    "v_accvgpr_read_b32 v35, a102\n"
    "v_mul_f32 v35, v35, v37\n"
    "v_accvgpr_write_b32 a102, v35\n"
    "v_accvgpr_read_b32 v35, a103\n"
    "v_mul_f32 v35, v35, v37\n"
    "v_accvgpr_write_b32 a103, v35\n"
    "v_accvgpr_read_b32 v35, a116\n"
    "v_mul_f32 v35, v35, v37\n"
    "v_accvgpr_write_b32 a116, v35\n"
    "v_accvgpr_read_b32 v35, a117\n"
    "v_mul_f32 v35, v35, v37\n"
    "v_accvgpr_write_b32 a117, v35\n"
    "v_accvgpr_read_b32 v35, a118\n"
    "v_mul_f32 v35, v35, v37\n"
    "v_accvgpr_write_b32 a118, v35\n"
    "v_accvgpr_read_b32 v35, a119\n"
    "v_mul_f32 v35, v35, v37\n"
    "v_accvgpr_write_b32 a119, v35\n"
    "v_max3_f32 v34, v112, v113, v114\n"
    "v_max3_f32 v34, v34, v115, v116\n"
    "v_max3_f32 v34, v34, v117, v118\n"
    "v_max_f32 v34, v34, v119\n"
    "v_mov_b32 v35, v34\n"
    "s_nop 1\n"
    "v_permlane16_swap_b32 v34, v35\n"
    "s_nop 0\n"
    "v_max_f32 v34, v34, v35\n"
    "v_mov_b32 v35, v34\n"
    "s_nop 1\n"
    "v_permlane32_swap_b32 v34, v35\n"
    "s_nop 0\n"
    "v_max_f32 v34, v34, v35\n"
    "v_mul_f32 v34, s57, v34\n"
    "v_add_f32 v34, s41, v34\n"
    "v_max_f32 v34, v34, v30\n"
    "v_sub_f32 v36, v30, v34\n"
    "v_mov_b32 v30, v34\n"
    "v_exp_f32 v37, v36\n"
    "v_sub_f32 v164, 0, v34\n"
    "v_sub_f32 v165, 0, v34\n"
    "v_fma_f32 v112, v112, s57, v164\n"
    "v_fma_f32 v113, v113, s57, v164\n"
    "v_fma_f32 v114, v114, s57, v164\n"
    "v_fma_f32 v115, v115, s57, v164\n"
    "v_fma_f32 v116, v116, s57, v164\n"
    "v_fma_f32 v117, v117, s57, v164\n"
    "v_fma_f32 v118, v118, s57, v164\n"
    "v_fma_f32 v119, v119, s57, v164\n"
    "v_exp_f32 v112, v112\n"
    "v_exp_f32 v113, v113\n"
    "v_exp_f32 v114, v114\n"
    "v_exp_f32 v115, v115\n"
    "v_exp_f32 v116, v116\n"
    "v_exp_f32 v117, v117\n"
    "v_exp_f32 v118, v118\n"
    "v_exp_f32 v119, v119\n"
    "s_nop 0\n"
    "v_cvt_pk_bf16_f32 v152, v112, v113\n"
    "v_cvt_pk_bf16_f32 v153, v114, v115\n"
    "v_cvt_pk_bf16_f32 v154, v116, v117\n"
    "v_cvt_pk_bf16_f32 v155, v118, v119\n"
    "v_mul_f32 v184, v184, v37\n"
    "v_mul_f32 v185, v185, v37\n"
    "v_mul_f32 v186, v186, v37\n"
    "v_mul_f32 v187, v187, v37\n"
    "s_nop 1\n"
    "v_mfma_f32_16x16x32_bf16 v[48:51], a[224:227], v[152:155], v[184:187]\n"
    "v_accvgpr_read_b32 v35, a8\n"
    "v_mul_f32 v35, v35, v37\n"
    "v_accvgpr_write_b32 a8, v35\n"
    "v_accvgpr_read_b32 v35, a9\n"
    "v_mul_f32 v35, v35, v37\n"
    "v_accvgpr_write_b32 a9, v35\n"
    "v_accvgpr_read_b32 v35, a10\n"
    "v_mul_f32 v35, v35, v37\n"
    "v_accvgpr_write_b32 a10, v35\n"
    "v_accvgpr_read_b32 v35, a11\n"
    "v_mul_f32 v35, v35, v37\n"
    "v_accvgpr_write_b32 a11, v35\n"
    "v_accvgpr_read_b32 v35, a24\n"
    "v_mul_f32 v35, v35, v37\n"
    "v_accvgpr_write_b32 a24, v35\n"
    "v_accvgpr_read_b32 v35, a25\n"
    "v_mul_f32 v35, v35, v37\n"
    "v_accvgpr_write_b32 a25, v35\n"
    "v_accvgpr_read_b32 v35, a26\n"
    "v_mul_f32 v35, v35, v37\n"
    "v_accvgpr_write_b32 a26, v35\n"
    "v_accvgpr_read_b32 v35, a27\n"
    "v_mul_f32 v35, v35, v37\n"
    "v_accvgpr_write_b32 a27, v35\n"
    "v_accvgpr_read_b32 v35, a40\n"
    "v_mul_f32 v35, v35, v37\n"
    "v_accvgpr_write_b32 a40, v35\n"
    "v_accvgpr_read_b32 v35, a41\n"
    "v_mul_f32 v35, v35, v37\n"
    "v_accvgpr_write_b32 a41, v35\n"
    "v_accvgpr_read_b32 v35, a42\n"
    "v_mul_f32 v35, v35, v37\n"
    "v_accvgpr_write_b32 a42, v35\n"
    "v_accvgpr_read_b32 v35, a43\n"
    "v_mul_f32 v35, v35, v37\n"
    "v_accvgpr_write_b32 a43, v35\n"
    "v_accvgpr_read_b32 v35, a56\n"
    "v_mul_f32 v35, v35, v37\n"
    "v_accvgpr_write_b32 a56, v35\n"
    "v_accvgpr_read_b32 v35, a57\n"
    "v_mul_f32 v35, v35, v37\n"
    "v_accvgpr_write_b32 a57, v35\n"
    "v_accvgpr_read_b32 v35, a58\n"
    "v_mul_f32 v35, v35, v37\n"
    "v_accvgpr_write_b32 a58, v35\n"
    "v_accvgpr_read_b32 v35, a59\n"
    "v_mul_f32 v35, v35, v37\n"
    "v_accvgpr_write_b32 a59, v35\n"
    "v_accvgpr_read_b32 v35, a72\n"
    "v_mul_f32 v35, v35, v37\n"
    "v_accvgpr_write_b32 a72, v35\n"
    "v_accvgpr_read_b32 v35, a73\n"
    "v_mul_f32 v35, v35, v37\n"
    "v_accvgpr_write_b32 a73, v35\n"
    "v_accvgpr_read_b32 v35, a74\n"
    "v_mul_f32 v35, v35, v37\n"
    "v_accvgpr_write_b32 a74, v35\n"
    "v_accvgpr_read_b32 v35, a75\n"
    "v_mul_f32 v35, v35, v37\n"
    "v_accvgpr_write_b32 a75, v35\n"
    "v_accvgpr_read_b32 v35, a88\n"
    "v_mul_f32 v35, v35, v37\n"
    "v_accvgpr_write_b32 a88, v35\n"
    "v_accvgpr_read_b32 v35, a89\n"
    "v_mul_f32 v35, v35, v37\n"
    "v_accvgpr_write_b32 a89, v35\n"
    "v_accvgpr_read_b32 v35, a90\n"
    "v_mul_f32 v35, v35, v37\n"
    "v_accvgpr_write_b32 a90, v35\n"
    "v_accvgpr_read_b32 v35, a91\n"
    "v_mul_f32 v35, v35, v37\n"
    "v_accvgpr_write_b32 a91, v35\n"
    "v_accvgpr_read_b32 v35, a104\n"
    "v_mul_f32 v35, v35, v37\n"
    "v_accvgpr_write_b32 a104, v35\n"
    "v_accvgpr_read_b32 v35, a105\n"
    "v_mul_f32 v35, v35, v37\n"
    "v_accvgpr_write_b32 a105, v35\n"
    "v_accvgpr_read_b32 v35, a106\n"
    "v_mul_f32 v35, v35, v37\n"
    "v_accvgpr_write_b32 a106, v35\n"
    "v_accvgpr_read_b32 v35, a107\n"
    "v_mul_f32 v35, v35, v37\n"
    "v_accvgpr_write_b32 a107, v35\n"
    "v_accvgpr_read_b32 v35, a120\n"
    "v_mul_f32 v35, v35, v37\n"
    "v_accvgpr_write_b32 a120, v35\n"
    "v_accvgpr_read_b32 v35, a121\n"
    "v_mul_f32 v35, v35, v37\n"
    "v_accvgpr_write_b32 a121, v35\n"
    "v_accvgpr_read_b32 v35, a122\n"
    "v_mul_f32 v35, v35, v37\n"
    "v_accvgpr_write_b32 a122, v35\n"
    "v_accvgpr_read_b32 v35, a123\n"
    "v_mul_f32 v35, v35, v37\n"
    "v_accvgpr_write_b32 a123, v35\n"
    "v_max3_f32 v34, v120, v121, v122\n"
    "v_max3_f32 v34, v34, v123, v124\n"
    "v_max3_f32 v34, v34, v125, v126\n"
    "v_max_f32 v34, v34, v127\n"
    "v_mov_b32 v35, v34\n"
    "s_nop 1\n"
    "v_permlane16_swap_b32 v34, v35\n"
    "s_nop 0\n"
    "v_max_f32 v34, v34, v35\n"
    "v_mov_b32 v35, v34\n"
    "s_nop 1\n"
    "v_permlane32_swap_b32 v34, v35\n"
    "s_nop 0\n"
    "v_max_f32 v34, v34, v35\n"
    "v_mul_f32 v34, s57, v34\n"
    "v_add_f32 v34, s41, v34\n"
    "v_max_f32 v34, v34, v31\n"
    "v_sub_f32 v36, v31, v34\n"
    "v_mov_b32 v31, v34\n"
    "v_exp_f32 v37, v36\n"
    "v_sub_f32 v166, 0, v34\n"
    "v_sub_f32 v167, 0, v34\n"
    "v_fma_f32 v120, v120, s57, v166\n"
    "v_fma_f32 v121, v121, s57, v166\n"
    "v_fma_f32 v122, v122, s57, v166\n"
    "v_fma_f32 v123, v123, s57, v166\n"
    "v_fma_f32 v124, v124, s57, v166\n"
    "v_fma_f32 v125, v125, s57, v166\n"
    "v_fma_f32 v126, v126, s57, v166\n"
    "v_fma_f32 v127, v127, s57, v166\n"
    "v_exp_f32 v120, v120\n"
    "v_exp_f32 v121, v121\n"
    "v_exp_f32 v122, v122\n"
    "v_exp_f32 v123, v123\n"
    "v_exp_f32 v124, v124\n"
    "v_exp_f32 v125, v125\n"
    "v_exp_f32 v126, v126\n"
    "v_exp_f32 v127, v127\n"
    "s_nop 0\n"
    "v_cvt_pk_bf16_f32 v156, v120, v121\n"
    "v_cvt_pk_bf16_f32 v157, v122, v123\n"
    "v_cvt_pk_bf16_f32 v158, v124, v125\n"
    "v_cvt_pk_bf16_f32 v159, v126, v127\n"
    "v_mul_f32 v188, v188, v37\n"
    "v_mul_f32 v189, v189, v37\n"
    "v_mul_f32 v190, v190, v37\n"
    "v_mul_f32 v191, v191, v37\n"
    "s_nop 1\n"
    "v_mfma_f32_16x16x32_bf16 v[52:55], a[224:227], v[156:159], v[188:191]\n"
    "v_accvgpr_read_b32 v35, a12\n"
    "v_mul_f32 v35, v35, v37\n"
    "v_accvgpr_write_b32 a12, v35\n"
    "v_accvgpr_read_b32 v35, a13\n"
    "v_mul_f32 v35, v35, v37\n"
    "v_accvgpr_write_b32 a13, v35\n"
    "v_accvgpr_read_b32 v35, a14\n"
    "v_mul_f32 v35, v35, v37\n"
    "v_accvgpr_write_b32 a14, v35\n"
    "v_accvgpr_read_b32 v35, a15\n"
    "v_mul_f32 v35, v35, v37\n"
    "v_accvgpr_write_b32 a15, v35\n"
    "v_accvgpr_read_b32 v35, a28\n"
    "v_mul_f32 v35, v35, v37\n"
    "v_accvgpr_write_b32 a28, v35\n"
    "v_accvgpr_read_b32 v35, a29\n"
    "v_mul_f32 v35, v35, v37\n"
    "v_accvgpr_write_b32 a29, v35\n"
    "v_accvgpr_read_b32 v35, a30\n"
    "v_mul_f32 v35, v35, v37\n"
    "v_accvgpr_write_b32 a30, v35\n"
    "v_accvgpr_read_b32 v35, a31\n"
    "v_mul_f32 v35, v35, v37\n"
    "v_accvgpr_write_b32 a31, v35\n"
    "v_accvgpr_read_b32 v35, a44\n"
    "v_mul_f32 v35, v35, v37\n"
    "v_accvgpr_write_b32 a44, v35\n"
    "v_accvgpr_read_b32 v35, a45\n"
    "v_mul_f32 v35, v35, v37\n"
    "v_accvgpr_write_b32 a45, v35\n"
    "v_accvgpr_read_b32 v35, a46\n"
    "v_mul_f32 v35, v35, v37\n"
    "v_accvgpr_write_b32 a46, v35\n"
    "v_accvgpr_read_b32 v35, a47\n"
    "v_mul_f32 v35, v35, v37\n"
    "v_accvgpr_write_b32 a47, v35\n"
    "v_accvgpr_read_b32 v35, a60\n"
    "v_mul_f32 v35, v35, v37\n"
    "v_accvgpr_write_b32 a60, v35\n"
    "v_accvgpr_read_b32 v35, a61\n"
    "v_mul_f32 v35, v35, v37\n"
    "v_accvgpr_write_b32 a61, v35\n"
    "v_accvgpr_read_b32 v35, a62\n"
    "v_mul_f32 v35, v35, v37\n"
    "v_accvgpr_write_b32 a62, v35\n"
    "v_accvgpr_read_b32 v35, a63\n"
    "v_mul_f32 v35, v35, v37\n"
    "v_accvgpr_write_b32 a63, v35\n"
    "v_accvgpr_read_b32 v35, a76\n"
    "v_mul_f32 v35, v35, v37\n"
    "v_accvgpr_write_b32 a76, v35\n"
    "v_accvgpr_read_b32 v35, a77\n"
    "v_mul_f32 v35, v35, v37\n"
    "v_accvgpr_write_b32 a77, v35\n"
    "v_accvgpr_read_b32 v35, a78\n"
    "v_mul_f32 v35, v35, v37\n"
    "v_accvgpr_write_b32 a78, v35\n"
    "v_accvgpr_read_b32 v35, a79\n"
    "v_mul_f32 v35, v35, v37\n"
    "v_accvgpr_write_b32 a79, v35\n"
    "v_accvgpr_read_b32 v35, a92\n"
    "v_mul_f32 v35, v35, v37\n"
    "v_accvgpr_write_b32 a92, v35\n"
    "v_accvgpr_read_b32 v35, a93\n"
    "v_mul_f32 v35, v35, v37\n"
    "v_accvgpr_write_b32 a93, v35\n"
    "v_accvgpr_read_b32 v35, a94\n"
    "v_mul_f32 v35, v35, v37\n"
    "v_accvgpr_write_b32 a94, v35\n"
    "v_accvgpr_read_b32 v35, a95\n"
    "v_mul_f32 v35, v35, v37\n"
    "v_accvgpr_write_b32 a95, v35\n"
    "v_accvgpr_read_b32 v35, a108\n"
    "v_mul_f32 v35, v35, v37\n"
    "v_accvgpr_write_b32 a108, v35\n"
    "v_accvgpr_read_b32 v35, a109\n"
    "v_mul_f32 v35, v35, v37\n"
    "v_accvgpr_write_b32 a109, v35\n"
    "v_accvgpr_read_b32 v35, a110\n"
    "v_mul_f32 v35, v35, v37\n"
    "v_accvgpr_write_b32 a110, v35\n"
    "v_accvgpr_read_b32 v35, a111\n"
    "v_mul_f32 v35, v35, v37\n"
    "v_accvgpr_write_b32 a111, v35\n"
    "v_accvgpr_read_b32 v35, a124\n"
    "v_mul_f32 v35, v35, v37\n"
    "v_accvgpr_write_b32 a124, v35\n"
    "v_accvgpr_read_b32 v35, a125\n"
    "v_mul_f32 v35, v35, v37\n"
    "v_accvgpr_write_b32 a125, v35\n"
    "v_accvgpr_read_b32 v35, a126\n"
    "v_mul_f32 v35, v35, v37\n"
    "v_accvgpr_write_b32 a126, v35\n"
    "v_accvgpr_read_b32 v35, a127\n"
    "v_mul_f32 v35, v35, v37\n"
    "v_accvgpr_write_b32 a127, v35\n"
    "s_nop 7\n"
    "s_add_u32 s30, s30, 4\n"
    "s_addc_u32 s31, s31, 0\n"
    "s_setpc_b64 s[30:31]\n"
    "END_%=:\n"
    : "+s"(kp), "+s"(blk), "+v"(tid) :
    : "v1", "v2", "v3", "v4", "v5", "v6", "v7", "v8", "v9", "v10", "v11", "v12", "v13", "v14", "v15", "v16", "v17", "v18", "v19", "v20", "v21", "v22", "v23", "v24", "v25", "v26", "v27", "v28", "v29", "v30", "v31", "v32", "v33", "v34", "v35", "v36", "v37", "v38", "v39", "v40", "v41", "v42", "v43", "v44", "v45", "v46", "v47", "v48", "v49", "v50", "v51", "v52", "v53", "v54", "v55", "v56", "v57", "v58", "v59", "v60", "v61", "v62", "v63", "v64", "v65", "v66", "v67", "v68", "v69", "v70", "v71", "v72", "v73", "v74", "v75", "v76", "v77", "v78", "v79", "v80", "v81", "v82", "v83", "v84", "v85", "v86", "v87", "v88", "v89", "v90", "v91", "v92", "v93", "v94", "v95", "v96", "v97", "v98", "v99", "v100", "v101", "v102", "v103", "v104", "v105", "v106", "v107", "v108", "v109", "v110", "v111", "v112", "v113", "v114", "v115", "v116", "v117", "v118", "v119", "v120", "v121", "v122", "v123", "v124", "v125", "v126", "v127", "v128", "v129", "v130", "v131", "v132", "v133", "v134", "v135", "v136", "v137", "v138", "v139", "v140", "v141", "v142", "v143", "v144", "v145", "v146", "v147", "v148", "v149", "v150", "v151", "v152", "v153", "v154", "v155", "v156", "v157", "v158", "v159", "v160", "v161", "v162", "v163", "v164", "v165", "v166", "v167", "v168", "v169", "v170", "v171", "v172", "v173", "v174", "v175", "v176", "v177", "v178", "v179", "v180", "v181", "v182", "v183", "v184", "v185", "v186", "v187", "v188", "v189", "v190", "v191", "v192", "v193", "v194", "v195", "v196", "v197", "v198", "v199", "v200", "v201", "v202", "v203", "v204", "v205", "v206", "v207", "v208", "v209", "v210", "v211", "v212", "v213", "v214", "v215", "v216", "v217", "v218", "v219", "v220", "v221", "v222", "v223", "v224", "v225", "v226", "v227", "v228", "v229", "v230", "v231", "v232", "v233", "v234", "v235", "v236", "v237", "v238", "v239", "v240", "v241", "v242", "v243", "v244", "v245", "v246", "v247", "v248", "v249", "v250", "v251", "v252", "v253", "v254", "v255", "a0", "a1", "a2", "a3", "a4", "a5", "a6", "a7", "a8", "a9", "a10", "a11", "a12", "a13", "a14", "a15", "a16", "a17", "a18", "a19", "a20", "a21", "a22", "a23", "a24", "a25", "a26", "a27", "a28", "a29", "a30", "a31", "a32", "a33", "a34", "a35", "a36", "a37", "a38", "a39", "a40", "a41", "a42", "a43", "a44", "a45", "a46", "a47", "a48", "a49", "a50", "a51", "a52", "a53", "a54", "a55", "a56", "a57", "a58", "a59", "a60", "a61", "a62", "a63", "a64", "a65", "a66", "a67", "a68", "a69", "a70", "a71", "a72", "a73", "a74", "a75", "a76", "a77", "a78", "a79", "a80", "a81", "a82", "a83", "a84", "a85", "a86", "a87", "a88", "a89", "a90", "a91", "a92", "a93", "a94", "a95", "a96", "a97", "a98", "a99", "a100", "a101", "a102", "a103", "a104", "a105", "a106", "a107", "a108", "a109", "a110", "a111", "a112", "a113", "a114", "a115", "a116", "a117", "a118", "a119", "a120", "a121", "a122", "a123", "a124", "a125", "a126", "a127", "a128", "a129", "a130", "a131", "a132", "a133", "a134", "a135", "a136", "a137", "a138", "a139", "a140", "a141", "a142", "a143", "a144", "a145", "a146", "a147", "a148", "a149", "a150", "a151", "a152", "a153", "a154", "a155", "a156", "a157", "a158", "a159", "a160", "a161", "a162", "a163", "a164", "a165", "a166", "a167", "a168", "a169", "a170", "a171", "a172", "a173", "a174", "a175", "a176", "a177", "a178", "a179", "a180", "a181", "a182", "a183", "a184", "a185", "a186", "a187", "a188", "a189", "a190", "a191", "a192", "a193", "a194", "a195", "a196", "a197", "a198", "a199", "a200", "a201", "a202", "a203", "a204", "a205", "a206", "a207", "a208", "a209", "a210", "a211", "a212", "a213", "a214", "a215", "a216", "a217", "a218", "a219", "a220", "a221", "a222", "a223", "a224", "a225", "a226", "a227", "a228", "a229", "a230", "a231", "a232", "a233", "a234", "a235", "a236", "a237", "a238", "a239", "a240", "a241", "a242", "a243", "a244", "a245", "a246", "a247", "a248", "a249", "a250", "a251", "a252", "a253", "a254", "a255", "s8", "s9", "s10", "s11", "s12", "s13", "s14", "s15", "s16", "s17", "s18", "s19", "s20", "s21", "s22", "s23", "s24", "s25", "s26", "s27", "s28", "s29", "s30", "s31", "s34", "s35", "s36", "s37", "s38", "s39", "s40", "s41", "s42", "s43", "s44", "s45", "s46", "s47", "s48", "s49", "s50", "s51", "s52", "s53", "s54", "s55", "s56", "s57", "s58", "s59", "s60", "s61", "s62", "s63", "s64", "s65", "s66", "s67", "s68", "s69", "s70", "s71", "s72", "s73", "s74", "s75", "s76", "s77", "s78", "s79", "s80", "s81", "s82", "s83", "s84", "s85", "s86", "s87", "s88", "s89", "s90", "s91", "s92", "s93", "s94", "s95", "s96", "s97", "s98", "s99", "vcc", "m0", "scc", "memory");
  karg_t ka = (karg_t)kp;
  tail_body((const GAS u16*)ka[8], (const GAS u16*)ka[9], (const GAS u16*)ka[10], (const GAS u16*)ka[11], (const GAS u16*)ka[12], (GAS char*)ka[13], (GAS u16*)ka[14], lds, (int)tid, (int)blk);
  asm volatile("K_MAIN_CODE_END:");
}

extern "C" void kernel_launch(void* const* d_in, const int* in_sizes, int n_in, void* d_out, int out_size, void* d_ws, size_t ws_size, hipStream_t stream) {
  char* ws = (char*)d_ws;
  if (n_in != 12 || out_size != S * D || ws_size < WS_END) return;
  hipFuncSetAttribute((const void*)k_main, hipFuncAttributeMaxDynamicSharedMemorySize, 160 * 1024);
  k_prep<<<256, 256, 0, stream>>>((const u16*)d_in[10], (const u16*)d_in[11], (const u16*)d_in[0], ws);
  hipLaunchKernelGGL(k_main, dim3(256), dim3(256), 160 * 1024, stream, (const void*)(ws + WS_NFR), (const void*)(ws + WS_PAR), (const void*)d_in[3], (const void*)d_in[1], (const void*)d_in[2],
                     (const void*)d_in[4], (void*)(ws + WS_PART), (void*)nullptr, (const void*)d_in[0], (const void*)d_in[6], (const void*)d_in[7], (const void*)d_in[8],
                     (const void*)d_in[9], (void*)ws, d_out);
}
```
